```python
import torch

def _softmax(x, axis=-1):
    return torch.softmax(x, dim=axis)

def _gelu(x):
    return torch.nn.functional.gelu(x, approximate='tanh')

def _silu(x):
    return torch.nn.functional.silu(x)

def _sigmoid(x):
    return torch.sigmoid(x)

def _layernorm(x, axis=-1, eps=1e-05):
    m = x.mean(dim=axis, keepdim=True)
    v = x.var(dim=axis, keepdim=True, unbiased=False)
    return (x - m) / torch.sqrt(v + eps)

def _scatter_add(tgt, idx, src, axis=0):
    return tgt.clone().index_add_(axis, idx, src)

def _segment_sum(src, seg, n):
    out = torch.zeros((n,) + src.shape[1:], dtype=src.dtype)
    return out.index_add_(0, seg, src)

def _pad(x, pw):
    flat = tuple((v for pair in reversed(pw) for v in pair))
    return torch.nn.functional.pad(x, flat)
kernel_name = 'parallel_sum__ssm_conv_s4d__mixer_gmlp__prompt'

def setup_inputs():

    _w = lambda a: (lambda t: t.to(torch.bfloat16) if t.is_floating_point() else t)(torch.as_tensor(a))

    def _A_setup():
        Bsz, L, D, N = (1, 2048, 448, 256)
        return {'u': _w((lambda *s: torch.randn(*s, dtype=torch.bfloat16) * 0.05)(Bsz, L, D)), 'log_A_real': _w((lambda *s: torch.randn(*s, dtype=torch.bfloat16) * 12.8)(D, N) * 0.5), 'A_imag': _w((lambda *s: torch.randn(*s, dtype=torch.bfloat16) * 0.1)(D, N) * 3.14), 'C_real': _w((lambda *s: torch.randn(*s, dtype=torch.bfloat16) * 3.2)(D, N) / N ** 0.5), 'C_imag': _w((lambda *s: torch.randn(*s, dtype=torch.bfloat16) * 3.2)(D, N) / N ** 0.5), 'log_dt': _w((lambda *s: torch.randn(*s, dtype=torch.bfloat16) * 0.1)(D) * 0.5 - 4.0), 'D_skip': _w((lambda *s: torch.randn(*s, dtype=torch.bfloat16) * 0.8)(D))}
    _A_in = _A_setup()

    def _B_setup():
        B, S, D, F = (1, 2048, 448, 1792)
        Fh = F // 2
        return {'x': _w((lambda *s: torch.randn(*s, dtype=torch.bfloat16) * 0.05)(B, S, D)), 'w_in': _w((lambda *s: torch.randn(*s, dtype=torch.bfloat16) * 0.4)(D, 2 * Fh)), 'w_sp': _w(0.01 * (lambda *s: torch.randn(*s, dtype=torch.bfloat16) * 0.4)(S, S)), 'b_sp': _w(1.0 + 0.01 * (lambda *s: torch.randn(*s, dtype=torch.bfloat16) * 0.05)(S)), 'w_out': _w((lambda *s: torch.randn(*s, dtype=torch.bfloat16) * 0.05)(Fh, D))}
    _B_in = _B_setup()
    return {'A_u': _A_in['u'], 'A_log_A_real': _A_in['log_A_real'], 'A_A_imag': _A_in['A_imag'], 'A_C_real': _A_in['C_real'], 'A_C_imag': _A_in['C_imag'], 'A_log_dt': _A_in['log_dt'], 'A_D_skip': _A_in['D_skip'], 'B_x': _B_in['x'], 'B_w_in': _B_in['w_in'], 'B_w_sp': _B_in['w_sp'], 'B_b_sp': _B_in['b_sp'], 'B_w_out': _B_in['w_out']}

def reference(A_u, A_log_A_real, A_A_imag, A_C_real, A_C_imag, A_log_dt, A_D_skip, B_x, B_w_in, B_w_sp, B_b_sp, B_w_out):

    def _A_ref(u, log_A_real, A_imag, C_real, C_imag, log_dt, D_skip):
        L = u.shape[1]
        dt = torch.exp(log_dt)
        A = -torch.exp(log_A_real) + 1j * A_imag
        C = (C_real + 1j * C_imag) * dt[:, None]
        pos = (lambda x, dt: x.to(dt))(torch.arange(L), log_dt.dtype)
        P = torch.exp((A * dt[:, None])[:, :, None] * pos[None, None, :])
        K = 2.0 * torch.einsum('dn,dnl->dl', C, P).real
        L2 = 2 * L
        u_t = (lambda x, *d: x.permute(*d))(u, 0, 2, 1)
        Kf = (lambda x, *a, **k: torch.fft.rfft(x.float(), *a, **k))(K, L2)
        Uf = (lambda x, *a, **k: torch.fft.rfft(x.float(), *a, **k))(u_t, L2)
        y = (lambda x, *a, **k: torch.fft.irfft(x, *a, **k).to(torch.bfloat16))(Uf * Kf[None, :, :], L2)[:, :, :L]
        y = (lambda x, *d: x.permute(*d))(y, 0, 2, 1)
        return y + u * D_skip[None, None, :]

    def _B_ref(x, w_in, w_sp, b_sp, w_out):
        B, S, D = x.shape
        F = w_in.shape[-1]
        Fh = F // 2
        z = _gelu(torch.einsum('bsd,df->bsf', x, w_in))
        z1 = z[..., :Fh].clone()
        z2 = _layernorm(z[..., Fh:], axis=-1)
        g = torch.einsum('ts,bsf->btf', w_sp, z2) + b_sp[None, :, None]
        return torch.einsum('bsf,fd->bsd', z1 * g, w_out)
    x = A_u
    xn = _layernorm(x, axis=-1)
    return x + _A_ref(xn, A_log_A_real, A_A_imag, A_C_real, A_C_imag, A_log_dt, A_D_skip) + _B_ref(xn, B_w_in, B_w_sp, B_b_sp, B_w_out)
```

```cpp
#include <hip/hip_runtime.h>
#include <hip/hip_bf16.h>
typedef unsigned short u16;
__device__ __forceinline__ float bf2f(u16 v){ return __uint_as_float(((unsigned)v)<<16); }
__device__ __forceinline__ u16 f2bf(float f){ __bf16 b = (__bf16)f; return __builtin_bit_cast(u16,b); }
__device__ __forceinline__ float rbf(float f){ return bf2f(f2bf(f)); }
constexpr int L=2048, D=448, NM=256, F=1792, FH=896;

__global__ void ln_rows(const u16* x, int ldx, u16* y, int ldy, int n){
  int row=blockIdx.x; int t=threadIdx.x; __shared__ float red[256];
  float s=0; for(int i=t;i<n;i+=256) s+=bf2f(x[(size_t)row*ldx+i]);
  red[t]=s; __syncthreads(); for(int o=128;o>0;o>>=1){ if(t<o) red[t]+=red[t+o]; __syncthreads(); }
  float m=red[0]/n; __syncthreads();
  float q=0; for(int i=t;i<n;i+=256){ float d=bf2f(x[(size_t)row*ldx+i])-m; q+=d*d; }
  red[t]=q; __syncthreads(); for(int o=128;o>0;o>>=1){ if(t<o) red[t]+=red[t+o]; __syncthreads(); }
  float v=red[0]/n;
  float mb=rbf(m), sd=rbf(sqrtf(rbf(rbf(v)+1e-5f)));
  for(int i=t;i<n;i+=256) y[(size_t)row*ldy+i]=f2bf(rbf(bf2f(x[(size_t)row*ldx+i])-mb)/sd);
}
__global__ void kgen(const u16* lar,const u16* aim,const u16* cr,const u16* ci,const u16* ldt,float* K){
  int d=blockIdx.y; int l=blockIdx.x*256+threadIdx.x;
  float dt=rbf(expf(bf2f(ldt[d]))); float p=rbf((float)l); float acc=0;
  for(int n=0;n<NM;n++){ float zr=-rbf(expf(bf2f(lar[d*NM+n])))*dt, zi=bf2f(aim[d*NM+n])*dt;
    float e=expf(zr*p); float s,c; sincosf(zi*p,&s,&c);
    acc+= e*(bf2f(cr[d*NM+n])*dt*c - bf2f(ci[d*NM+n])*dt*s); }
  K[d*L+l]=2.f*acc;
}
__global__ void conv(const float* K,const u16* xn,const u16* dsk,u16* ya){
  int d=blockIdx.y; int l=blockIdx.x*256+threadIdx.x; float acc=0;
  for(int j=0;j<=l;j++) acc+=K[d*L+j]*bf2f(xn[(l-j)*D+d]);
  ya[l*D+d]=f2bf(rbf(acc)+rbf(bf2f(xn[l*D+d])*bf2f(dsk[d])));
}
template<int MODE> __global__ void gemm(const u16* A,int lda,const u16* B,int ldb,int K,u16* C,int ldc,const u16* e0,const u16* e1){
  __shared__ float sa[16][17], sb[16][17];
  int tx=threadIdx.x&15, ty=threadIdx.x>>4; int r=blockIdx.y*16+ty, c=blockIdx.x*16+tx; float acc=0;
  for(int k=0;k<K;k+=16){ sa[ty][tx]=bf2f(A[(size_t)r*lda+k+tx]); sb[ty][tx]=bf2f(B[(size_t)(k+ty)*ldb+c]); __syncthreads();
    for(int i=0;i<16;i++) acc+=sa[ty][i]*sb[i][tx]; __syncthreads(); }
  if(MODE==0){ float v=rbf(acc); float u=0.7978845608028654f*(v+0.044715f*v*v*v); C[(size_t)r*ldc+c]=f2bf(0.5f*v*(1.f+tanhf(u))); }
  if(MODE==1){ float g=rbf(rbf(acc)+bf2f(e0[r])); C[(size_t)r*ldc+c]=f2bf(bf2f(e1[(size_t)r*F+c])*g); }
  if(MODE==2){ C[(size_t)r*ldc+c]=f2bf(rbf(bf2f(e0[(size_t)r*D+c])+bf2f(e1[(size_t)r*D+c]))+rbf(acc)); }
}
extern "C" void kernel_launch(void* const* d_in, const int* in_sizes, int n_in, void* d_out, int out_size, void* d_ws, size_t ws_size, hipStream_t stream) {
  const u16* x=(const u16*)d_in[0]; const u16* lar=(const u16*)d_in[1]; const u16* aim=(const u16*)d_in[2]; const u16* cr=(const u16*)d_in[3]; const u16* ci=(const u16*)d_in[4];
  const u16* ldt=(const u16*)d_in[5]; const u16* dsk=(const u16*)d_in[6]; const u16* win=(const u16*)d_in[8]; const u16* wsp=(const u16*)d_in[9]; const u16* bsp=(const u16*)d_in[10]; const u16* wout=(const u16*)d_in[11];
  char* ws=(char*)d_ws; u16* xn=(u16*)ws; float* K=(float*)(ws+(4<<20)); u16* ya=(u16*)(ws+(8<<20)); u16* z=(u16*)(ws+(12<<20)); u16* z2=(u16*)(ws+(20<<20)); u16* h=(u16*)(ws+(24<<20));
  ln_rows<<<L,256,0,stream>>>(x,D,xn,D,D);
  kgen<<<dim3(L/256,D),256,0,stream>>>(lar,aim,cr,ci,ldt,K);
  conv<<<dim3(L/256,D),256,0,stream>>>(K,xn,dsk,ya);
  gemm<0><<<dim3(F/16,L/16),256,0,stream>>>(xn,D,win,F,D,z,F,nullptr,nullptr);
  ln_rows<<<L,256,0,stream>>>(z+FH,F,z2,FH,FH);
  gemm<1><<<dim3(FH/16,L/16),256,0,stream>>>(wsp,L,z2,FH,L,h,FH,bsp,z);
  gemm<2><<<dim3(D/16,L/16),256,0,stream>>>(h,FH,wout,D,FH,(u16*)d_out,D,x,ya);
}
```

```cpp
#include <hip/hip_runtime.h>
#include <stdint.h>

typedef unsigned short u16;
typedef unsigned int u32;
typedef unsigned long long u64;
typedef __attribute__((ext_vector_type(4))) u32 u32x4;
typedef __attribute__((ext_vector_type(2))) u32 u32x2;
typedef __attribute__((ext_vector_type(4))) float f32x4;
typedef __attribute__((ext_vector_type(2))) float f32x2;
typedef __attribute__((ext_vector_type(16))) float f32x16;
typedef __attribute__((ext_vector_type(8))) __bf16 bf16x8;
typedef __attribute__((ext_vector_type(2))) __bf16 bf16x2;
typedef __attribute__((ext_vector_type(8))) _Float16 f16x8;
typedef __attribute__((ext_vector_type(2))) _Float16 f16x2;
typedef __attribute__((ext_vector_type(4))) short s16x4;
#define LDSAS __attribute__((address_space(3)))
#define GAS __attribute__((address_space(1)))
#define DEV __device__ __forceinline__

constexpr int SEQ = 2048, DM = 448, NMODE = 256, FF = 1792, FH = 896;

constexpr size_t WS_Z2F = 0;
constexpr size_t WS_Z1F = 4u << 20;
constexpr size_t WS_HF  = 8u << 20;
constexpr size_t WS_UT  = 12u << 20;
constexpr size_t WS_YT  = 14u << 20;
constexpr size_t WS_RK  = 16u << 20;
constexpr size_t WS_KS  = 18u << 20;
constexpr size_t WS_GR  = 19u << 20;
constexpr size_t WS_EP  = 20u << 20;

DEV float bf2f(u32 v) { return __uint_as_float(v << 16); }
DEV float bflo(u32 v) { return __uint_as_float(v << 16); }
DEV float bfhi(u32 v) { return __uint_as_float(v & 0xffff0000u); }
DEV u32 pk_bf16(float a, float b) { bf16x2 v = {(__bf16)a, (__bf16)b}; return __builtin_bit_cast(u32, v); }
DEV u32 pk_f16(float a, float b) { f16x2 v = {(_Float16)a, (_Float16)b}; return __builtin_bit_cast(u32, v); }
DEV f16x2 as_h2(u32 v) { return __builtin_bit_cast(f16x2, v); }
DEV float rbf(float a) { return __uint_as_float(pk_bf16(a, a) << 16); }
DEV f32x4 mfma_bf(u32x4 a, u32x4 b, f32x4 c) {
  return __builtin_amdgcn_mfma_f32_16x16x32_bf16(__builtin_bit_cast(bf16x8, a), __builtin_bit_cast(bf16x8, b), c, 0, 0, 0);
}
DEV f32x4 mfma_h(u32x4 a, u32x4 b, f32x4 c) {
  return __builtin_amdgcn_mfma_f32_16x16x32_f16(__builtin_bit_cast(f16x8, a), __builtin_bit_cast(f16x8, b), c, 0, 0, 0);
}
DEV f32x16 mfma32_h(u32x4 a, u32x4 b, f32x16 c) {
  return __builtin_amdgcn_mfma_f32_32x32x16_f16(__builtin_bit_cast(f16x8, a), __builtin_bit_cast(f16x8, b), c, 0, 0, 0);
}
DEV u32x2 tr_read(const char* p) {
  s16x4 v = __builtin_amdgcn_ds_read_tr16_b64_v4i16((LDSAS s16x4*)p);
  return __builtin_bit_cast(u32x2, v);
}
DEV u32x4 tr_frag(const char* p0, const char* p1) {
  u32x2 a = tr_read(p0), b = tr_read(p1);
  return (u32x4){a.x, a.y, b.x, b.y};
}
DEV u32 lds_addr(const void* p) { return (u32)(uintptr_t)(LDSAS const char*)p; }
DEV void dma16(const void* sbase, u32 voff, u32 ldst) {
  asm volatile("s_mov_b32 m0, %2\n\ts_nop 0\n\tglobal_load_lds_dwordx4 %0, %1" ::"v"(voff), "s"(sbase), "s"(ldst) : "memory");
}
template <int OFF> DEV void gload16(u32x4& d, const void* sbase, u32 voff) {
  asm volatile("global_load_dwordx4 %0, %1, %2 offset:%3" : "=v"(d) : "v"(voff), "s"(sbase), "i"(OFF) : "memory");
}
template <int N> DEV void wait_vm() { asm volatile("s_waitcnt vmcnt(%0)" ::"i"(N) : "memory"); }
DEV void pin(u32x4& a) { asm volatile("" : "+v"(a)); }
DEV void bar() { asm volatile("s_waitcnt lgkmcnt(0)\n\ts_barrier" ::: "memory"); }
DEV float xsum16(float v) { auto r = __builtin_amdgcn_permlane16_swap(__float_as_uint(v), __float_as_uint(v), false, false); return __uint_as_float(r[0]) + __uint_as_float(r[1]); }
DEV float xsum32(float v) { auto r = __builtin_amdgcn_permlane32_swap(__float_as_uint(v), __float_as_uint(v), false, false); return __uint_as_float(r[0]) + __uint_as_float(r[1]); }
DEV f32x2 ln_scalars(float sum, float sq, float inv_n) {
  float m = sum * inv_n;
  float v = fmaxf(sq * inv_n - m * m, 0.f);
  float sd = rbf(__builtin_amdgcn_sqrtf(rbf(rbf(v) + 1e-5f)));
  return (f32x2){rbf(m), __builtin_amdgcn_rcpf(sd)};
}
DEV f32x2 gelu2(f32x2 x) {
  const float c1 = -2.3022081983f, c2 = -0.1029432396f;
  f32x2 t = x * x;
  t = t * c2 + c1;
  t = t * x;
  f32x2 e = {__builtin_amdgcn_exp2f(t.x), __builtin_amdgcn_exp2f(t.y)};
  e = e + 1.0f;
  f32x2 r = {__builtin_amdgcn_rcpf(e.x), __builtin_amdgcn_rcpf(e.y)};
  return x * r;
}
DEV f32x2 cmul(f32x2 a, f32x2 b) {
  f32x2 t = a.xx * b;
  f32x2 bs = {-b.y, b.x};
  return __builtin_elementwise_fma(a.yy, bs, t);
}
DEV f32x2 csqr(f32x2 a) { return (f32x2){a.x * a.x - a.y * a.y, 2.f * a.x * a.y}; }


template <int M> constexpr int k1_wait() { return 6 * (6 - M) + 2 + 2 * (M > 0 ? M - 1 : 0); }

struct K1G {
  const char* win; const char* xb; u32 xoff[4]; u32 woff; u32 wdst; int m0; int colz2; int colz1;
};
template <int I> DEV void k1_issue_w(const K1G& g) {
  constexpr int hf = I / 14, kk = I % 14;
  int line = (kk >> 1) + g.m0; line = line >= 7 ? line - 7 : line;
  const char* sb = g.win + (size_t)(line * 64 + (kk & 1) * 32) * (FF * 2) + (hf ? g.colz1 : g.colz2);
  dma16(sb, g.woff, g.wdst + (I & 15) * 4096);
}
template <int M> DEV void k1_prologue(const K1G& g, u32x4 (&xr)[7][4]) {
  int line = M + g.m0; line = line >= 7 ? line - 7 : line;
  const char* sb = g.xb + line * 128;
#pragma unroll
  for (int i = 0; i < 4; i++) gload16<0>(xr[M][i], sb, g.xoff[i]);
  k1_issue_w<2 * M>(g);
  k1_issue_w<2 * M + 1>(g);
  if constexpr (M < 6) k1_prologue<M + 1>(g, xr);
}
template <int I> DEV void k1_kstep(const char* const (&wp)[4], u32x4 xf0, u32x4 xf1, f32x4 (&acc)[2][4], f32x4& cs, u32x4 ones) {
  u32x4 wf[4];
#pragma unroll
  for (int p = 0; p < 4; p++) wf[p] = tr_frag(wp[p] + (I & 15) * 4096, wp[p] + (I & 15) * 4096 + 512);
#pragma unroll
  for (int p = 0; p < 4; p++) {
    acc[0][p] = mfma_bf(wf[p], xf0, acc[0][p]);
    acc[1][p] = mfma_bf(wf[p], xf1, acc[1][p]);
  }
  cs = mfma_bf(wf[0], ones, cs);
}
template <int M>
DEV void k1_stage(const K1G& g, u32x4 (&xr)[7][4], char* stw, const char* str0, const char* str1, const char* const (&wp)[4],
                  f32x4 (&acc)[2][4], f32x4& cs, f32x4 (&rsum)[2], f32x4 (&gram)[2], u32x4 ones) {
  wait_vm<k1_wait<M>()>();
#pragma unroll
  for (int i = 0; i < 4; i++) pin(xr[M][i]);
#pragma unroll
  for (int i = 0; i < 4; i++) *(u32x4*)(stw + (i >> 1) * 2048 + (i & 1) * 128) = xr[M][i];
  xr[M][0] = *(const u32x4*)(str0);
  xr[M][1] = *(const u32x4*)(str0 + 2048);
  xr[M][2] = *(const u32x4*)(str1);
  xr[M][3] = *(const u32x4*)(str1 + 2048);
  bar();
  if constexpr (M >= 1) { k1_issue_w<2 * M + 14>(g); k1_issue_w<2 * M + 15>(g); }
  k1_kstep<2 * M>(wp, xr[M][0], xr[M][1], acc, cs, ones);
  k1_kstep<2 * M + 1>(wp, xr[M][2], xr[M][3], acc, cs, ones);
#pragma unroll
  for (int i = 0; i < 4; i++) {
    rsum[i & 1] = mfma_bf(ones, xr[M][i], rsum[i & 1]);
    gram[i & 1] = mfma_bf(xr[M][i], xr[M][i], gram[i & 1]);
  }
  if constexpr (M < 6) k1_stage<M + 1>(g, xr, stw, str0, str1, wp, acc, cs, rsum, gram, ones);
}
template <int KK>
DEV void k1_z1loop(u32x4 (&xr)[7][4], const char* const (&wp)[4], f32x4 (&acc)[2][4], f32x4& cs, u32x4 ones) {
  k1_kstep<14 + KK>(wp, xr[KK >> 1][(KK & 1) * 2], xr[KK >> 1][(KK & 1) * 2 + 1], acc, cs, ones);
  if constexpr (KK < 13) k1_z1loop<KK + 1>(xr, wp, acc, cs, ones);
}
DEV void k1_act(f32x4 (&acc)[2][4], const char* csbuf, int w, int q, const f32x2 (&ln1)[2]) {
#pragma unroll
  for (int p = 0; p < 4; p++) {
    f32x4 c = *(const f32x4*)(csbuf + (p ^ w) * 64 + q * 16);
#pragma unroll
    for (int t = 0; t < 2; t++) {
      f32x2 a = (acc[t][p].xy - c.xy * ln1[t].x) * ln1[t].y;
      f32x2 b = (acc[t][p].zw - c.zw * ln1[t].x) * ln1[t].y;
      a = gelu2(a); b = gelu2(b);
      acc[t][p] = (f32x4){a.x, a.y, b.x, b.y};
    }
  }
}

DEV void k1_gemm(char* smem, int b, const u16* x, const u16* win, char* ws) {
  const int xcd = b & 7, idx = b >> 3;
  const int panel = (xcd >> 1) * 4 + idx / 7, sib = (xcd & 1) * 7 + idx % 7;
  const int w = __builtin_amdgcn_readfirstlane(threadIdx.x >> 6), l = threadIdx.x & 63;
  const int rho = l & 15, q = l >> 4;
  char* const ring = smem;
  char* const stg = smem + 65536 + w * 4096;
  K1G g;
  g.win = (const char*)win + (size_t)(8 * w) * (FF * 2);
  g.xb = (const char*)x + (size_t)(panel * 128 + w * 32) * (DM * 2);
#pragma unroll
  for (int i = 0; i < 4; i++) g.xoff[i] = (u32)((16 * (i >> 1) + 2 * (l >> 3) + (i & 1)) * (DM * 2) + (l & 7) * 16);
  g.woff = (u32)((l >> 3) * (FF * 2) + (((l & 7) ^ ((((l >> 4) & 1) | ((w & 1) << 1)) << 1)) * 16));
  g.wdst = lds_addr(ring) + w * 1024;
  g.m0 = sib >> 1;
  g.colz2 = FH * 2 + sib * 128;
  g.colz1 = sib * 128;
  u32x4 xr[7][4];
  k1_prologue<0>(g, xr);
  k1_issue_w<14>(g);
  k1_issue_w<15>(g);
  const int grp = panel * 4 + w;
  u32 tag;
  asm volatile("s_load_dword %0, %1, 0x0\n\ts_waitcnt lgkmcnt(0)" : "=s"(tag) : "s"(ws + WS_EP + grp * 256) : "memory");
  tag += 1u;

  char* stw = stg + (l >> 3) * 256 + (((l & 7) ^ (l >> 3)) * 16);
  const char* str0 = stg + rho * 128 + (((0 + q) ^ (rho >> 1)) * 16);
  const char* str1 = stg + rho * 128 + (((4 + q) ^ (rho >> 1)) * 16);
  const char* wp[4];
  const int qq = (l >> 2) & 3, pp = l & 3;
  {
    int sg = (qq >> 1) | ((q & 1) << 1);
#pragma unroll
    for (int f = 0; f < 4; f++) wp[f] = ring + (8 * q + qq) * 128 + (((f ^ w) ^ sg) * 32) + 8 * pp;
  }
  const u32x4 ones = {0x3f803f80u, 0x3f803f80u, 0x3f803f80u, 0x3f803f80u};
  f32x4 acc2[2][4] = {}, cs = {}, rsum[2] = {}, gram[2] = {};
  k1_stage<0>(g, xr, stw, str0, str1, wp, acc2, cs, rsum, gram, ones);

  char* const csb2 = smem + 65536;
  char* const csb1 = smem + 65536 + 4096;
  bar();
  if (rho == 0) *(f32x4*)(csb2 + w * 64 + q * 16) = cs;
  f32x2 ln1[2];
#pragma unroll
  for (int t = 0; t < 2; t++) {
    float d = (rho & 2) ? ((rho & 1) ? gram[t].w : gram[t].z) : ((rho & 1) ? gram[t].y : gram[t].x);
    d = ((rho >> 2) == q) ? d : 0.f;
    d = xsum32(xsum16(d));
    ln1[t] = ln_scalars(rsum[t].x, d, 1.f / DM);
  }
  bar();
  k1_act(acc2, csb2, w, q, ln1);
  GAS u64* gr = (GAS u64*)(ws + WS_GR) + (size_t)grp * 14 * 64;
  {
    float st[2][2];
#pragma unroll
    for (int t = 0; t < 2; t++) {
      f32x2 s = {0.f, 0.f}, s2 = {0.f, 0.f};
#pragma unroll
      for (int p = 0; p < 4; p++) {
        s += acc2[t][p].xy; s += acc2[t][p].zw;
        s2 += acc2[t][p].xy * acc2[t][p].xy; s2 += acc2[t][p].zw * acc2[t][p].zw;
      }
      st[t][0] = xsum32(xsum16(s.x + s.y));
      st[t][1] = xsum32(xsum16(s2.x + s2.y));
    }
    float v = (q & 2) ? ((q & 1) ? st[1][1] : st[0][1]) : ((q & 1) ? st[1][0] : st[0][0]);
    __hip_atomic_store(gr + sib * 64 + l, ((u64)tag << 32) | __float_as_uint(v), __ATOMIC_RELAXED, __HIP_MEMORY_SCOPE_AGENT);
  }
  wait_vm<1>();
  bar();
  {
    char* ub = stg + 1024;
#pragma unroll
    for (int t = 0; t < 2; t++) {
      const u32x4 va = xr[0][t], vb = xr[0][2 + t];
      const u32 msk = (sib & 1) ? 0xffffffffu : 0u;
      u32x4 v = (va & ~msk) | (vb & msk);
      u32x4 o;
#pragma unroll
      for (int j = 0; j < 4; j++) {
        float a = rbf(rbf(bflo(v[j]) - ln1[t].x) * ln1[t].y), bb = rbf(rbf(bfhi(v[j]) - ln1[t].x) * ln1[t].y);
        o[j] = pk_f16(a, bb);
      }
      *(u32x4*)(ub + (16 * t + rho) * 80 + q * 16) = o;
    }
    char* ut = ws + WS_UT + (size_t)(sib * 32 + rho) * (SEQ * 2) + (panel * 128 + w * 32 + 8 * q) * 2;
#pragma unroll
    for (int c = 0; c < 2; c++) {
      const char* rp = ub + (8 * q + qq) * 80 + c * 32 + 8 * pp;
      u32x4 o = tr_frag(rp, rp + 4 * 80);
      *(u32x4*)(ut + (size_t)c * 16 * (SEQ * 2)) = o;
    }
  }
  f32x4 acc1[2][4] = {};
  cs = (f32x4){0.f, 0.f, 0.f, 0.f};
  k1_z1loop<0>(xr, wp, acc1, cs, ones);
  if (rho == 0) *(f32x4*)(csb1 + w * 64 + q * 16) = cs;
  u64 gv[14];
#pragma unroll
  for (int i = 0; i < 14; i++) gv[i] = __hip_atomic_load(gr + i * 64 + l, __ATOMIC_RELAXED, __HIP_MEMORY_SCOPE_AGENT);
  bar();
  k1_act(acc1, csb1, w, q, ln1);
  {
    char* z1 = ws + WS_Z1F + ((size_t)(panel * 8 + w * 2) * 56 + sib * 4) * 512 + l * 8;
#pragma unroll
    for (int t = 0; t < 2; t++)
#pragma unroll
      for (int p = 0; p < 4; p++)
        *(u32x2*)(z1 + (t * 56 + (p ^ w)) * 512) = (u32x2){pk_bf16(acc1[t][p].x, acc1[t][p].y), pk_bf16(acc1[t][p].z, acc1[t][p].w)};
  }
  float tot;
  for (u32 spins = 0;; spins++) {
    bool ok = true; tot = 0.f;
#pragma unroll
    for (int i = 0; i < 14; i++) { ok &= (u32)(gv[i] >> 32) == tag; tot += __uint_as_float((u32)gv[i]); }
    if (__all(ok) || spins > (1u << 22)) break;
    __builtin_amdgcn_s_sleep(2);
#pragma unroll
    for (int i = 0; i < 14; i++) gv[i] = __hip_atomic_load(gr + i * 64 + l, __ATOMIC_RELAXED, __HIP_MEMORY_SCOPE_AGENT);
  }
  if (sib == 0 && l == 0) __hip_atomic_store((GAS u32*)(ws + WS_EP + grp * 256), tag, __ATOMIC_RELAXED, __HIP_MEMORY_SCOPE_AGENT);
  f32x2 ln2[2];
  {
    auto a = __builtin_amdgcn_permlane16_swap(__float_as_uint(tot), __float_as_uint(tot), false, false);
#pragma unroll
    for (int t = 0; t < 2; t++) {
      auto bb = __builtin_amdgcn_permlane32_swap(a[t], a[t], false, false);
      ln2[t] = ln_scalars(__uint_as_float(bb[0]), __uint_as_float(bb[1]), 1.f / FH);
    }
  }
  {
    bar();
    const int gw = ((rho >> 3) << 3) | (((rho >> 1) & 1) << 2) | (((rho >> 2) & 1) << 1) | (rho & 1);
#pragma unroll
    for (int t = 0; t < 2; t++)
#pragma unroll
      for (int p = 0; p < 4; p++) {
        f32x2 a = (acc2[t][p].xy - ln2[t].x) * ln2[t].y, bb = (acc2[t][p].zw - ln2[t].x) * ln2[t].y;
        *(u32x2*)(stg + (16 * t + rho) * 128 + (((4 * p + q) ^ gw) * 8)) = (u32x2){pk_bf16(a.x, a.y), pk_bf16(bb.x, bb.y)};
      }
    const int r0 = 8 * q + qq, r1 = r0 + 4;
    const int g0 = (((r0 >> 3) & 1) << 3) | (((r0 >> 1) & 1) << 2) | (((r0 >> 2) & 1) << 1) | (r0 & 1);
    const int g1 = g0 ^ 2;
    const int kap = panel * 4 + w;
#pragma unroll
    for (int p = 0; p < 4; p++) {
      u32x4 o = tr_frag(stg + r0 * 128 + (((4 * p + pp) ^ g0) * 8), stg + r1 * 128 + (((4 * p + pp) ^ g1) * 8));
      int ft = sib * 4 + (p ^ w);
      int pn = ft / 7, fi = ft - pn * 7;
      *(u32x4*)(ws + WS_Z2F + ((size_t)(pn * 64 + kap) * 7 + fi) * 1024 + l * 16) = o;
    }
  }
}

constexpr int KG_ROW = 272, KG_WAVE = 53 * KG_ROW;
DEV f32x2 cexp_pow2(float gl, float fh, float fl, float p) {
  float m = __builtin_amdgcn_exp2f(gl * p);
  float r = __builtin_amdgcn_fractf(fh * p) + fl * p;
  return (f32x2){m * __builtin_amdgcn_cosf(r), m * __builtin_amdgcn_sinf(r)};
}
DEV void k1_kgen(char* smem, int kb, const u16* lar, const u16* aim, const u16* cre, const u16* cim, const u16* ldt, char* ws) {
  const int w = __builtin_amdgcn_readfirstlane(threadIdx.x >> 6), l = threadIdx.x & 63;
  const int d = kb * 2 + (w >> 1), hw = w & 1;
  const size_t o = (size_t)d * NMODE;
  const u32 vla = *(const u32*)(lar + o + hw * 128 + 2 * l), vai = *(const u32*)(aim + o + hw * 128 + 2 * l);
  const u32 vcr = *(const u32*)(cre + o + hw * 128 + 2 * l), vci = *(const u32*)(cim + o + hw * 128 + 2 * l);
  const u32 ocr = *(const u32*)(cre + o + (hw ^ 1) * 128 + 2 * l), oci = *(const u32*)(cim + o + (hw ^ 1) * 128 + 2 * l);
  const float dt = rbf(expf(bf2f(ldt[d])));
  float S = fabsf(bflo(vcr)) + fabsf(bfhi(vcr)) + fabsf(bflo(vci)) + fabsf(bfhi(vci));
  float S2 = fabsf(bflo(ocr)) + fabsf(bfhi(ocr)) + fabsf(bflo(oci)) + fabsf(bfhi(oci));
  S = hw ? S2 + S : S + S2;
#pragma unroll
  for (int m = 1; m < 16; m <<= 1) S += __shfl_xor(S, m);
  S = xsum32(xsum16(S));
  int e = (int)((__float_as_uint(S) >> 23) & 255u) - 125;
  e = e < -100 ? -100 : (e > 100 ? 100 : e);
  e = __builtin_amdgcn_readfirstlane(e);
  const float sc = __uint_as_float((u32)(127 + 15 - e) << 23);
  char* tab = smem + w * KG_WAVE;
  f32x16 acc = {};
#pragma unroll 1
  for (int rnd = 0; rnd < 2; rnd++) {
    const float la = rnd ? bfhi(vla) : bflo(vla), ai = rnd ? bfhi(vai) : bflo(vai);
    const f32x2 c0 = {(rnd ? bfhi(vcr) : bflo(vcr)) * sc, (rnd ? bfhi(vci) : bflo(vci)) * sc};
    const float zr = -rbf(expf(la)) * dt, zi = ai * dt;
    const float gl = zr * 1.44269504089f;
    const float fh = zi * 0.15915494309f;
    const float fl = fmaf(zi, 0.15915494309f, -fh) + zi * 6.4206382e-9f;
    u32* tw = (u32*)(tab + l * 4);
#define TW(row, v) tw[(row) * (KG_ROW / 4)] = (v)
    const f32x2 e1 = cexp_pow2(gl, fh, fl, 1.f), e2 = csqr(e1), e4 = csqr(e2);
    const f32x2 e8 = cexp_pow2(gl, fh, fl, 8.f), e16 = csqr(e8), e32 = csqr(e16);
    const f32x2 e64 = cexp_pow2(gl, fh, fl, 64.f), e128 = csqr(e64);
    const f32x2 e256 = cexp_pow2(gl, fh, fl, 256.f), e512 = csqr(e256);
    const f32x2 e1024 = cexp_pow2(gl, fh, fl, 1024.f), e2048 = csqr(e1024);
    {
      f32x2 t[16];
      t[0] = (f32x2){32768.f, 0.f};
      t[1] = e8 * 32768.f;
      t[2] = e16 * 32768.f; t[3] = cmul(t[1], e16);
#pragma unroll
      for (int i = 0; i < 4; i++) t[4 + i] = cmul(t[i], e32);
#pragma unroll
      for (int i = 0; i < 8; i++) t[8 + i] = cmul(t[i], e64);
#pragma unroll
      for (int i = 0; i < 16; i++) { TW(i, pk_f16(t[i].x, t[i].y)); f32x2 u = cmul(t[i], e128); TW(16 + i, pk_f16(u.x, u.y)); }
    }
    {
      f32x2 a[21];
      a[0] = c0; a[1] = cmul(c0, e1); a[2] = cmul(c0, e2); a[3] = cmul(a[1], e2);
#pragma unroll
      for (int i = 0; i < 4; i++) a[4 + i] = cmul(a[i], e4);
#pragma unroll
      for (int i = 0; i < 4; i++) a[8 + i] = cmul(a[2 * i], e256);
      a[12] = cmul(a[0], e512); a[13] = cmul(a[4], e512); a[14] = cmul(a[8], e512); a[15] = cmul(a[10], e512);
      a[16] = cmul(a[0], e1024); a[17] = cmul(a[8], e1024); a[18] = cmul(a[12], e1024); a[19] = cmul(a[14], e1024);
      a[20] = cmul(a[0], e2048);
#pragma unroll
      for (int i = 0; i < 21; i++) TW(32 + i, pk_f16(a[i].x, -a[i].y));
    }
#undef TW
    const int r = l & 31, h = l >> 5;
    const char* pa = tab + (32 + (r < 21 ? r : 20)) * KG_ROW + h * 16;
    const char* pb = tab + r * KG_ROW + h * 16;
#pragma unroll
    for (int kk = 0; kk < 8; kk++) acc = mfma32_h(*(const u32x4*)(pa + kk * 32), *(const u32x4*)(pb + kk * 32), acc);
  }
  float* part = (float*)(smem + (w | 1) * KG_WAVE);
  u16* cmp = (u16*)(smem + (w & 2) * KG_WAVE);
  if (hw) {
#pragma unroll
    for (int i = 0; i < 12; i++) part[i * 64 + l] = acc[i];
  }
  __syncthreads();
  if (!hw) {
    const int col = l & 31, h = l >> 5;
#pragma unroll
    for (int i = 0; i < 12; i++) {
      float v = (acc[i] + part[i * 64 + l]) * (1.f / 32768.f);
      const int j = i & 3, gq = i >> 2;
      int id;
      if (gq == 0) id = j + 4 * h + 8 * col;
      else if (gq == 1) id = h ? 384 + 64 * (j >> 1) + (j & 1) + 2 * col : 256 + j + 4 * col;
      else id = h ? ((j == 0 && col == 0) ? 640 : 700) : 512 + 32 * j + col;
      cmp[id] = __builtin_bit_cast(u16, (_Float16)v);
    }
    if (l == 0) *(float*)(ws + WS_KS + d * 4) = dt * __uint_as_float((u32)(127 + e - 14) << 23);
  }
  __syncthreads();
  {
    const int t = hw * 64 + l;
#pragma unroll
    for (int c = 0; c < 2; c++) {
      const int x0 = (c * 128 + t) * 8;
      u32x4 ov;
#pragma unroll
      for (int j = 0; j < 4; j++) {
        const int l0 = 2047 - (x0 + 2 * j), l1 = l0 - 1;
        const u32 bits = pk_bf16((float)l0, (float)l1);
        int i0 = (int)(bits & 0xffffu) - 0x4280, i1 = (int)(bits >> 16) - 0x4280;
        i0 = l0 < 128 ? l0 : i0; i1 = l1 < 128 ? l1 : i1;
        ov[j] = (u32)cmp[i0] | ((u32)cmp[i1] << 16);
      }
      *(u32x4*)(ws + WS_RK + (size_t)d * (SEQ * 2) + x0 * 2) = ov;
    }
  }
}

__global__ __launch_bounds__(256, 2) void k1(const u16* x, const u16* win, const u16* lar, const u16* aim, const u16* cre,
                                             const u16* cim, const u16* ldt, char* ws) {
  __shared__ __attribute__((aligned(16))) char smem[81920];
  if (blockIdx.x < 224) k1_gemm(smem, blockIdx.x, x, win, ws);
  else k1_kgen(smem, blockIdx.x - 224, lar, aim, cre, cim, ldt, ws);
}

constexpr int k2_pos_B(int i) {
  int n = 0;
  n += 8; n += 7; if (i == 0) return n;
  n += 7; if (i == 1) return n;
  n += 8; n += 7; if (i == 2) return n;
  for (int s = 0; s < 16; s++) { if ((s & 1) && (s + 3) / 2 <= 7) n += 8; if (s + 3 <= 15) { n += 7; if (s + 3 == i) return n; } }
  return -1;
}
constexpr int k2_pos_A(int a) {
  if (a == 0) return 8;
  if (a == 1) return 30;
  int n = 37;
  for (int s = 0; s < 16; s++) { if ((s & 1) && (s + 3) / 2 <= 7) { n += 8; if ((s + 3) / 2 == a) return n; } if (s + 3 <= 15) n += 7; }
  return -1;
}
constexpr int k2_issued_before(int i) {
  int n = 37;
  for (int s = 0; s < i; s++) { if ((s & 1) && (s + 3) / 2 <= 7) n += 8; if (s + 3 <= 15) n += 7; }
  return n;
}
constexpr int k2_wait(int i) {
  int need = k2_pos_B(i);
  if ((i & 1) && (i + 1) / 2 <= 7) { int a = k2_pos_A((i + 1) / 2); need = a > need ? a : need; }
  return k2_issued_before(i) - need;
}
struct K2G { const char* wsp; const char* z2; u32 aoff[2]; u32 boff; u32 adst; int w; };
template <int SS> DEV void k2_issue_a(const K2G& g) {
#pragma unroll
  for (int i = 0; i < 8; i++) dma16(g.wsp + SS * 128 + (size_t)i * 8 * 4096, g.aoff[i & 1], g.adst + (SS & 1) * 8192 + ((i ^ (2 * g.w)) * 1024));
}
template <int I, int F> DEV void k2_issue_b1(const K2G& g, u32x4& d) { gload16<(F - 3) * 1024>(d, g.z2 + I * 7168, g.boff); }
template <int I> DEV void k2_issue_b(const K2G& g, u32x4 (&bf)[7]) {
  k2_issue_b1<I, 0>(g, bf[0]); k2_issue_b1<I, 1>(g, bf[1]); k2_issue_b1<I, 2>(g, bf[2]); k2_issue_b1<I, 3>(g, bf[3]);
  k2_issue_b1<I, 4>(g, bf[4]); k2_issue_b1<I, 5>(g, bf[5]); k2_issue_b1<I, 6>(g, bf[6]);
}
template <int I> DEV u32x4 k2_read_a(const char* ap0, const char* ap1, int t) {
  return *(const u32x4*)(((I & 1) ? ap1 : ap0) + ((I >> 1) & 1) * 8192 + t * 2048);
}
template <int I, int F> DEV void k2_last(const K2G& g, u32x4 (&bf)[7], const u32x4& a, f32x4 (&acc)[7]) {
  acc[F] = mfma_bf(bf[F], a, acc[F]);
  if constexpr (I + 3 <= 15) { __builtin_amdgcn_sched_barrier(0); k2_issue_b1<I + 3, F>(g, bf[F]); __builtin_amdgcn_sched_barrier(0); }
  if constexpr (F < 6) k2_last<I, F + 1>(g, bf, a, acc);
}
template <int I>
DEV void k2_step(const K2G& g, u32x4 (&bf)[3][7], u32x4 (&af)[4], const char* ap0, const char* ap1, f32x4 (&acc)[4][7]) {
  wait_vm<k2_wait(I)>();
#pragma unroll
  for (int f = 0; f < 7; f++) pin(bf[I % 3][f]);
  if constexpr ((I & 1) && (I + 3) / 2 <= 7) {
    asm volatile("s_waitcnt lgkmcnt(0)" ::: "memory");
    k2_issue_a<(I + 3) / 2>(g);
  }
#pragma unroll
  for (int t = 0; t < 3; t++) {
#pragma unroll
    for (int f = 0; f < 7; f++) acc[t][f] = mfma_bf(bf[I % 3][f], af[t], acc[t][f]);
    __builtin_amdgcn_sched_barrier(0);
    if constexpr (I < 15) af[t] = k2_read_a<I + 1>(ap0, ap1, t);
    __builtin_amdgcn_sched_barrier(0);
  }
  k2_last<I, 0>(g, bf[I % 3], af[3], acc[3]);
  if constexpr (I < 15) af[3] = k2_read_a<I + 1>(ap0, ap1, 3);
  if constexpr (I < 15) k2_step<I + 1>(g, bf, af, ap0, ap1, acc);
}

DEV void k2_gemm(char* smem, int b, const u16* wsp, const u16* bsp, char* ws) {
  const int xcd = b & 7, idx = b >> 3;
  const int tp = (xcd >> 1) * 8 + (idx >> 2), fp = (xcd & 1) * 4 + (idx & 3);
  const int w = __builtin_amdgcn_readfirstlane(threadIdx.x >> 6), l = threadIdx.x & 63;
  const int rho = l & 15, q = l >> 4;
  char* const reg = smem + w * 16384;
  K2G g;
  g.wsp = (const char*)wsp + (size_t)tp * 64 * 4096 + w * 1024;
  g.z2 = ws + WS_Z2F + ((size_t)(fp * 64 + w * 16) * 7 + 3) * 1024;
  g.aoff[0] = (u32)((l >> 3) * 4096 + (((l & 7) ^ (l >> 4)) * 16));
  g.aoff[1] = (u32)((l >> 3) * 4096 + (((l & 7) ^ ((l >> 4) | 4)) * 16));
  g.boff = l * 16;
  g.adst = lds_addr(reg);
  g.w = w;
  u32x4 bf[3][7], af[4];
  k2_issue_a<0>(g); k2_issue_b<0>(g, bf[0]); k2_issue_b<1>(g, bf[1]); k2_issue_a<1>(g); k2_issue_b<2>(g, bf[2]);
  const char* ap0 = reg + rho * 128 + (((0 + q) ^ (rho >> 1)) * 16);
  const char* ap1 = reg + rho * 128 + (((4 + q) ^ (rho >> 1)) * 16);
  f32x4 acc[4][7] = {};
  wait_vm<29>();
#pragma unroll
  for (int t = 0; t < 4; t++) af[t] = k2_read_a<0>(ap0, ap1, t);
  k2_step<0>(g, bf, af, ap0, ap1, acc);
  const int tt = tp * 4 + w;
  u32x2 z1[7];
#pragma unroll
  for (int f = 0; f < 7; f++) z1[f] = *(const u32x2*)(ws + WS_Z1F + ((size_t)tt * 56 + fp * 7 + f) * 512 + l * 8);
  const float bs = bf2f(bsp[tt * 16 + rho]);
#pragma unroll
  for (int f = 0; f < 7; f++) {
    *(f32x4*)(reg + f * 1024 + l * 16) = acc[1][f];
    *(f32x4*)(reg + (7 + f) * 1024 + l * 16) = acc[3][f];
  }
  bar();
  char* const in1 = smem + (w ^ 1) * 16384;
#pragma unroll
  for (int f = 0; f < 7; f++) {
    acc[0][f] += *(const f32x4*)(in1 + f * 1024 + l * 16);
    acc[2][f] += *(const f32x4*)(in1 + (7 + f) * 1024 + l * 16);
  }
#pragma unroll
  for (int f = 0; f < 7; f++) *(f32x4*)(in1 + f * 1024 + l * 16) = acc[2][f];
  bar();
  char* const in2 = smem + (w ^ 3) * 16384;
  u32x2 hv[7];
#pragma unroll
  for (int f = 0; f < 7; f++) {
    f32x4 v = acc[0][f] + *(const f32x4*)(in2 + f * 1024 + l * 16);
    v = v + bs;
    hv[f] = (u32x2){pk_bf16(v.x * bflo(z1[f].x), v.y * bfhi(z1[f].x)), pk_bf16(v.z * bflo(z1[f].y), v.w * bfhi(z1[f].y))};
  }
  char* hb = ws + WS_HF + ((size_t)tt * 28 + ((fp * 7) >> 1)) * 1024 + l * 16;
  if (fp & 1) {
    *(u32x2*)(hb + 8) = hv[0];
#pragma unroll
    for (int i = 0; i < 3; i++) *(u32x4*)(hb + (i + 1) * 1024) = (u32x4){hv[1 + 2 * i].x, hv[1 + 2 * i].y, hv[2 + 2 * i].x, hv[2 + 2 * i].y};
  } else {
#pragma unroll
    for (int i = 0; i < 3; i++) *(u32x4*)(hb + i * 1024) = (u32x4){hv[2 * i].x, hv[2 * i].y, hv[1 + 2 * i].x, hv[1 + 2 * i].y};
    *(u32x2*)(hb + 3 * 1024) = hv[6];
  }
}

constexpr int CV_CS = 4160, CV_T = 8 * CV_CS, CV_U = 4608, CV_CH = CV_T + CV_U;
DEV void k2_conv(char* smem, int cb, const u16* dsk, char* ws) {
  const int w = __builtin_amdgcn_readfirstlane(threadIdx.x >> 6), l = threadIdx.x & 63;
  const int d = cb * 2 + (w >> 1), hw = w & 1, t = hw * 64 + l;
  const int rho = l & 15, q = l >> 4;
  char* const T = smem + (w >> 1) * CV_CH;
  char* const U = T + CV_T;
  const char* rk = ws + WS_RK + (size_t)d * (SEQ * 2);
  const char* ut = ws + WS_UT + (size_t)d * (SEQ * 2);
  u32x4 ra[2], rb[2], uu[2];
#pragma unroll
  for (int c = 0; c < 2; c++) {
    const int y = c * 128 + t;
    ra[c] = *(const u32x4*)(rk + y * 16);
    rb[c] = *(const u32x4*)(rk + (y < 255 ? y + 1 : y) * 16);
    uu[c] = *(const u32x4*)(ut + y * 16);
  }
  const float ks = *(const float*)(ws + WS_KS + d * 4);
  const float dk = bf2f(dsk[d]);
  const u32x4 zero = {0u, 0u, 0u, 0u};
  if (t < 32) *(u32x4*)(U + t * 16) = zero;
  else if (t < 40) *(u32x4*)(T + (t - 32) * CV_CS + 4096) = zero;
#pragma unroll
  for (int c = 0; c < 2; c++) {
    const int y = c * 128 + t;
    *(u32x4*)(U + 512 + y * 16) = uu[c];
    if (c == 1 && y == 255) rb[c] = zero;
    u32 dw[8] = {ra[c].x, ra[c].y, ra[c].z, ra[c].w, rb[c].x, rb[c].y, rb[c].z, rb[c].w};
    u32 od[7];
#pragma unroll
    for (int i = 0; i < 7; i++) od[i] = __builtin_amdgcn_alignbit(dw[i + 1], dw[i], 16);
#pragma unroll
    for (int k = 0; k < 4; k++) {
      *(u32x4*)(T + (2 * k) * CV_CS + y * 16) = (u32x4){dw[k], dw[k + 1], dw[k + 2], dw[k + 3]};
      *(u32x4*)(T + (2 * k + 1) * CV_CS + y * 16) = (u32x4){od[k], od[k + 1], od[k + 2], od[k + 3]};
    }
  }
  __syncthreads();
  const char* pa = T + (7 - (rho & 7)) * CV_CS + (255 - (rho >> 3) + q) * 16;
  const char* pb = U + 512 + rho * 32 + q * 16;
  f32x4 acc[8] = {};
#pragma unroll 1
  for (int ci = 0; ci < 4; ci++) {
    const int r = 1 + 2 * (hw * 4 + ci);
    u32x4 A[8], B[8];
#pragma unroll
    for (int i = 0; i < 8; i++) {
      A[i] = *(const u32x4*)(pa - 32 * r - 512 * i);
      B[i] = *(const u32x4*)(pb - 32 * r + 512 * i);
    }
#pragma unroll
    for (int s = 0; s < 8; s++)
#pragma unroll
      for (int a = 0; a <= s; a++) acc[s] = mfma_h(A[a], B[s - a], acc[s]);
  }
  __syncthreads();
  {
    char* box = T + hw * 4096;
    if (hw) {
#pragma unroll
      for (int i = 0; i < 4; i++) *(f32x4*)(box + i * 1024 + l * 16) = acc[i];
#pragma unroll
      for (int i = 0; i < 4; i++) acc[i] = acc[4 + i];
    } else {
#pragma unroll
      for (int i = 0; i < 4; i++) *(f32x4*)(box + i * 1024 + l * 16) = acc[4 + i];
    }
  }
  __syncthreads();
  {
    const char* box = T + (hw ^ 1) * 4096;
#pragma unroll
    for (int i = 0; i < 4; i++) {
      const int pos = 256 * (4 * hw + i) + 16 * rho + 4 * q;
      f32x4 v = (acc[i] + *(const f32x4*)(box + i * 1024 + l * 16)) * ks;
      u32x2 ub = *(const u32x2*)(U + 512 + pos * 2);
      f16x2 u0 = as_h2(ub.x), u1 = as_h2(ub.y);
      float y0 = rbf(v.x) + rbf((float)u0.x * dk), y1 = rbf(v.y) + rbf((float)u0.y * dk);
      float y2 = rbf(v.z) + rbf((float)u1.x * dk), y3 = rbf(v.w) + rbf((float)u1.y * dk);
      *(u32x2*)(ws + WS_YT + (size_t)d * (SEQ * 2) + pos * 2) = (u32x2){pk_bf16(y0, y1), pk_bf16(y2, y3)};
    }
  }
}

__global__ __launch_bounds__(256, 2) void k2(const u16* wsp, const u16* bsp, const u16* dsk, char* ws) {
  __shared__ __attribute__((aligned(16))) char smem[81920];
  if (blockIdx.x < 256) k2_gemm(smem, blockIdx.x, wsp, bsp, ws);
  else k2_conv(smem, blockIdx.x - 256, dsk, ws);
}

struct K3G { const char* wo; const char* hf; u32 woff; u32 hoff[4]; u32 wdst; };
template <int I> DEV void k3_issue(const K3G& g, u32x4 (&hf)[7][4]) {
#pragma unroll
  for (int i = 0; i < 4; i++) dma16(g.wo + (size_t)(I * 32 + i * 8) * (DM * 2), g.woff, g.wdst + (I * 4 + i) * 1024);
#pragma unroll
  for (int t = 0; t < 4; t++) gload16<0>(hf[I][t], g.hf + I * 1024, g.hoff[t]);
  if constexpr (I < 6) k3_issue<I + 1>(g, hf);
}
template <int I> DEV void k3_step(u32x4 (&hf)[7][4], const char* const (&wp)[4], f32x4 (&acc)[4][4]) {
  wait_vm<8 * (6 - I) + 4>();
#pragma unroll
  for (int t = 0; t < 4; t++) pin(hf[I][t]);
  u32x4 wf[4];
#pragma unroll
  for (int p = 0; p < 4; p++) wf[p] = tr_frag(wp[p] + I * 4096, wp[p] + I * 4096 + 2048);
#pragma unroll
  for (int p = 0; p < 4; p++)
#pragma unroll
    for (int t = 0; t < 4; t++) acc[t][p] = mfma_bf(wf[p], hf[I][t], acc[t][p]);
  if constexpr (I < 6) k3_step<I + 1>(hf, wp, acc);
}
__global__ __launch_bounds__(256, 1) void k3(const u16* x, const u16* wout, u16* out, char* ws) {
  __shared__ __attribute__((aligned(16))) char smem[4 * 28672 + 8192];
  const int b = blockIdx.x, xcd = b & 7, idx = b >> 3;
  const int tp = xcd * 4 + idx / 7, dp = idx % 7;
  const int w = __builtin_amdgcn_readfirstlane(threadIdx.x >> 6), l = threadIdx.x & 63;
  const int rho = l & 15, q = l >> 4, qq = (l >> 2) & 3, pp = l & 3;
  char* const reg = smem + w * 28672;
  char* const Y = smem + 4 * 28672;
  K3G g;
  g.wo = (const char*)wout + (size_t)(w * 224) * (DM * 2) + dp * 128;
  g.hf = ws + WS_HF + ((size_t)tp * 4 * 28 + w * 7) * 1024;
  g.woff = (u32)((l >> 3) * (DM * 2) + (((l & 7) ^ ((l >> 4) << 1)) * 16));
#pragma unroll
  for (int t = 0; t < 4; t++) g.hoff[t] = (u32)((t ^ w) * 28 * 1024 + l * 16);
  g.wdst = lds_addr(reg);
  u32x4 hf[7][4];
  k3_issue<0>(g, hf);
#pragma unroll
  for (int i = 0; i < 2; i++)
    dma16(ws + WS_YT + (size_t)(dp * 64 + w * 16 + i * 8) * (SEQ * 2) + tp * 128, (u32)((l >> 3) * (SEQ * 2) + (((l & 7) ^ ((l >> 4) << 1)) * 16)),
          lds_addr(Y) + (w * 2 + i) * 1024);
  u32x4 xv[2];
  const u32 xo = (u32)((l >> 3) * (DM * 2) + (l & 7) * 16);
  const size_t xb = (size_t)(tp * 64 + w * 16) * (DM * 2) + dp * 128;
  gload16<0>(xv[0], (const char*)x + xb, xo);
  gload16<0>(xv[1], (const char*)x + xb + 8 * (DM * 2), xo);
  const char* wp[4];
  const int sg = ((q & 1) << 1) | (qq >> 1);
#pragma unroll
  for (int p = 0; p < 4; p++) wp[p] = reg + (4 * q + qq) * 128 + ((p ^ sg) * 32) + 8 * pp;
  f32x4 acc[4][4] = {};
  k3_step<0>(hf, wp, acc);
  wait_vm<0>();
  pin(xv[0]); pin(xv[1]);
#pragma unroll
  for (int p = 0; p < 4; p++) {
    *(f32x4*)(reg + p * 1024 + l * 16) = acc[1][p];
    *(f32x4*)(reg + (4 + p) * 1024 + l * 16) = acc[3][p];
  }
  bar();
  {
    const char* in1 = smem + (w ^ 1) * 28672;
#pragma unroll
    for (int p = 0; p < 4; p++) {
      acc[0][p] += *(const f32x4*)(in1 + p * 1024 + l * 16);
      acc[2][p] += *(const f32x4*)(in1 + (4 + p) * 1024 + l * 16);
    }
#pragma unroll
    for (int p = 0; p < 4; p++) *(f32x4*)(reg + (8 + p) * 1024 + l * 16) = acc[2][p];
  }
  bar();
  {
    const char* in2 = smem + (w ^ 2) * 28672;
    char* fin = reg + 12288;
#pragma unroll
    for (int p = 0; p < 4; p++) {
      f32x4 v = acc[0][p] + *(const f32x4*)(in2 + (8 + p) * 1024 + l * 16);
      u32x2 yv = tr_read(Y + (16 * p + 4 * q + qq) * 128 + ((w ^ sg) * 32) + 8 * pp);
      v += (f32x4){bflo(yv.x), bfhi(yv.x), bflo(yv.y), bfhi(yv.y)};
      *(f32x4*)(fin + rho * 272 + p * 64 + q * 16) = v;
    }
#pragma unroll
    for (int i = 0; i < 2; i++) {
      const char* rp = fin + (8 * i + (l >> 3)) * 272 + (l & 7) * 32;
      f32x4 a = *(const f32x4*)(rp), c = *(const f32x4*)(rp + 16);
      u32x4 o = {pk_bf16(a.x + bflo(xv[i].x), a.y + bfhi(xv[i].x)), pk_bf16(a.z + bflo(xv[i].y), a.w + bfhi(xv[i].y)),
                 pk_bf16(c.x + bflo(xv[i].z), c.y + bfhi(xv[i].z)), pk_bf16(c.z + bflo(xv[i].w), c.w + bfhi(xv[i].w))};
      *(u32x4*)((char*)out + xb + (size_t)i * 8 * (DM * 2) + xo) = o;
    }
  }
}

extern "C" void kernel_launch(void* const* d_in, const int* in_sizes, int n_in, void* d_out, int out_size, void* d_ws, size_t ws_size,
                              hipStream_t stream) {
  const u16* x = (const u16*)d_in[0];
  char* ws = (char*)d_ws;
  k1<<<448, 256, 0, stream>>>(x, (const u16*)d_in[8], (const u16*)d_in[1], (const u16*)d_in[2], (const u16*)d_in[3], (const u16*)d_in[4],
                              (const u16*)d_in[5], ws);
  k2<<<480, 256, 0, stream>>>((const u16*)d_in[9], (const u16*)d_in[10], (const u16*)d_in[6], ws);
  k3<<<224, 256, 0, stream>>>(x, (const u16*)d_in[11], (u16*)d_out, ws);
}
```

```cpp
#include <hip/hip_runtime.h>
#include <stdint.h>

typedef unsigned short u16;
typedef unsigned int u32;
typedef unsigned long long u64;
typedef __attribute__((ext_vector_type(4))) u32 u32x4;
typedef __attribute__((ext_vector_type(2))) u32 u32x2;
typedef __attribute__((ext_vector_type(4))) float f32x4;
typedef __attribute__((ext_vector_type(2))) float f32x2;
typedef __attribute__((ext_vector_type(16))) float f32x16;
typedef __attribute__((ext_vector_type(8))) __bf16 bf16x8;
typedef __attribute__((ext_vector_type(2))) __bf16 bf16x2;
typedef __attribute__((ext_vector_type(8))) _Float16 f16x8;
typedef __attribute__((ext_vector_type(2))) _Float16 f16x2;
typedef __attribute__((ext_vector_type(4))) short s16x4;
#define LDSAS __attribute__((address_space(3)))
#define GAS __attribute__((address_space(1)))
#define DEV __device__ __forceinline__
#if defined(__HIP_DEVICE_COMPILE__)
#define NOPK __attribute__((target("no-packed-fp32-ops")))
#else
#define NOPK
#endif
#define SB() __builtin_amdgcn_sched_barrier(0)

constexpr int SEQ = 2048, DM = 448, NMODE = 256, FF = 1792, FH = 896;

constexpr size_t WS_Z2F = 0;
constexpr size_t WS_Z1F = 4u << 20;
constexpr size_t WS_HF  = 8u << 20;
constexpr size_t WS_UT  = 12u << 20;
constexpr size_t WS_YT  = 14u << 20;
constexpr size_t WS_GR  = 16u << 20;
constexpr size_t WS_EP  = 17u << 20;

#define STAMP(i)
#define STAMP_DECL
#define STAMP_ARG
#define STAMP_PASS
#define STAMP_FLUSH(kern)
DEV float bf2f(u32 v) { return __uint_as_float(v << 16); }
DEV float bflo(u32 v) { return __uint_as_float(v << 16); }
DEV float bfhi(u32 v) { return __uint_as_float(v & 0xffff0000u); }
DEV u32 pk_bf16(float a, float b) { bf16x2 v = {(__bf16)a, (__bf16)b}; return __builtin_bit_cast(u32, v); }
DEV u32 pk_f16(float a, float b) { f16x2 v = {(_Float16)a, (_Float16)b}; return __builtin_bit_cast(u32, v); }
DEV f16x2 as_h2(u32 v) { return __builtin_bit_cast(f16x2, v); }
DEV float rbf(float a) { return __uint_as_float(pk_bf16(a, a) << 16); }
DEV f32x4 mfma_bf(u32x4 a, u32x4 b, f32x4 c) {
  return __builtin_amdgcn_mfma_f32_16x16x32_bf16(__builtin_bit_cast(bf16x8, a), __builtin_bit_cast(bf16x8, b), c, 0, 0, 0);
}
DEV f32x4 mfma_h(u32x4 a, u32x4 b, f32x4 c) {
  return __builtin_amdgcn_mfma_f32_16x16x32_f16(__builtin_bit_cast(f16x8, a), __builtin_bit_cast(f16x8, b), c, 0, 0, 0);
}
DEV f32x16 mfma32_h(u32x4 a, u32x4 b, f32x16 c) {
  return __builtin_amdgcn_mfma_f32_32x32x16_f16(__builtin_bit_cast(f16x8, a), __builtin_bit_cast(f16x8, b), c, 0, 0, 0);
}
DEV u32x2 tr_read(const char* p) {
  s16x4 v = __builtin_amdgcn_ds_read_tr16_b64_v4i16((LDSAS s16x4*)p);
  return __builtin_bit_cast(u32x2, v);
}
DEV u32x4 tr_frag(const char* p0, const char* p1) {
  u32x2 a = tr_read(p0), b = tr_read(p1);
  return (u32x4){a.x, a.y, b.x, b.y};
}
DEV u32 lds_addr(const void* p) { return (u32)(uintptr_t)(LDSAS const char*)p; }
DEV void dma16(const void* sbase, u32 voff, u32 ldst) {
  asm volatile("s_mov_b32 m0, %2\n\ts_nop 0\n\tglobal_load_lds_dwordx4 %0, %1" ::"v"(voff), "s"(sbase), "s"(ldst) : "memory");
}
template <int OFF> DEV void gload16(u32x4& d, const void* sbase, u32 voff) {
  asm volatile("global_load_dwordx4 %0, %1, %2 offset:%3" : "=v"(d) : "v"(voff), "s"(sbase), "i"(OFF) : "memory");
}
DEV void gstore16(void* sbase, u32 voff, u32x4 d) {
  asm volatile("global_store_dwordx4 %0, %1, %2 sc1\n\ts_nop 1" ::"v"(voff), "v"(d), "s"(sbase) : "memory");
}
DEV void gstore8(void* sbase, u32 voff, u32x2 d) {
  asm volatile("global_store_dwordx2 %0, %1, %2 sc1" ::"v"(voff), "v"(d), "s"(sbase) : "memory");
}
DEV void gstore16p(void* sbase, u32 voff, u32x4 d) {
  asm volatile("global_store_dwordx4 %0, %1, %2\n\ts_nop 1" ::"v"(voff), "v"(d), "s"(sbase) : "memory");
}
#define Z2STORE gstore16
DEV void gload4(u32& d, const void* sbase, u32 voff) {
  asm volatile("global_load_dword %0, %1, %2" : "=v"(d) : "v"(voff), "s"(sbase) : "memory");
}
template <int N> DEV void wait_vm() { asm volatile("s_waitcnt vmcnt(%0)" ::"i"(N) : "memory"); }
DEV void pin(u32x4& a) { asm volatile("" : "+v"(a)); }
DEV void bar() { asm volatile("s_waitcnt lgkmcnt(0)\n\ts_barrier" ::: "memory"); }
DEV float xsum16(float v) { auto r = __builtin_amdgcn_permlane16_swap(__float_as_uint(v), __float_as_uint(v), false, false); return __uint_as_float(r[0]) + __uint_as_float(r[1]); }
DEV float xsum32(float v) { auto r = __builtin_amdgcn_permlane32_swap(__float_as_uint(v), __float_as_uint(v), false, false); return __uint_as_float(r[0]) + __uint_as_float(r[1]); }
DEV f32x2 ln_scalars(float sum, float sq, float inv_n) {
  float m = sum * inv_n;
  float v = fmaxf(sq * inv_n - m * m, 0.f);
  float sd = rbf(__builtin_amdgcn_sqrtf(rbf(rbf(v) + 1e-5f)));
  return (f32x2){rbf(m), __builtin_amdgcn_rcpf(sd)};
}
DEV f32x2 gelu2(f32x2 x) {
  const float c1 = -2.3022081983f, c2 = -0.1029432396f;
  f32x2 t = x * x;
  t = t * c2 + c1;
  t = t * x;
  f32x2 e = {__builtin_amdgcn_exp2f(t.x), __builtin_amdgcn_exp2f(t.y)};
  e = e + 1.0f;
  f32x2 r = {__builtin_amdgcn_rcpf(e.x), __builtin_amdgcn_rcpf(e.y)};
  return x * r;
}
DEV f32x2 cmul(f32x2 a, f32x2 b) { return (f32x2){fmaf(a.x, b.x, -(a.y * b.y)), fmaf(a.x, b.y, a.y * b.x)}; }
DEV f32x2 csqr(f32x2 a) { return (f32x2){a.x * a.x - a.y * a.y, 2.f * a.x * a.y}; }

#define K1_PRO 12
#define K1_PF 4
constexpr int K1_NOPS = 28 + K1_PF;
constexpr int k1_issued(int m) { int n = K1_PRO + 3 * m; return n > K1_NOPS ? K1_NOPS : n; }
constexpr int k1_need(int m) { return m <= 5 ? 3 * m + 6 : 22; }
constexpr int k1_slot(int u) { return u < 9 ? u : u - 9; }
constexpr int K1_X = 73728, K1_UT = K1_X + 65536, K1_CS = K1_UT + 32 * 272, K1_LN = K1_CS + 1024, K1_PB = K1_LN + 1024,
              K1_TOT = K1_PB + 1024, K1_FLG = K1_TOT + 2048, K1_DUMP = K1_FLG + 64, K1_LDS = K1_DUMP + 1024;

typedef LDSAS char* lptr;
DEV lptr pinned(lptr p) { asm volatile("" : "+v"(p)); return p; }
DEV u32x4 lread16(lptr p) { return *(LDSAS u32x4*)p; }
DEV u32x4 ltr_frag(lptr p0, lptr p1) {
  s16x4 a = __builtin_amdgcn_ds_read_tr16_b64_v4i16((LDSAS s16x4*)p0), b = __builtin_amdgcn_ds_read_tr16_b64_v4i16((LDSAS s16x4*)p1);
  u32x2 x = __builtin_bit_cast(u32x2, a), y = __builtin_bit_cast(u32x2, b);
  return (u32x4){x.x, x.y, y.x, y.y};
}

struct K1G { const char* win; const char* xb; const char* pf; u32 xoff[2]; u32 woff; u32 wdst; u32 xdst; u32 l16; u32 dump; int m0; lptr str[4]; lptr wp[4]; };
template <int OP> DEV void k1_op(const K1G& g) {
  if constexpr (OP < 21 && OP % 3 < 2) {
    constexpr int M = OP / 3, R = OP % 3;
    int line = M + g.m0; line = line >= 7 ? line - 7 : line;
    dma16(g.xb + line * 128, g.xoff[R], g.xdst + (M & 3) * 16384 + R * 1024);
  } else if constexpr (OP < 28) {
    constexpr int U = OP < 21 ? OP / 3 : OP - 14;
    int line = U % 7 + g.m0; line = line >= 7 ? line - 7 : line;
    dma16(g.win + (size_t)line * 64 * (FF * 2) + (U < 7 ? FH * 2 : 0), g.woff, g.wdst + k1_slot(U) * 8192);
  } else {
    dma16(g.pf + (OP - 28) * 1024, g.l16, g.dump);
  }
}
template <int A, int B> DEV void k1_ops(const K1G& g) {
  if constexpr (A < B) { k1_op<A>(g); k1_ops<A + 1, B>(g); }
}
template <int M> DEV void k1_xr(const K1G& g, u32x4 (&xf)[7][4]) {
#pragma unroll
  for (int i = 0; i < 4; i++) xf[M][i] = lread16(g.str[i] + (M & 3) * 16384);
}
template <int U> DEV u32x4 k1_wf(const K1G& g, int f) {
  if constexpr (k1_slot(U) < 8) return ltr_frag(g.wp[f] + k1_slot(U) * 8192, g.wp[f] + k1_slot(U) * 8192 + 512);
  else { lptr p = pinned(g.wp[f] + 65536); return ltr_frag(p, p + 512); }
}
template <int M, int U> DEV void k1_main(const K1G& g, u32x4 (&xf)[7][4], u32x4 (&wf)[4], f32x4 (&acc)[2][2], f32x4& cs, u32x4 ones) {
  cs = mfma_bf(wf[0], ones, cs);
#pragma unroll
  for (int f = 0; f < 4; f++) {
    acc[0][f & 1] = mfma_bf(wf[f], xf[M][(f >> 1) * 2], acc[0][f & 1]);
    acc[1][f & 1] = mfma_bf(wf[f], xf[M][(f >> 1) * 2 + 1], acc[1][f & 1]);
    SB(); if constexpr (U < 13) wf[f] = k1_wf<U + 1>(g, f); SB();
  }
}
template <int M, bool EARLY>
DEV void k1_stage(const K1G& g, u32x4 (&xf)[7][4], u32x4 (&wf)[4], f32x4 (&acc)[2][2], f32x4& cs, f32x4& rsum, f32x4& gram, u32x4 ones STAMP_ARG) {
  asm volatile("s_waitcnt vmcnt(%0) lgkmcnt(8)\n\ts_barrier" ::"i"(k1_issued(M) - k1_need(M)) : "memory");
  STAMP(16 + M);
  if constexpr (M < 6) k1_xr<M + 1>(g, xf);
  if constexpr (EARLY) k1_ops<k1_issued(M), k1_issued(M + 1)>(g);
  SB();
  rsum = mfma_bf(ones, xf[M][0], rsum);
  gram = mfma_bf(xf[M][0], xf[M][0], gram);
  rsum = mfma_bf(ones, xf[M][2], rsum);
  gram = mfma_bf(xf[M][2], xf[M][2], gram);
  k1_main<M, M>(g, xf, wf, acc, cs, ones);
  if constexpr (!EARLY) k1_ops<k1_issued(M), k1_issued(M + 1)>(g);
  if constexpr (M < 6) k1_stage<M + 1, EARLY>(g, xf, wf, acc, cs, rsum, gram, ones STAMP_PASS);
}
template <int M> DEV void k1_z1loop(const K1G& g, u32x4 (&xf)[7][4], u32x4 (&wf)[4], f32x4 (&acc)[2][2], f32x4& cs, u32x4 ones) {
  k1_main<M, M + 7>(g, xf, wf, acc, cs, ones);
  if constexpr (M < 6) k1_z1loop<M + 1>(g, xf, wf, acc, cs, ones);
}
DEV void k1_act(f32x4 (&acc)[2][2], const char* csb, int sw, const f32x2 (&ln1)[2]) {
#pragma unroll
  for (int p = 0; p < 2; p++) {
    const f32x4 c = *(const f32x4*)(csb + (p ^ sw) * 64) + *(const f32x4*)(csb + 256 + (p ^ sw) * 64);
#pragma unroll
    for (int t = 0; t < 2; t++) {
      f32x2 a = (acc[t][p].xy - c.xy * ln1[t].x) * ln1[t].y;
      f32x2 b = (acc[t][p].zw - c.zw * ln1[t].x) * ln1[t].y;
      a = gelu2(a); b = gelu2(b);
      acc[t][p] = (f32x4){a.x, a.y, b.x, b.y};
    }
  }
}
DEV float k1_z2act(f32x4 (&acc2)[2][2], const char* csb, int sw, const f32x2 (&ln1)[2], int q, int ch) {
  k1_act(acc2, csb, sw, ln1);
  float st[2][2];
#pragma unroll
  for (int t = 0; t < 2; t++) {
    float s = 0.f, s2 = 0.f;
#pragma unroll
    for (int p = 0; p < 2; p++)
#pragma unroll
      for (int j = 0; j < 4; j++) { s += acc2[t][p][j]; s2 = fmaf(acc2[t][p][j], acc2[t][p][j], s2); }
    st[t][0] = xsum32(xsum16(s));
    st[t][1] = xsum32(xsum16(s2));
  }
  const bool t1 = ((q ^ ch) & 1) != 0;
  return (q & 2) ? (t1 ? st[1][1] : st[0][1]) : (t1 ? st[1][0] : st[0][0]);
}
DEV void k1_body(char* smem, const u16* x, const u16* win, const u16* wsp, const u16* wout, char* ws) {
  STAMP_DECL; STAMP(0);
  const int b = blockIdx.x, xcd = b & 7, idx = b >> 3;
  const int w = __builtin_amdgcn_readfirstlane(threadIdx.x >> 6), l = threadIdx.x & 63;
  if (b >= 224) {
    const int j = (idx - 28) * 8 + w;
    const char* p = (const char*)wsp + ((size_t)xcd * 1024 + 224 * K1_PF) * 1024;
    constexpr int REST = 1024 - 224 * K1_PF;
#pragma unroll 1
    for (int i = j; i < REST; i += 32) dma16(p + (size_t)i * 1024, l * 16, lds_addr(smem) + w * 1024);
    const char* po = (const char*)wout + (size_t)(xcd * 98) * 1024;
#pragma unroll
    for (int k = 0; k < 4; k++) { int c = j + 32 * k; c = c < 98 ? c : 97; dma16(po + c * 1024, l * 16, lds_addr(smem) + w * 1024); }
    STAMP(1); STAMP_FLUSH(0);
    return;
  }
  const int panel = (xcd >> 1) * 4 + idx / 7, sib = (xcd & 1) * 7 + idx % 7;
  const int rg = w & 3, ch = w >> 2, sw = rg & 1, hi = rg >> 1;
  const int rho = l & 15, q = l >> 4, qq = (l >> 2) & 3, pp = l & 3;
  const lptr lds = (lptr)smem;
  K1G g;
  g.win = (const char*)win + (size_t)(8 * w) * (FF * 2) + sib * 128;
  g.xb = (const char*)x + (size_t)(panel * 128 + rg * 32 + ch * 16) * (DM * 2);
  g.pf = (const char*)wsp + ((size_t)xcd * 1024 + (idx * 8 + w) * K1_PF) * 1024;
  g.xoff[0] = (u32)((l >> 3) * (DM * 2) + (((l & 7) ^ (l >> 4)) * 16));
  g.xoff[1] = (u32)((8 + (l >> 3)) * (DM * 2) + (((l & 7) ^ ((l >> 4) | 4)) * 16));
  g.xdst = lds_addr(smem) + K1_X + rg * 4096 + ch * 2048;
  g.woff = (u32)((l >> 3) * (FF * 2) + (((l & 7) ^ ((((l >> 4) & 1) | ((w & 1) << 1)) << 1)) * 16));
  g.wdst = lds_addr(smem) + w * 1024;
  g.l16 = l * 16;
  g.dump = lds_addr(smem) + K1_DUMP;
  g.m0 = sib >> 1;
  u32x4 xf[7][4], wf[4];
  k1_ops<0, K1_PRO>(g);
  const int grp = panel * 4 + rg;
  u32 tag;
  asm volatile("s_load_dword %0, %1, 0x0\n\ts_waitcnt lgkmcnt(0)" : "=s"(tag) : "s"(ws + WS_EP + grp * 256) : "memory");
  tag += 1u;
  if (threadIdx.x < 4) *(u32*)(smem + K1_FLG + threadIdx.x * 4) = 0u;

#pragma unroll
  for (int i = 0; i < 4; i++)
    g.str[i] = pinned(lds + K1_X + rg * 4096 + ((i & 1) ^ ch) * 2048 + rho * 128 + (((4 * ((i >> 1) ^ hi) + q) ^ (rho >> 1)) * 16));
  {
    const int sg = (qq >> 1) | ((q & 1) << 1);
#pragma unroll
    for (int f = 0; f < 4; f++) g.wp[f] = lds + (((f >> 1) ^ hi) * 4096) + (8 * q + qq) * 128 + (((2 * ch + ((f & 1) ^ sw)) ^ sg) * 32) + 8 * pp;
  }
  const u32x4 ones = {0x3f803f80u, 0x3f803f80u, 0x3f803f80u, 0x3f803f80u};
  f32x4 acc2[2][2] = {}, cs = {}, rsum = {}, gram = {};
  STAMP(1);
  wait_vm<K1_PRO - 3>();
  bar();
  STAMP(2);
  k1_xr<0>(g, xf);
#pragma unroll
  for (int f = 0; f < 4; f++) wf[f] = k1_wf<0>(g, f);
  if (ch == 0) k1_stage<0, true>(g, xf, wf, acc2, cs, rsum, gram, ones STAMP_PASS);
  else k1_stage<0, false>(g, xf, wf, acc2, cs, rsum, gram, ones STAMP_PASS);
  k1_ops<k1_issued(7), K1_NOPS>(g);
  char* const csw = smem + K1_CS + hi * 256 + (2 * ch + sw) * 64 + q * 16;
  const char* const csr = smem + K1_CS + ch * 128 + q * 16;
  if (rho == 0) *(f32x4*)(csw) = cs;
  STAMP(3);
  f32x2 ln1[2];
  {
    float d = (rho & 2) ? ((rho & 1) ? gram.w : gram.z) : ((rho & 1) ? gram.y : gram.x);
    d = ((rho >> 2) == q) ? d : 0.f;
    d = xsum32(xsum16(d));
    ln1[0] = ln_scalars(rsum.x, d, 1.f / DM);
    if (q == 0) *(f32x2*)(smem + K1_LN + w * 128 + rho * 8) = ln1[0];
  }
  {
    const u32x4 va = xf[0][0], vb = xf[0][2];
    const u32 msk = ((sib ^ hi) & 1) ? 0xffffffffu : 0u;
    const u32x4 v = (va & ~msk) | (vb & msk);
    u16* ub = (u16*)(smem + K1_UT + (8 * q) * 272 + (rg * 32 + ch * 16 + rho) * 2);
#pragma unroll
    for (int j = 0; j < 4; j++) {
      float a = rbf(rbf(bflo(v[j]) - ln1[0].x) * ln1[0].y), bb = rbf(rbf(bfhi(v[j]) - ln1[0].x) * ln1[0].y);
      u32 o = pk_f16(a, bb);
      ub[(2 * j) * 136] = (u16)o;
      ub[(2 * j + 1) * 136] = (u16)(o >> 16);
    }
  }
  wait_vm<K1_PF>();
  bar();
  STAMP(4);
  ln1[1] = *(const f32x2*)(smem + K1_LN + (w ^ 4) * 128 + rho * 8);
  {
    u32x4 o = *(const u32x4*)(smem + K1_UT + (w * 4 + q) * 272 + rho * 16);
    gstore16(ws, (u32)(WS_UT + (sib * 32 + w * 4 + q) * (SEQ * 2) + panel * 256 + rho * 16), o);
  }
  GAS u64* gr = (GAS u64*)(ws + WS_GR) + (size_t)grp * 14 * 64;
  f32x4 acc1[2][2] = {};
  cs = (f32x4){0.f, 0.f, 0.f, 0.f};
  volatile LDSAS u32* flg = (volatile LDSAS u32*)(lds + K1_FLG + rg * 4);
  volatile LDSAS float* pb = (volatile LDSAS float*)(lds + K1_PB + rg * 256 + l * 4);
  if (ch == 0) {
    const float v = k1_z2act(acc2, csr, sw, ln1, q, ch);
    *pb = v;
    *flg = 1u;
    SB();
    k1_z1loop<0>(g, xf, wf, acc1, cs, ones);
    if (rho == 0) *(f32x4*)(csw + 512) = cs;
  } else {
    k1_z1loop<0>(g, xf, wf, acc1, cs, ones);
    if (rho == 0) *(f32x4*)(csw + 512) = cs;
    SB();
    float v = k1_z2act(acc2, csr, sw, ln1, q, ch);
    while (__builtin_amdgcn_readfirstlane(*flg) == 0u) __builtin_amdgcn_s_sleep(1);
    v += *pb;
    __hip_atomic_store(gr + sib * 64 + l, ((u64)tag << 32) | __float_as_uint(v), __ATOMIC_RELAXED, __HIP_MEMORY_SCOPE_AGENT);
  }
  STAMP(5);
  bar();
  STAMP(6);
  k1_act(acc1, csr + 512, sw, ln1);
  u32x4 z1o[2];
#pragma unroll
  for (int t = 0; t < 2; t++) {
    u32x2 a = {pk_bf16(acc1[t][0].x, acc1[t][0].y), pk_bf16(acc1[t][0].z, acc1[t][0].w)};
    u32x2 bb = {pk_bf16(acc1[t][1].x, acc1[t][1].y), pk_bf16(acc1[t][1].z, acc1[t][1].w)};
    z1o[t] = sw ? (u32x4){bb.x, bb.y, a.x, a.y} : (u32x4){a.x, a.y, bb.x, bb.y};
    pin(z1o[t]);
  }
  STAMP(7);
  float tot;
  for (u32 spins = 0;; spins++) {
    u64 gv[7];
#pragma unroll
    for (int i = 0; i < 7; i++) gv[i] = __hip_atomic_load(gr + (ch * 7 + i) * 64 + l, __ATOMIC_RELAXED, __HIP_MEMORY_SCOPE_AGENT);
    bool ok = true; tot = 0.f;
#pragma unroll
    for (int i = 0; i < 7; i++) { ok &= (u32)(gv[i] >> 32) == tag; tot += __uint_as_float((u32)gv[i]); }
    if (__all(ok) || spins > (1u << 22)) break;
    __builtin_amdgcn_s_sleep(1);
  }
  *(float*)(smem + K1_TOT + w * 256 + l * 4) = tot;
  STAMP(8);
#pragma unroll
  for (int t = 0; t < 2; t++) gstore16(ws, (u32)(WS_Z1F + ((panel * 8 + rg * 2 + (t ^ ch)) * 28 + sib * 2 + ch) * 1024 + l * 16), z1o[t]);
  bar();
  if (sib == 0 && ch == 0 && l == 0) __hip_atomic_store((GAS u32*)(ws + WS_EP + grp * 256), tag, __ATOMIC_RELAXED, __HIP_MEMORY_SCOPE_AGENT);
  f32x2 ln2[2];
  {
    const float other = *(const float*)(smem + K1_TOT + (w ^ 4) * 256 + l * 4);
    tot = ch ? other + tot : tot + other;
    auto a = __builtin_amdgcn_permlane16_swap(__float_as_uint(tot), __float_as_uint(tot), false, false);
    const u32 a0 = ch ? a[1] : a[0], a1 = ch ? a[0] : a[1];
    auto b0 = __builtin_amdgcn_permlane32_swap(a0, a0, false, false);
    auto b1 = __builtin_amdgcn_permlane32_swap(a1, a1, false, false);
    ln2[0] = ln_scalars(__uint_as_float(b0[0]), __uint_as_float(b0[1]), 1.f / FH);
    ln2[1] = ln_scalars(__uint_as_float(b1[0]), __uint_as_float(b1[1]), 1.f / FH);
  }
  {
    char* const stg = smem + K1_X + w * 4096;
#pragma unroll
    for (int t = 0; t < 2; t++)
#pragma unroll
      for (int p = 0; p < 2; p++) {
        f32x2 a = (acc2[t][p].xy - ln2[t].x) * ln2[t].y, bb = (acc2[t][p].zw - ln2[t].x) * ln2[t].y;
        *(u32x2*)(stg + (16 * (t ^ ch) + rho) * 72 + p * 32 + q * 8) = (u32x2){pk_bf16(a.x, a.y), pk_bf16(bb.x, bb.y)};
      }
    const int kap = panel * 4 + rg;
#pragma unroll
    for (int p = 0; p < 2; p++) {
      const char* rp = stg + (8 * q + qq) * 72 + p * 32 + pp * 8;
      u32x4 o = tr_frag(rp, rp + 4 * 72);
      const int ft = sib * 4 + 2 * ch + (p ^ sw);
      Z2STORE(ws, (u32)(WS_Z2F + (((ft / 7) * 64 + kap) * 7 + ft % 7) * 1024 + l * 16), o);
    }
  }
  STAMP(9); STAMP_FLUSH(0);
}
__global__ NOPK __launch_bounds__(512, 2) void k1(const u16* x, const u16* win, const u16* wsp, const u16* wout, char* ws) {
  __shared__ __attribute__((aligned(16))) char smem[K1_LDS];
  k1_body(smem, x, win, wsp, wout, ws);
}

constexpr int K2_EPI = 5;
#define K2_PF 7
constexpr int k2_step_ops(int s) { return (((s & 1) && (s + 3) / 2 <= 7) ? 8 : 0) + (s + 3 <= 15 ? 7 : 0) + (s == 13 ? K2_EPI + K2_PF : 0); }
constexpr int k2_pos_B(int i) {
  if (i == 0) return 15;
  if (i == 1) return 22;
  int n = 37;
  for (int s = 0; s + 3 <= i; s++) n += k2_step_ops(s);
  return n;
}
constexpr int k2_pos_A(int a) {
  if (a == 0) return 8;
  if (a == 1) return 30;
  int n = 37;
  for (int s = 0; s < 2 * a - 3; s++) n += k2_step_ops(s);
  return n + 8;
}
constexpr int k2_issued_before(int i) { int n = 37; for (int s = 0; s < i; s++) n += k2_step_ops(s); return n; }
constexpr int k2_wait(int i) {
  int need = k2_pos_B(i);
  if ((i & 1) && (i + 1) / 2 <= 7) { int a = k2_pos_A((i + 1) / 2); need = a > need ? a : need; }
  return k2_issued_before(i) - need;
}
#define K2_MFMA mfma_bf
struct K2G { const char* wsp; const char* z2; const char* z1; const char* bsp; const char* pf; u32 aoff[2]; u32 boff; u32 adst; u32 dump; int w; int rot; };
template <int SS> DEV void k2_issue_a(const K2G& g) {
  const char* sb = g.wsp + ((SS + g.rot) & 7) * 128;
#pragma unroll
  for (int i = 0; i < 8; i++) dma16(sb + (size_t)i * 8 * 4096, g.aoff[i & 1], g.adst + (SS & 1) * 8192 + ((i ^ (2 * g.w)) * 1024));
}
template <int I> DEV const char* k2_bbase(const K2G& g) { return g.z2 + ((I + 2 * g.rot) & 15) * 7168; }
template <int F> DEV void k2_issue_b1(const K2G& g, const char* sb, u32x4& d) { gload16<(F - 3) * 1024>(d, sb, g.boff); }
template <int I> DEV void k2_issue_b(const K2G& g, u32x4 (&bf)[7]) {
  const char* sb = k2_bbase<I>(g);
  k2_issue_b1<0>(g, sb, bf[0]); k2_issue_b1<1>(g, sb, bf[1]); k2_issue_b1<2>(g, sb, bf[2]); k2_issue_b1<3>(g, sb, bf[3]);
  k2_issue_b1<4>(g, sb, bf[4]); k2_issue_b1<5>(g, sb, bf[5]); k2_issue_b1<6>(g, sb, bf[6]);
}
template <int I> DEV u32x4 k2_read_a(const char* ap0, const char* ap1, int t) {
  return *(const u32x4*)(((I & 1) ? ap1 : ap0) + ((I >> 1) & 1) * 8192 + t * 2048);
}
template <int I, int F> DEV void k2_last(const K2G& g, const char* sb, u32x4 (&bf)[7], const u32x4& a, f32x4 (&acc)[7]) {
  acc[F] = K2_MFMA(bf[F], a, acc[F]);
  if constexpr (I + 3 <= 15) { __builtin_amdgcn_sched_barrier(0); k2_issue_b1<F>(g, sb, bf[F]); __builtin_amdgcn_sched_barrier(0); }
  if constexpr (F < 6) k2_last<I, F + 1>(g, sb, bf, a, acc);
}
template <int I>
DEV void k2_step(const K2G& g, u32x4 (&bf)[3][7], u32x4 (&af)[4], const char* ap0, const char* ap1, f32x4 (&acc)[4][7], u32x4 (&ep)[4], u32& bw) {
  wait_vm<k2_wait(I)>();
#pragma unroll
  for (int f = 0; f < 7; f++) pin(bf[I % 3][f]);
  if constexpr ((I & 1) && (I + 3) / 2 <= 7) {
    asm volatile("s_waitcnt lgkmcnt(0)" ::: "memory");
    k2_issue_a<(I + 3) / 2>(g);
  }
#pragma unroll
  for (int t = 0; t < 3; t++) {
#pragma unroll
    for (int f = 0; f < 7; f++) acc[t][f] = K2_MFMA(bf[I % 3][f], af[t], acc[t][f]);
    __builtin_amdgcn_sched_barrier(0);
    if constexpr (I < 15) af[t] = k2_read_a<I + 1>(ap0, ap1, t);
    __builtin_amdgcn_sched_barrier(0);
  }
  k2_last<I, 0>(g, k2_bbase<I + 3>(g), bf[I % 3], af[3], acc[3]);
  if constexpr (I < 15) af[3] = k2_read_a<I + 1>(ap0, ap1, 3);
  if constexpr (I == 13) {
    gload16<0>(ep[0], g.z1, g.boff); gload16<1024>(ep[1], g.z1, g.boff); gload16<2048>(ep[2], g.z1, g.boff); gload16<3072>(ep[3], g.z1, g.boff);
    gload4(bw, g.bsp, (g.boff >> 3) & 28u);
#pragma unroll
    for (int i = 0; i < K2_PF; i++) dma16(g.pf + i * 1024, g.boff, g.dump);
  }
  if constexpr (I < 15) k2_step<I + 1>(g, bf, af, ap0, ap1, acc, ep, bw);
}

DEV void k2_gemm(char* smem, int b, const u16* wsp, const u16* bsp, const u16* wout, char* ws) {
  STAMP_DECL; STAMP(0);
  const int xcd = b & 7, idx = b >> 3;
  const int tp = (xcd >> 1) * 8 + (idx >> 2), fp = (xcd & 1) * 4 + (idx & 3);
  const int w = __builtin_amdgcn_readfirstlane(threadIdx.x >> 6), l = threadIdx.x & 63;
  const int rho = l & 15, q = l >> 4;
  const int tt = tp * 4 + w;
  char* const reg = smem + w * 16384;
  K2G g;
  g.wsp = (const char*)wsp + (size_t)tp * 64 * 4096 + w * 1024;
  g.z2 = ws + WS_Z2F + ((size_t)(fp * 64 + w * 16) * 7 + 3) * 1024;
  g.z1 = ws + WS_Z1F + ((size_t)tt * 28 + ((fp * 7) >> 1)) * 1024;
  g.bsp = (const char*)bsp + tt * 32;
  {
    int pc = (idx * 4 + w) * 7;
    pc = pc > 784 - 7 ? 784 - 7 : pc;
    g.pf = (const char*)wout + pc * 1024;
  }
  g.aoff[0] = (u32)((l >> 3) * 4096 + (((l & 7) ^ (l >> 4)) * 16));
  g.aoff[1] = (u32)((l >> 3) * 4096 + (((l & 7) ^ ((l >> 4) | 4)) * 16));
  g.boff = l * 16;
  g.adst = lds_addr(reg);
  g.dump = lds_addr(smem) + 65536 + w * 1024;
  g.w = w;
  g.rot = 0;
  u32x4 bf[3][7], af[4], ep[4];
  u32 bw;
  k2_issue_a<0>(g); k2_issue_b<0>(g, bf[0]); k2_issue_b<1>(g, bf[1]); k2_issue_a<1>(g); k2_issue_b<2>(g, bf[2]);
  const char* ap0 = reg + rho * 128 + (((0 + q) ^ (rho >> 1)) * 16);
  const char* ap1 = reg + rho * 128 + (((4 + q) ^ (rho >> 1)) * 16);
  f32x4 acc[4][7] = {};
  STAMP(1);
  wait_vm<29>();
  STAMP(2);
#pragma unroll
  for (int t = 0; t < 4; t++) af[t] = k2_read_a<0>(ap0, ap1, t);
  k2_step<0>(g, bf, af, ap0, ap1, acc, ep, bw);
  STAMP(3);
#pragma unroll
  for (int f = 0; f < 7; f++) {
    *(f32x4*)(reg + f * 1024 + l * 16) = acc[1][f];
    *(f32x4*)(reg + (7 + f) * 1024 + l * 16) = acc[3][f];
  }
  bar();
  STAMP(4);
  char* const in1 = smem + (w ^ 1) * 16384;
#pragma unroll
  for (int f = 0; f < 7; f++) acc[2][f] += *(const f32x4*)(in1 + (7 + f) * 1024 + l * 16);
#pragma unroll
  for (int f = 0; f < 7; f++) *(f32x4*)(in1 + (7 + f) * 1024 + l * 16) = acc[2][f];
#pragma unroll
  for (int f = 0; f < 7; f++) acc[0][f] += *(const f32x4*)(in1 + f * 1024 + l * 16);
  bar();
  char* const in2 = smem + (w ^ 3) * 16384;
#pragma unroll
  for (int i = 0; i < 1; i++) wait_vm<K2_PF>();
#pragma unroll
  for (int i = 0; i < 4; i++) pin(ep[i]);
  asm volatile("" : "+v"(bw));
  const float bs = (rho & 1) ? bfhi(bw) : bflo(bw);
  STAMP(5);
  const int odd = fp & 1;
  u32x2 hv[8];
#pragma unroll
  for (int f = 0; f < 7; f++) {
    f32x4 v = acc[0][f] + *(const f32x4*)(in2 + (7 + f) * 1024 + l * 16);
    v = v + bs;
    const u32x4 ze = ep[f >> 1], zo = ep[(f + 1) >> 1];
    const u32 za = odd ? ((f & 1) ? zo.x : zo.z) : ((f & 1) ? ze.z : ze.x);
    const u32 zb = odd ? ((f & 1) ? zo.y : zo.w) : ((f & 1) ? ze.w : ze.y);
    hv[f] = (u32x2){pk_bf16(v.x * bflo(za), v.y * bfhi(za)), pk_bf16(v.z * bflo(zb), v.w * bfhi(zb))};
  }
  const u32 hb = (u32)(WS_HF + (tt * 28 + ((fp * 7) >> 1)) * 1024 + l * 16);
  if (odd) {
    gstore8(ws, hb + 8, hv[0]);
#pragma unroll
    for (int i = 0; i < 3; i++) gstore16(ws, hb + (i + 1) * 1024, (u32x4){hv[1 + 2 * i].x, hv[1 + 2 * i].y, hv[2 + 2 * i].x, hv[2 + 2 * i].y});
  } else {
#pragma unroll
    for (int i = 0; i < 3; i++) gstore16(ws, hb + i * 1024, (u32x4){hv[2 * i].x, hv[2 * i].y, hv[1 + 2 * i].x, hv[1 + 2 * i].y});
    gstore8(ws, hb + 3 * 1024, hv[6]);
  }
  STAMP(6); STAMP_FLUSH(1);
}

constexpr int KG_ROW = 272, KG_WAVE = 53 * KG_ROW;
constexpr int CV_CS = 4160, CV_T = 8 * CV_CS, CV_U = 2 * CV_T, CV_CMP = CV_U + 2 * 4608;
DEV f32x2 cexp_pow2(float gl, float fh, float fl, float p) {
  float m = __builtin_amdgcn_exp2f(gl * p);
  float r = __builtin_amdgcn_fractf(fh * p) + fl * p;
  return (f32x2){m * __builtin_amdgcn_cosf(r), m * __builtin_amdgcn_sinf(r)};
}
DEV void k2_ssm(char* smem, int cb, const u16* lar, const u16* aim, const u16* cre, const u16* cim, const u16* ldt, const u16* dsk, char* ws) {
  STAMP_DECL; STAMP(0);
#define SSM_PRIO 2
  __builtin_amdgcn_s_setprio(SSM_PRIO);
  const int w = __builtin_amdgcn_readfirstlane(threadIdx.x >> 6), l = threadIdx.x & 63;
  const int ch = w >> 1, d = cb * 2 + ch, hw = w & 1, t = hw * 64 + l;
  const int rho = l & 15, q = l >> 4;
  const size_t o = (size_t)d * NMODE;
  const u32 vla = *(const u32*)(lar + o + hw * 128 + 2 * l), vai = *(const u32*)(aim + o + hw * 128 + 2 * l);
  const u32 vcr = *(const u32*)(cre + o + hw * 128 + 2 * l), vci = *(const u32*)(cim + o + hw * 128 + 2 * l);
  const u32 ocr = *(const u32*)(cre + o + (hw ^ 1) * 128 + 2 * l), oci = *(const u32*)(cim + o + (hw ^ 1) * 128 + 2 * l);
  const float dt = rbf(expf(bf2f(ldt[d])));
  const float dk = bf2f(dsk[d]);
  char* const T = smem + ch * CV_T;
  char* const U = smem + CV_U + ch * 4608;
  u16* const cmp = (u16*)(smem + CV_CMP + ch * 1408);
  {
    const char* ut = ws + WS_UT + (size_t)d * (SEQ * 2);
    const u32x4 u0 = *(const u32x4*)(ut + t * 16), u1 = *(const u32x4*)(ut + 2048 + t * 16);
    if (t < 32) *(u32x4*)(U + t * 16) = (u32x4){0u, 0u, 0u, 0u};
    *(u32x4*)(U + 512 + t * 16) = u0;
    *(u32x4*)(U + 2560 + t * 16) = u1;
  }
  float S = fabsf(bflo(vcr)) + fabsf(bfhi(vcr)) + fabsf(bflo(vci)) + fabsf(bfhi(vci));
  float S2 = fabsf(bflo(ocr)) + fabsf(bfhi(ocr)) + fabsf(bflo(oci)) + fabsf(bfhi(oci));
  S = hw ? S2 + S : S + S2;
#pragma unroll
  for (int m = 1; m < 16; m <<= 1) S += __shfl_xor(S, m);
  S = xsum32(xsum16(S));
  int e = (int)((__float_as_uint(S) >> 23) & 255u) - 125;
  e = e < -100 ? -100 : (e > 100 ? 100 : e);
  e = __builtin_amdgcn_readfirstlane(e);
  const float sc = __uint_as_float((u32)(127 + 15 - e) << 23);
  const float ks = dt * __uint_as_float((u32)(127 + e - 14) << 23);
  STAMP(1);
  char* tab = smem + w * KG_WAVE;
  f32x16 kacc = {};
#pragma unroll 1
  for (int rnd = 0; rnd < 2; rnd++) {
    const float la = rnd ? bfhi(vla) : bflo(vla), ai = rnd ? bfhi(vai) : bflo(vai);
    const f32x2 c0 = {(rnd ? bfhi(vcr) : bflo(vcr)) * sc, (rnd ? bfhi(vci) : bflo(vci)) * sc};
    const float zr = -rbf(expf(la)) * dt, zi = ai * dt;
    const float gl = zr * 1.44269504089f;
    const float fh = zi * 0.15915494309f;
    const float fl = fmaf(zi, 0.15915494309f, -fh) + zi * 6.4206382e-9f;
    u32* tw = (u32*)(tab + l * 4);
#define TW(row, v) tw[(row) * (KG_ROW / 4)] = (v)
    const f32x2 e1 = cexp_pow2(gl, fh, fl, 1.f), e2 = csqr(e1), e4 = csqr(e2);
    const f32x2 e8 = cexp_pow2(gl, fh, fl, 8.f), e16 = csqr(e8), e32 = csqr(e16);
    const f32x2 e64 = cexp_pow2(gl, fh, fl, 64.f), e128 = csqr(e64);
    const f32x2 e256 = cexp_pow2(gl, fh, fl, 256.f), e512 = csqr(e256);
    const f32x2 e1024 = cexp_pow2(gl, fh, fl, 1024.f), e2048 = csqr(e1024);
    {
      f32x2 tb[16];
      tb[0] = (f32x2){32768.f, 0.f};
      tb[1] = e8 * 32768.f;
      tb[2] = e16 * 32768.f; tb[3] = cmul(tb[1], e16);
#pragma unroll
      for (int i = 0; i < 4; i++) tb[4 + i] = cmul(tb[i], e32);
#pragma unroll
      for (int i = 0; i < 8; i++) tb[8 + i] = cmul(tb[i], e64);
#pragma unroll
      for (int i = 0; i < 16; i++) { TW(i, pk_f16(tb[i].x, tb[i].y)); f32x2 u = cmul(tb[i], e128); TW(16 + i, pk_f16(u.x, u.y)); }
    }
    {
      f32x2 a[21];
      a[0] = c0; a[1] = cmul(c0, e1); a[2] = cmul(c0, e2); a[3] = cmul(a[1], e2);
#pragma unroll
      for (int i = 0; i < 4; i++) a[4 + i] = cmul(a[i], e4);
#pragma unroll
      for (int i = 0; i < 4; i++) a[8 + i] = cmul(a[2 * i], e256);
      a[12] = cmul(a[0], e512); a[13] = cmul(a[4], e512); a[14] = cmul(a[8], e512); a[15] = cmul(a[10], e512);
      a[16] = cmul(a[0], e1024); a[17] = cmul(a[8], e1024); a[18] = cmul(a[12], e1024); a[19] = cmul(a[14], e1024);
      a[20] = cmul(a[0], e2048);
#pragma unroll
      for (int i = 0; i < 21; i++) TW(32 + i, pk_f16(a[i].x, -a[i].y));
    }
#undef TW
    const int r = l & 31, h = l >> 5;
    const char* pa = tab + (32 + (r < 21 ? r : 20)) * KG_ROW + h * 16;
    const char* pb = tab + r * KG_ROW + h * 16;
#pragma unroll
    for (int kk = 0; kk < 8; kk++) kacc = mfma32_h(*(const u32x4*)(pa + kk * 32), *(const u32x4*)(pb + kk * 32), kacc);
  }
  STAMP(2);
  float* part = (float*)(smem + (w | 1) * KG_WAVE);
  if (hw) {
#pragma unroll
    for (int i = 0; i < 12; i++) part[i * 64 + l] = kacc[i];
  }
  __syncthreads();
  if (!hw) {
    const int col = l & 31, h = l >> 5;
#pragma unroll
    for (int i = 0; i < 12; i++) {
      float v = (kacc[i] + part[i * 64 + l]) * (1.f / 32768.f);
      const int j = i & 3, gq = i >> 2;
      int id;
      if (gq == 0) id = j + 4 * h + 8 * col;
      else if (gq == 1) id = h ? 384 + 64 * (j >> 1) + (j & 1) + 2 * col : 256 + j + 4 * col;
      else id = h ? ((j == 0 && col == 0) ? 640 : 700) : 512 + 32 * j + col;
      cmp[id] = __builtin_bit_cast(u16, (_Float16)v);
    }
  }
  __syncthreads();
  u32x4 ra[2];
#pragma unroll
  for (int c = 0; c < 2; c++) {
    const int x0 = (c * 128 + t) * 8;
#pragma unroll
    for (int j = 0; j < 4; j++) {
      const int l0 = 2047 - (x0 + 2 * j), l1 = l0 - 1;
      const u32 bits = pk_bf16((float)l0, (float)l1);
      int i0 = (int)(bits & 0xffffu) - 0x4280, i1 = (int)(bits >> 16) - 0x4280;
      if (c == 1) { i0 = l0 < 128 ? l0 : i0; i1 = l1 < 128 ? l1 : i1; }
      ra[c][j] = (u32)cmp[i0] | ((u32)cmp[i1] << 16);
    }
    *(u32x4*)(T + x0 * 2) = ra[c];
  }
  if (t < 8) *(u32x4*)(T + t * CV_CS + 4096) = (u32x4){0u, 0u, 0u, 0u};
  __syncthreads();
#pragma unroll
  for (int c = 0; c < 2; c++) {
    const int y = c * 128 + t;
    const u32x4 rb = *(const u32x4*)(T + y * 16 + 16);
    u32 dw[8] = {ra[c].x, ra[c].y, ra[c].z, ra[c].w, rb.x, rb.y, rb.z, rb.w};
    u32 od[7];
#pragma unroll
    for (int i = 0; i < 7; i++) od[i] = __builtin_amdgcn_alignbit(dw[i + 1], dw[i], 16);
#pragma unroll
    for (int k = 0; k < 4; k++) {
      if (k) *(u32x4*)(T + (2 * k) * CV_CS + y * 16) = (u32x4){dw[k], dw[k + 1], dw[k + 2], dw[k + 3]};
      *(u32x4*)(T + (2 * k + 1) * CV_CS + y * 16) = (u32x4){od[k], od[k + 1], od[k + 2], od[k + 3]};
    }
  }
  __syncthreads();
  STAMP(3);
  const char* pa = T + (7 - (rho & 7)) * CV_CS + (255 - (rho >> 3) + q) * 16;
  const char* pb = U + 512 + rho * 32 + q * 16;
  f32x4 acc[8] = {};
#pragma unroll 1
  for (int ci = 0; ci < 4; ci++) {
    const int r = 1 + 2 * (hw * 4 + ci);
    u32x4 A[8], B[8];
#pragma unroll
    for (int i = 0; i < 8; i++) {
      A[i] = *(const u32x4*)(pa - 32 * r - 512 * i);
      B[i] = *(const u32x4*)(pb - 32 * r + 512 * i);
    }
#pragma unroll
    for (int s = 0; s < 8; s++)
#pragma unroll
      for (int a = 0; a <= s; a++) acc[s] = mfma_h(A[a], B[s - a], acc[s]);
  }
  STAMP(4);
  __syncthreads();
  {
    char* box = T + hw * 4096;
    if (hw) {
#pragma unroll
      for (int i = 0; i < 4; i++) *(f32x4*)(box + i * 1024 + l * 16) = acc[i];
#pragma unroll
      for (int i = 0; i < 4; i++) acc[i] = acc[4 + i];
    } else {
#pragma unroll
      for (int i = 0; i < 4; i++) *(f32x4*)(box + i * 1024 + l * 16) = acc[4 + i];
    }
  }
  __syncthreads();
  {
    const char* box = T + (hw ^ 1) * 4096;
    u32x2 yv[4];
#pragma unroll
    for (int i = 0; i < 4; i++) {
      const int pos = 256 * (4 * hw + i) + 16 * rho + 4 * q;
      f32x4 v = (acc[i] + *(const f32x4*)(box + i * 1024 + l * 16)) * ks;
      u32x2 ub = *(const u32x2*)(U + 512 + pos * 2);
      f16x2 u0 = as_h2(ub.x), u1 = as_h2(ub.y);
      float y0 = rbf(v.x) + rbf((float)u0.x * dk), y1 = rbf(v.y) + rbf((float)u0.y * dk);
      float y2 = rbf(v.z) + rbf((float)u1.x * dk), y3 = rbf(v.w) + rbf((float)u1.y * dk);
      yv[i] = (u32x2){pk_bf16(y0, y1), pk_bf16(y2, y3)};
    }
#pragma unroll
    for (int i = 0; i < 4; i += 2) {
      auto sx = __builtin_amdgcn_permlane16_swap(yv[i].x, yv[i + 1].x, false, false);
      auto sy = __builtin_amdgcn_permlane16_swap(yv[i].y, yv[i + 1].y, false, false);
      const u32 off = (u32)(512 * (4 * hw + i + (q & 1)) + 32 * rho + 16 * (q >> 1));
      gstore16(ws, (u32)(WS_YT + d * (SEQ * 2)) + off, (u32x4){sx[0], sy[0], sx[1], sy[1]});
    }
  }
  STAMP(5); STAMP_FLUSH(1);
}

__global__ NOPK __launch_bounds__(256, 2) void k2(const u16* wsp, const u16* bsp, const u16* wout, const u16* lar, const u16* aim, const u16* cre,
                                             const u16* cim, const u16* ldt, const u16* dsk, char* ws) {
  __shared__ __attribute__((aligned(16))) char smem[81920];
  if (blockIdx.x < 256) k2_gemm(smem, blockIdx.x, wsp, bsp, wout, ws);
  else k2_ssm(smem, blockIdx.x - 256, lar, aim, cre, cim, ldt, dsk, ws);
}

#define K3_LEAD 3
struct K3G { const char* wo; const char* hf; u32 woff; u32 hoff[4]; u32 wdst; const char* yt; u32 yoff; u32 ydst; const char* xb; u32 xo; };
template <int I> DEV void k3_issue(const K3G& g, u32x4 (&hf)[7][4], u32x4 (&xv)[2]) {
  if constexpr (I < 7) {
#pragma unroll
    for (int i = 0; i < 4; i++) dma16(g.wo + (size_t)(I * 32 + i * 8) * (DM * 2), g.woff, g.wdst + (I * 4 + i) * 1024);
#pragma unroll
    for (int t = 0; t < 4; t++) gload16<0>(hf[I][t], g.hf + I * 1024, g.hoff[t]);
  } else if constexpr (I == 7) {
    dma16(g.yt, g.yoff, g.ydst);
    dma16(g.yt + 32 * (SEQ * 2), g.yoff, g.ydst + 1024);
    gload16<0>(xv[0], g.xb, g.xo);
    gload16<0>(xv[1], g.xb + 8 * (DM * 2), g.xo);
  }
}
constexpr int k3_cnt(int units) { return units <= 7 ? 8 * units : 60; }
template <int I> DEV void k3_step(const K3G& g, u32x4 (&hf)[7][4], u32x4 (&xv)[2], const char* const (&wp)[4], f32x4 (&acc)[4][4]) {
  wait_vm<k3_cnt(I + K3_LEAD) - 8 * (I + 1)>();
#pragma unroll
  for (int t = 0; t < 4; t++) pin(hf[I][t]);
  u32x4 wf[4];
#pragma unroll
  for (int p = 0; p < 4; p++) wf[p] = tr_frag(wp[p] + I * 4096, wp[p] + I * 4096 + 2048);
  k3_issue<I + K3_LEAD>(g, hf, xv);
#pragma unroll
  for (int p = 0; p < 4; p++)
#pragma unroll
    for (int t = 0; t < 4; t++) acc[t][p] = mfma_bf(wf[p], hf[I][t], acc[t][p]);
  if constexpr (I < 6) k3_step<I + 1>(g, hf, xv, wp, acc);
}
template <int A, int B> DEV void k3_pro(const K3G& g, u32x4 (&hf)[7][4], u32x4 (&xv)[2]) {
  if constexpr (A < B) { k3_issue<A>(g, hf, xv); k3_pro<A + 1, B>(g, hf, xv); }
}
__global__ __launch_bounds__(256, 1) void k3(const u16* x, const u16* wout, u16* out, char* ws) {
  __shared__ __attribute__((aligned(16))) char smem[4 * 28672 + 8192];
  STAMP_DECL; STAMP(0);
  const int b = blockIdx.x, xcd = b & 7, idx = b >> 3;
  const int tp = xcd * 4 + idx / 7, dp = idx % 7;
  const int w = __builtin_amdgcn_readfirstlane(threadIdx.x >> 6), l = threadIdx.x & 63;
  const int rho = l & 15, q = l >> 4, qq = (l >> 2) & 3, pp = l & 3;
  char* const reg = smem + w * 28672;
  char* const Y = smem + 4 * 28672;
  K3G g;
  g.wo = (const char*)wout + (size_t)(w * 224) * (DM * 2) + dp * 128;
  g.hf = ws + WS_HF + ((size_t)tp * 4 * 28 + w * 7) * 1024;
  g.woff = (u32)((l >> 3) * (DM * 2) + (((l & 7) ^ ((l >> 4) << 1)) * 16));
#pragma unroll
  for (int t = 0; t < 4; t++) g.hoff[t] = (u32)((t ^ w) * 28 * 1024 + l * 16);
  g.wdst = lds_addr(reg);
  g.yt = ws + WS_YT + (size_t)(dp * 64) * (SEQ * 2) + tp * 128 + w * 32;
  g.yoff = (u32)((l >> 1) * (SEQ * 2) + (l & 1) * 16);
  g.ydst = lds_addr(Y) + w * 2048;
  g.xo = (u32)((l >> 3) * (DM * 2) + (l & 7) * 16);
  const size_t xb = (size_t)(tp * 64 + w * 16) * (DM * 2) + dp * 128;
  g.xb = (const char*)x + xb;
  u32x4 hf[7][4], xv[2];
  k3_pro<0, K3_LEAD>(g, hf, xv);
  const char* wp[4];
  const int sg = ((q & 1) << 1) | (qq >> 1);
#pragma unroll
  for (int p = 0; p < 4; p++) wp[p] = reg + (4 * q + qq) * 128 + ((p ^ sg) * 32) + 8 * pp;
  f32x4 acc[4][4] = {};
  STAMP(1);
  k3_step<0>(g, hf, xv, wp, acc);
  STAMP(2);
#pragma unroll
  for (int t = 1; t < 4; t++)
#pragma unroll
    for (int p = 0; p < 4; p++) *(f32x4*)(reg + t * 4352 + rho * 272 + p * 64 + q * 16) = acc[t][p];
  wait_vm<0>();
  pin(xv[0]); pin(xv[1]);
#pragma unroll
  for (int p = 0; p < 4; p++) {
    u32x2 yv = tr_read(Y + w * 2048 + (16 * p + 4 * q + qq) * 32 + 8 * pp);
    *(f32x4*)(reg + rho * 272 + p * 64 + q * 16) = acc[0][p] + (f32x4){bflo(yv.x), bfhi(yv.x), bflo(yv.y), bfhi(yv.y)};
  }
  bar();
  STAMP(3);
#pragma unroll
  for (int i = 0; i < 2; i++) {
    f32x4 a = {bflo(xv[i].x), bfhi(xv[i].x), bflo(xv[i].y), bfhi(xv[i].y)}, c = {bflo(xv[i].z), bfhi(xv[i].z), bflo(xv[i].w), bfhi(xv[i].w)};
#pragma unroll
    for (int t = 0; t < 4; t++) {
      const char* rp = smem + (w ^ t) * 28672 + t * 4352 + (8 * i + (l >> 3)) * 272 + (l & 7) * 32;
      a += *(const f32x4*)(rp); c += *(const f32x4*)(rp + 16);
    }
    u32x4 o = {pk_bf16(a.x, a.y), pk_bf16(a.z, a.w), pk_bf16(c.x, c.y), pk_bf16(c.z, c.w)};
    gstore16(out, (u32)xb + i * 8 * (DM * 2) + g.xo, o);
  }
  STAMP(4); STAMP_FLUSH(2);
}

#define K1_GRID 256
#define K2_GRID 480
extern "C" void kernel_launch(void* const* d_in, const int* in_sizes, int n_in, void* d_out, int out_size, void* d_ws, size_t ws_size,
                              hipStream_t stream) {
  const u16* x = (const u16*)d_in[0];
  char* ws = (char*)d_ws;
  k1<<<K1_GRID, 512, 0, stream>>>(x, (const u16*)d_in[8], (const u16*)d_in[9], (const u16*)d_in[11], ws);
  k2<<<K2_GRID, 256, 0, stream>>>((const u16*)d_in[9], (const u16*)d_in[10], (const u16*)d_in[11], (const u16*)d_in[1], (const u16*)d_in[2],
                              (const u16*)d_in[3], (const u16*)d_in[4], (const u16*)d_in[5], (const u16*)d_in[6], ws);
  k3<<<224, 256, 0, stream>>>(x, (const u16*)d_in[11], (u16*)d_out, ws);
}
```

```cpp
#include <hip/hip_runtime.h>
#include <stdint.h>

typedef unsigned short u16;
typedef unsigned int u32;
typedef unsigned long long u64;
typedef __attribute__((ext_vector_type(4))) u32 u32x4;
typedef __attribute__((ext_vector_type(2))) u32 u32x2;
typedef __attribute__((ext_vector_type(4))) float f32x4;
typedef __attribute__((ext_vector_type(2))) float f32x2;
typedef __attribute__((ext_vector_type(16))) float f32x16;
typedef __attribute__((ext_vector_type(8))) __bf16 bf16x8;
typedef __attribute__((ext_vector_type(2))) __bf16 bf16x2;
typedef __attribute__((ext_vector_type(8))) _Float16 f16x8;
typedef __attribute__((ext_vector_type(2))) _Float16 f16x2;
typedef __attribute__((ext_vector_type(4))) short s16x4;
#define LDSAS __attribute__((address_space(3)))
#define GAS __attribute__((address_space(1)))
#define DEV __device__ __forceinline__
#if defined(__HIP_DEVICE_COMPILE__)
#define NOPK __attribute__((target("no-packed-fp32-ops")))
#else
#define NOPK
#endif
#define SB() __builtin_amdgcn_sched_barrier(0)

constexpr int SEQ = 2048, DM = 448, NMODE = 256, FF = 1792, FH = 896;

constexpr size_t WS_Z2F = 0;
constexpr size_t WS_Z1F = 4u << 20;
constexpr size_t WS_HF  = 8u << 20;
constexpr size_t WS_UT  = 12u << 20;
constexpr size_t WS_YT  = 14u << 20;
constexpr size_t WS_GR  = 16u << 20;
constexpr size_t WS_EP  = 17u << 20;

#define STAMP(i)
#define STAMP_DECL
#define STAMP_ARG
#define STAMP_PASS
#define STAMP_FLUSH(kern)
DEV float bf2f(u32 v) { return __uint_as_float(v << 16); }
DEV float bflo(u32 v) { return __uint_as_float(v << 16); }
DEV float bfhi(u32 v) { return __uint_as_float(v & 0xffff0000u); }
DEV u32 pk_bf16(float a, float b) { bf16x2 v = {(__bf16)a, (__bf16)b}; return __builtin_bit_cast(u32, v); }
DEV u32 pk_f16(float a, float b) { f16x2 v = {(_Float16)a, (_Float16)b}; return __builtin_bit_cast(u32, v); }
DEV f16x2 as_h2(u32 v) { return __builtin_bit_cast(f16x2, v); }
DEV float rbf(float a) { return __uint_as_float(pk_bf16(a, a) << 16); }
DEV f32x4 mfma_bf(u32x4 a, u32x4 b, f32x4 c) {
  return __builtin_amdgcn_mfma_f32_16x16x32_bf16(__builtin_bit_cast(bf16x8, a), __builtin_bit_cast(bf16x8, b), c, 0, 0, 0);
}
DEV f32x4 mfma_h(u32x4 a, u32x4 b, f32x4 c) {
  return __builtin_amdgcn_mfma_f32_16x16x32_f16(__builtin_bit_cast(f16x8, a), __builtin_bit_cast(f16x8, b), c, 0, 0, 0);
}
DEV f32x16 mfma32_h(u32x4 a, u32x4 b, f32x16 c) {
  return __builtin_amdgcn_mfma_f32_32x32x16_f16(__builtin_bit_cast(f16x8, a), __builtin_bit_cast(f16x8, b), c, 0, 0, 0);
}
DEV u32x2 tr_read(const char* p) {
  s16x4 v = __builtin_amdgcn_ds_read_tr16_b64_v4i16((LDSAS s16x4*)p);
  return __builtin_bit_cast(u32x2, v);
}
DEV u32x4 tr_frag(const char* p0, const char* p1) {
  u32x2 a = tr_read(p0), b = tr_read(p1);
  return (u32x4){a.x, a.y, b.x, b.y};
}
#define LDMOD ""
#define DMAMOD ""
DEV u32 lds_addr(const void* p) { return (u32)(uintptr_t)(LDSAS const char*)p; }
DEV void dma16(const void* sbase, u32 voff, u32 ldst) {
  asm volatile("s_mov_b32 m0, %2\n\ts_nop 0\n\tglobal_load_lds_dwordx4 %0, %1 " DMAMOD ::"v"(voff), "s"(sbase), "s"(ldst) : "memory");
}
template <int OFF> DEV void gload16(u32x4& d, const void* sbase, u32 voff) {
  asm volatile("global_load_dwordx4 %0, %1, %2 offset:%3 " LDMOD : "=v"(d) : "v"(voff), "s"(sbase), "i"(OFF) : "memory");
}
DEV void gstore16(void* sbase, u32 voff, u32x4 d) {
  asm volatile("global_store_dwordx4 %0, %1, %2 sc1\n\ts_nop 1" ::"v"(voff), "v"(d), "s"(sbase) : "memory");
}
DEV void gstore8(void* sbase, u32 voff, u32x2 d) {
  asm volatile("global_store_dwordx2 %0, %1, %2 sc1" ::"v"(voff), "v"(d), "s"(sbase) : "memory");
}
DEV void gstore16p(void* sbase, u32 voff, u32x4 d) {
  asm volatile("global_store_dwordx4 %0, %1, %2\n\ts_nop 1" ::"v"(voff), "v"(d), "s"(sbase) : "memory");
}
#define Z2STORE gstore16
DEV void gload4(u32& d, const void* sbase, u32 voff) {
  asm volatile("global_load_dword %0, %1, %2" : "=v"(d) : "v"(voff), "s"(sbase) : "memory");
}
template <int N> DEV void wait_vm() { asm volatile("s_waitcnt vmcnt(%0)" ::"i"(N) : "memory"); }
DEV void touch(u32& d, const void* p) { asm volatile("global_load_dword %0, %1, off" : "=v"(d) : "v"(p) : "memory"); }
DEV void keep(u32& a) { asm volatile("" : "+v"(a)); }
DEV void pin(u32x4& a) { asm volatile("" : "+v"(a)); }
DEV void bar() { asm volatile("s_waitcnt lgkmcnt(0)\n\ts_barrier" ::: "memory"); }
DEV float xsum16(float v) { auto r = __builtin_amdgcn_permlane16_swap(__float_as_uint(v), __float_as_uint(v), false, false); return __uint_as_float(r[0]) + __uint_as_float(r[1]); }
DEV float xsum32(float v) { auto r = __builtin_amdgcn_permlane32_swap(__float_as_uint(v), __float_as_uint(v), false, false); return __uint_as_float(r[0]) + __uint_as_float(r[1]); }
DEV f32x2 ln_scalars(float sum, float sq, float inv_n) {
  float m = sum * inv_n;
  float v = fmaxf(sq * inv_n - m * m, 0.f);
  float sd = rbf(__builtin_amdgcn_sqrtf(rbf(rbf(v) + 1e-5f)));
  return (f32x2){rbf(m), __builtin_amdgcn_rcpf(sd)};
}
DEV f32x2 gelu2(f32x2 x) {
  const float c1 = -2.3022081983f, c2 = -0.1029432396f;
  f32x2 t = x * x;
  t = t * c2 + c1;
  t = t * x;
  f32x2 e = {__builtin_amdgcn_exp2f(t.x), __builtin_amdgcn_exp2f(t.y)};
  e = e + 1.0f;
  f32x2 r = {__builtin_amdgcn_rcpf(e.x), __builtin_amdgcn_rcpf(e.y)};
  return x * r;
}
DEV f32x2 cmul(f32x2 a, f32x2 b) { return (f32x2){fmaf(a.x, b.x, -(a.y * b.y)), fmaf(a.x, b.y, a.y * b.x)}; }
DEV f32x2 csqr(f32x2 a) { return (f32x2){a.x * a.x - a.y * a.y, 2.f * a.x * a.y}; }

#define K1_PKACT
#define K1_PUBDELAY 8
#define K1_PRIO 2
#define K1_PF 4
constexpr int k1_slot(int u) { return u < 9 ? u : u - 9; }
constexpr int K1_X = 73728, K1_UT = K1_X + 65536, K1_CS = K1_UT + 32 * 272, K1_LN = K1_CS + 512, K1_LN1 = K1_LN + 1024, K1_CNT = K1_LN1 + 1024, K1_DUMP = K1_CNT + 64, K1_LDS = K1_DUMP + 4096;

typedef LDSAS char* lptr;
DEV lptr pinned(lptr p) { asm volatile("" : "+v"(p)); return p; }
DEV u32x4 lread16(lptr p) { return *(LDSAS u32x4*)p; }
DEV u32x4 ltr_frag(lptr p0, lptr p1) {
  s16x4 a = __builtin_amdgcn_ds_read_tr16_b64_v4i16((LDSAS s16x4*)p0), b = __builtin_amdgcn_ds_read_tr16_b64_v4i16((LDSAS s16x4*)p1);
  u32x2 x = __builtin_bit_cast(u32x2, a), y = __builtin_bit_cast(u32x2, b);
  return (u32x4){x.x, x.y, y.x, y.y};
}
DEV void mma(f32x4& c, const u32x4& a, const u32x4& b) { asm volatile("v_mfma_f32_16x16x32_bf16 %0, %1, %2, %0" : "+v"(c) : "v"(a), "v"(b)); }
DEV void mma0(f32x4& c, const u32x4& a, const u32x4& b) { asm volatile("v_mfma_f32_16x16x32_bf16 %0, %1, %2, 0" : "=&v"(c) : "v"(a), "v"(b)); }
template <bool FIRST> DEV void mmaf(f32x4& c, const u32x4& a, const u32x4& b) { if constexpr (FIRST) mma0(c, a, b); else mma(c, a, b); }
DEV void mma_drain() { SB(); asm volatile("s_nop 7\n\ts_nop 1" ::: "memory"); SB(); }
DEV void sbar() { asm volatile("s_barrier" ::: "memory"); }

struct K1L { const char* win; const char* xb; u32 xoff[2]; u32 woff[2]; u32 wdst; u32 xdst; int m0; };
template <int U> DEV void k1_unit(const K1L& g) {
  int line = U % 7 + g.m0; line = line >= 7 ? line - 7 : line;
  if constexpr (U < 7) {
#pragma unroll
    for (int i = 0; i < 4; i++) dma16(g.xb + line * 128 + (i >> 1) * 16 * (DM * 2), g.xoff[i & 1], g.xdst + (U & 3) * 16384 + i * 1024);
  }
#pragma unroll
  for (int i = 0; i < 2; i++) dma16(g.win + (size_t)line * 64 * (FF * 2) + (U < 7 ? FH * 2 : 0), g.woff[i], g.wdst + k1_slot(U) * 8192 + i * 1024);
}
struct K1C { lptr str[4]; lptr wp[4]; };
DEV void k1_xr(const K1C& g, u32x4 (&xf)[4], int M) {
#pragma unroll
  for (int i = 0; i < 4; i++) xf[i] = lread16(g.str[i] + (M & 3) * 16384);
}
template <int U> DEV u32x4 k1_wf(const K1C& g, int f) {
  if constexpr (k1_slot(U) < 8) return ltr_frag(g.wp[f & 3] + k1_slot(U) * 8192 + (f >> 2) * 4096, g.wp[f & 3] + k1_slot(U) * 8192 + (f >> 2) * 4096 + 512);
  else { lptr p = pinned(g.wp[f & 3] + 65536); return ltr_frag(p + (f >> 2) * 4096, p + (f >> 2) * 4096 + 512); }
}
template <int U> DEV void k1_main(const K1C& g, const u32x4 (&xf)[4], u32x4 (&wf)[8], f32x4 (&acc)[2][4], f32x4& cs, const u32x4& ones) {
#pragma unroll
  for (int f = 0; f < 8; f++) {
    if (U % 7 == 0 && f < 4) {
      if (f == 0) mma0(cs, wf[f], ones);
      mma0(acc[0][f & 3], wf[f], xf[0]);
      mma0(acc[1][f & 3], wf[f], xf[1]);
    } else {
      if ((f & 3) == 0) mma(cs, wf[f], ones);
      mma(acc[0][f & 3], wf[f], xf[(f >> 2) * 2]);
      mma(acc[1][f & 3], wf[f], xf[(f >> 2) * 2 + 1]);
    }
    SB(); if constexpr (U % 7 < 6) wf[f] = k1_wf<U + 1>(g, f); SB();
  }
}
template <int M>
DEV void k1_stage(const K1C& g, u32x4 (&xf)[7][4], u32x4 (&wf)[8], f32x4 (&acc)[2][4], f32x4& cs, f32x4 (&rsum)[2], f32x4 (&gram)[2], const u32x4& ones) {
  asm volatile("s_waitcnt lgkmcnt(15)\n\ts_barrier" ::: "memory");
  if constexpr (M < 6) k1_xr(g, xf[M + 1], M + 1);
  SB();
#pragma unroll
  for (int i = 0; i < 4; i++) {
    if (M == 0 && i < 2) { mma0(rsum[i & 1], ones, xf[M][i]); mma0(gram[i & 1], xf[M][i], xf[M][i]); }
    else { mma(rsum[i & 1], ones, xf[M][i]); mma(gram[i & 1], xf[M][i], xf[M][i]); }
  }
  k1_main<M>(g, xf[M], wf, acc, cs, ones);
  if constexpr (M < 6) k1_stage<M + 1>(g, xf, wf, acc, cs, rsum, gram, ones);
}
template <int M> DEV void k1_z1loop(const K1C& g, u32x4 (&xf)[7][4], u32x4 (&wf)[8], f32x4 (&acc)[2][4], f32x4& cs, const u32x4& ones) {
  k1_main<M + 7>(g, xf[M], wf, acc, cs, ones);
  if constexpr (M < 6) k1_z1loop<M + 1>(g, xf, wf, acc, cs, ones);
}
DEV void k1_act(f32x4 (&acc)[2][4], const char* csb, int w, const f32x2 (&ln1)[2]) {
#pragma unroll
  for (int p = 0; p < 4; p++) {
    const f32x4 c = *(const f32x4*)(csb + (p ^ w) * 64);
#pragma unroll
    for (int t = 0; t < 2; t++) {
      f32x2 a = (acc[t][p].xy - c.xy * ln1[t].x) * ln1[t].y;
      f32x2 b = (acc[t][p].zw - c.zw * ln1[t].x) * ln1[t].y;
      a = gelu2(a); b = gelu2(b);
      acc[t][p] = (f32x4){a.x, a.y, b.x, b.y};
    }
  }
}
DEV void act4_stats(f32x4& x, const f32x4& c, float m, float rs, float c1, float& s, float& s2) {
  float x0 = x.x, x1 = x.y, x2 = x.z, x3 = x.w, t0, t1, t2, t3;
  asm("v_fma_f32 %0, -%10, %14, %0\n\tv_fma_f32 %1, -%11, %14, %1\n\tv_fma_f32 %2, -%12, %14, %2\n\tv_fma_f32 %3, -%13, %14, %3\n\t"
      "v_mul_f32 %0, %0, %15\n\tv_mul_f32 %1, %1, %15\n\tv_mul_f32 %2, %2, %15\n\tv_mul_f32 %3, %3, %15\n\t"
      "v_mul_f32 %4, %0, %0\n\tv_mul_f32 %5, %1, %1\n\tv_mul_f32 %6, %2, %2\n\tv_mul_f32 %7, %3, %3\n\t"
      "v_fmamk_f32 %4, %4, 0xbdd2d3e8, %16\n\tv_fmamk_f32 %5, %5, 0xbdd2d3e8, %16\n\tv_fmamk_f32 %6, %6, 0xbdd2d3e8, %16\n\tv_fmamk_f32 %7, %7, 0xbdd2d3e8, %16\n\t"
      "v_mul_f32 %4, %4, %0\n\tv_mul_f32 %5, %5, %1\n\tv_mul_f32 %6, %6, %2\n\tv_mul_f32 %7, %7, %3\n\t"
      "v_exp_f32 %4, %4\n\tv_exp_f32 %5, %5\n\tv_exp_f32 %6, %6\n\tv_exp_f32 %7, %7\n\t"
      "v_add_f32 %4, 1.0, %4\n\tv_add_f32 %5, 1.0, %5\n\tv_add_f32 %6, 1.0, %6\n\tv_add_f32 %7, 1.0, %7\n\t"
      "v_rcp_f32 %4, %4\n\tv_rcp_f32 %5, %5\n\tv_rcp_f32 %6, %6\n\tv_rcp_f32 %7, %7\n\t"
      "v_mul_f32 %0, %0, %4\n\tv_mul_f32 %1, %1, %5\n\tv_mul_f32 %2, %2, %6\n\tv_mul_f32 %3, %3, %7\n\t"
      "v_add_f32 %4, %0, %1\n\tv_fmac_f32 %9, %0, %0\n\tv_add_f32 %5, %2, %3\n\tv_fmac_f32 %9, %1, %1\n\t"
      "v_add_f32 %8, %8, %4\n\tv_fmac_f32 %9, %2, %2\n\tv_add_f32 %8, %8, %5\n\tv_fmac_f32 %9, %3, %3"
      : "+v"(x0), "+v"(x1), "+v"(x2), "+v"(x3), "=&v"(t0), "=&v"(t1), "=&v"(t2), "=&v"(t3), "+v"(s), "+v"(s2)
      : "v"(c.x), "v"(c.y), "v"(c.z), "v"(c.w), "v"(m), "v"(rs), "v"(c1));
  x = (f32x4){x0, x1, x2, x3};
}
DEV int lane_id() { int l = __builtin_amdgcn_mbcnt_hi(~0u, __builtin_amdgcn_mbcnt_lo(~0u, 0u)); asm volatile("" : "+v"(l)); return l; }
DEV void quad_sync(lptr cnt, int l) {
  asm volatile("s_waitcnt lgkmcnt(0)" ::: "memory");
  if (l == 0) __hip_atomic_fetch_add((LDSAS u32*)cnt, 1u, __ATOMIC_RELAXED, __HIP_MEMORY_SCOPE_WORKGROUP);
  while (__builtin_amdgcn_readfirstlane(*(volatile LDSAS u32*)cnt) < 4u) __builtin_amdgcn_s_sleep(0);
  asm volatile("" ::: "memory");
}
DEV void lbar0() { asm volatile("s_waitcnt vmcnt(6) lgkmcnt(0)\n\ts_barrier" ::: "memory"); }

DEV void k1_body(char* smem, const u16* x, const u16* win, const u16* wsp, const u16* wout, char* ws) {
  STAMP_DECL; STAMP(0);
  const int b = blockIdx.x, xcd = b & 7, idx = b >> 3;
  const int w8 = __builtin_amdgcn_readfirstlane(threadIdx.x >> 6), l = threadIdx.x & 63, w = w8 & 3;
  if (b >= 224) {
    const int j = (idx - 28) * 8 + w8;
    const char* p = (const char*)wsp + ((size_t)xcd * 1024 + 112 * K1_PF) * 1024;
    constexpr int REST = 1024 - 112 * K1_PF;
#pragma unroll 1
    for (int i = j; i < REST; i += 32) dma16(p + (size_t)i * 1024, l * 16, lds_addr(smem) + w8 * 1024);
    const char* po = (const char*)wout + (size_t)(xcd * 98) * 1024;
#pragma unroll
    for (int k = 0; k < 4; k++) { int c = j + 32 * k; c = c < 98 ? c : 97; dma16(po + c * 1024, l * 16, lds_addr(smem) + w8 * 1024); }
    STAMP(1); STAMP_FLUSH(0);
    return;
  }
  const int i7 = (idx * 37) >> 8;
  const int panel = (xcd >> 1) * 4 + i7, sib = (xcd & 1) * 7 + idx - 7 * i7;
  const lptr lds = (lptr)smem;
  K1L g;
  if (w8 >= 4) {
    g.win = (const char*)win + (size_t)(16 * w) * (FF * 2) + sib * 128;
    g.xb = (const char*)x + (size_t)(panel * 128 + w * 32) * (DM * 2);
    g.xoff[0] = (u32)((l >> 3) * (DM * 2) + (((l & 7) ^ (l >> 4)) * 16));
    g.xoff[1] = (u32)((8 + (l >> 3)) * (DM * 2) + (((l & 7) ^ ((l >> 4) | 4)) * 16));
    g.woff[0] = (u32)((l >> 3) * (FF * 2) + (((l & 7) ^ (((l >> 4) & 1) << 1)) * 16));
    g.woff[1] = (u32)((8 + (l >> 3)) * (FF * 2) + (((l & 7) ^ (((l >> 4) & 1) << 1) ^ 4) * 16));
    g.xdst = lds_addr(smem) + K1_X + w * 4096;
    g.wdst = lds_addr(smem) + w * 2048;
    g.m0 = sib >> 1;
    k1_unit<0>(g); k1_unit<1>(g);
  }
  K1C c;
  {
    const int rho = l & 15, q = l >> 4, qq = (l >> 2) & 3, pp = l & 3;
#pragma unroll
    for (int i = 0; i < 4; i++) c.str[i] = pinned(lds + K1_X + w * 4096 + (i & 1) * 2048 + rho * 128 + (((4 * (i >> 1) + q) ^ (rho >> 1)) * 16));
    const int sg = (qq >> 1) | ((q & 1) << 1);
#pragma unroll
    for (int f = 0; f < 4; f++) c.wp[f] = lds + (8 * q + qq) * 128 + (((f ^ w) ^ sg) * 32) + 8 * pp;
  }
  u32x4 ones = {0x3f803f80u, 0x3f803f80u, 0x3f803f80u, 0x3f803f80u};
  pin(ones);
  u32x4 xf[7][4], wf[8];
  f32x4 acc[2][4], cs;
  if (w8 >= 4) {
    lbar0();
    k1_unit<2>(g); k1_xr(c, xf[0], 0); lbar0();
    k1_unit<3>(g); k1_xr(c, xf[1], 1); lbar0();
    k1_unit<4>(g); k1_xr(c, xf[2], 2); lbar0();
    k1_unit<5>(g); k1_xr(c, xf[3], 3); lbar0();
    k1_unit<6>(g); k1_xr(c, xf[4], 4); lbar0();
    k1_unit<7>(g); k1_unit<8>(g); k1_unit<9>(g); k1_xr(c, xf[5], 5); lbar0();
    k1_unit<10>(g); k1_unit<11>(g); k1_unit<12>(g); k1_xr(c, xf[6], 6);
    asm volatile("s_waitcnt lgkmcnt(0)\n\ts_barrier" ::: "memory");
    k1_unit<13>(g);
    STAMP(1);
#pragma unroll
    for (int i = 0; i < K1_PF; i++)
      dma16((const char*)wsp + ((size_t)xcd * 1024 + (idx * 4 + w) * K1_PF + i) * 1024, l * 16, lds_addr(smem) + K1_DUMP + w * 1024);
    wait_vm<K1_PF>(); sbar();
    STAMP(2);
    f32x2 ln1[2];
    {
    const int rho = l & 15, q = l >> 4;
#pragma unroll
    for (int t = 0; t < 2; t++) ln1[t] = *(const f32x2*)(smem + K1_LN1 + w * 256 + (t * 16 + rho) * 8);
#pragma unroll
    for (int f = 0; f < 8; f++) wf[f] = k1_wf<7>(c, f);
#pragma unroll
    for (int t = 0; t < 2; t++) {
      const u32x4 va = xf[0][t], vb = xf[0][2 + t];
      const u32 msk = (sib & 1) ? 0xffffffffu : 0u;
      const u32x4 v = (va & ~msk) | (vb & msk);
      u16* ub = (u16*)(smem + K1_UT + (8 * q) * 272 + (w * 32 + t * 16 + rho) * 2);
#pragma unroll
      for (int j = 0; j < 4; j++) {
        float a = rbf(rbf(bflo(v[j]) - ln1[t].x) * ln1[t].y), bb = rbf(rbf(bfhi(v[j]) - ln1[t].x) * ln1[t].y);
        u32 o = pk_f16(a, bb);
        ub[(2 * j) * 136] = (u16)o;
        ub[(2 * j + 1) * 136] = (u16)(o >> 16);
      }
    }
}
    SB();
    k1_z1loop<0>(c, xf, wf, acc, cs, ones);
    mma_drain();
    const int l = lane_id(), rho = l & 15, q = l >> 4;
    if (rho == 0) *(f32x4*)(smem + K1_CS + 256 + w * 64 + q * 16) = cs;
    STAMP(3);
    quad_sync(lds + K1_CNT, l);
    STAMP(4);
#pragma unroll
    for (int i = 0; i < 2; i++) {
      const u32x4 uo = *(const u32x4*)(smem + K1_UT + (w * 8 + i * 4 + q) * 272 + rho * 16);
      gstore16(ws, (u32)(WS_UT + (sib * 32 + w * 8 + i * 4 + q) * (SEQ * 2) + panel * 256 + rho * 16), uo);
    }
    k1_act(acc, smem + K1_CS + 256 + q * 16, w, ln1);
#pragma unroll
    for (int t = 0; t < 2; t++)
#pragma unroll
      for (int cc = 0; cc < 2; cc++) {
        u32x2 a = {pk_bf16(acc[t][2 * cc].x, acc[t][2 * cc].y), pk_bf16(acc[t][2 * cc].z, acc[t][2 * cc].w)};
        u32x2 bb = {pk_bf16(acc[t][2 * cc + 1].x, acc[t][2 * cc + 1].y), pk_bf16(acc[t][2 * cc + 1].z, acc[t][2 * cc + 1].w)};
        const u32x4 o = (w & 1) ? (u32x4){bb.x, bb.y, a.x, a.y} : (u32x4){a.x, a.y, bb.x, bb.y};
        gstore16(ws, (u32)(WS_Z1F + ((panel * 8 + w * 2 + t) * 28 + sib * 2 + (cc ^ (w >> 1))) * 1024 + l * 16), o);
      }
    STAMP(5); STAMP_FLUSH(0);
    return;
  }
  const int grp = panel * 4 + w;
  u32 tag;
  asm volatile("s_load_dword %0, %1, 0x0\n\ts_waitcnt lgkmcnt(0)" : "=s"(tag) : "s"(ws + WS_EP + grp * 256) : "memory");
  tag += 1u;
  f32x4 rsum[2], gram[2];
  if (threadIdx.x < 2) *(u32*)(smem + K1_CNT + threadIdx.x * 4) = 0u;
  bar();
  STAMP(2);
  k1_xr(c, xf[0], 0);
#pragma unroll
  for (int f = 0; f < 8; f++) wf[f] = k1_wf<0>(c, f);
  k1_stage<0>(c, xf, wf, acc, cs, rsum, gram, ones);
  mma_drain();
  const int rho = l & 15, q = l >> 4, qq = (l >> 2) & 3, pp = l & 3;
  if (rho == 0) *(f32x4*)(smem + K1_CS + w * 64 + q * 16) = cs;
  STAMP(3);
  f32x2 ln1[2];
#pragma unroll
  for (int t = 0; t < 2; t++) {
    float d = (rho & 2) ? ((rho & 1) ? gram[t].w : gram[t].z) : ((rho & 1) ? gram[t].y : gram[t].x);
    d = ((rho >> 2) == q) ? d : 0.f;
    d = xsum32(xsum16(d));
    ln1[t] = ln_scalars(rsum[t].x, d, 1.f / DM);
    if (q == 0) *(f32x2*)(smem + K1_LN1 + w * 256 + (t * 16 + rho) * 8) = ln1[t];
  }
  bar();
  __builtin_amdgcn_s_setprio(K1_PRIO);
  STAMP(4);
  GAS u64* gr = (GAS u64*)(ws + WS_GR) + (size_t)grp * 14 * 64;
  {
    float st[2][2] = {{0.f, 0.f}, {0.f, 0.f}};
    k1_act(acc, smem + K1_CS + q * 16, w, ln1);
#pragma unroll
    for (int t = 0; t < 2; t++) {
      f32x2 s = {0.f, 0.f}, s2 = {0.f, 0.f};
#pragma unroll
      for (int p = 0; p < 4; p++) {
        s += acc[t][p].xy; s += acc[t][p].zw;
        s2 += acc[t][p].xy * acc[t][p].xy; s2 += acc[t][p].zw * acc[t][p].zw;
      }
      st[t][0] = s.x + s.y; st[t][1] = s2.x + s2.y;
    }
#pragma unroll
    for (int t = 0; t < 2; t++) { st[t][0] = xsum32(xsum16(st[t][0])); st[t][1] = xsum32(xsum16(st[t][1])); }
    const float v = (q & 2) ? ((q & 1) ? st[1][1] : st[0][1]) : ((q & 1) ? st[1][0] : st[0][0]);
    __hip_atomic_store(gr + sib * 64 + l, ((u64)tag << 32) | __float_as_uint(v), __ATOMIC_RELAXED, __HIP_MEMORY_SCOPE_AGENT);
  }
  STAMP(5);
  __builtin_amdgcn_s_sleep(K1_PUBDELAY);
  STAMP(6);
  u64 rv[14];
#pragma unroll
  for (int i = 0; i < 14; i++) rv[i] = __hip_atomic_load(gr + i * 64 + l, __ATOMIC_RELAXED, __HIP_MEMORY_SCOPE_AGENT);
  STAMP(7);
  float tot;
  for (u32 spins = 0;; spins++) {
    bool ok = true; tot = 0.f;
#pragma unroll
    for (int i = 0; i < 14; i++) { ok &= (u32)(rv[i] >> 32) == tag; tot += __uint_as_float((u32)rv[i]); }
    if (__all(ok) || spins > (1u << 20)) break;
#pragma unroll
    for (int i = 0; i < 14; i++) rv[i] = __hip_atomic_load(gr + i * 64 + l, __ATOMIC_RELAXED, __HIP_MEMORY_SCOPE_AGENT);
  }
  STAMP(8);
  if (sib == 0 && l == 0) __hip_atomic_store((GAS u32*)(ws + WS_EP + grp * 256), tag, __ATOMIC_RELAXED, __HIP_MEMORY_SCOPE_AGENT);
  {
    auto bb = __builtin_amdgcn_permlane32_swap(__float_as_uint(tot), __float_as_uint(tot), false, false);
    const f32x2 lr = ln_scalars(__uint_as_float(bb[0]), __uint_as_float(bb[1]), 1.f / FH);
    auto lm = __builtin_amdgcn_permlane16_swap(__float_as_uint(lr.x), __float_as_uint(lr.x), false, false);
    auto ls = __builtin_amdgcn_permlane16_swap(__float_as_uint(lr.y), __float_as_uint(lr.y), false, false);
    char* const stg = smem + K1_X + w * 8192;
#pragma unroll
    for (int t = 0; t < 2; t++) {
      const float m2 = __uint_as_float(lm[t]), r2 = __uint_as_float(ls[t]);
#pragma unroll
      for (int p = 0; p < 4; p++) {
        f32x2 a = (acc[t][p].xy - m2) * r2, b2 = (acc[t][p].zw - m2) * r2;
        *(u32x2*)(stg + (16 * t + rho) * 136 + p * 32 + q * 8) = (u32x2){pk_bf16(a.x, a.y), pk_bf16(b2.x, b2.y)};
      }
    }
#pragma unroll
    for (int p = 0; p < 4; p++) {
      const char* rp = stg + (8 * q + qq) * 136 + p * 32 + pp * 8;
      u32x4 o = tr_frag(rp, rp + 4 * 136);
      const int ft = sib * 4 + (p ^ w);
      Z2STORE(ws, (u32)(WS_Z2F + (((ft / 7) * 64 + grp) * 7 + ft % 7) * 1024 + l * 16), o);
    }
  }
  STAMP(9); STAMP_FLUSH(0);
}
#define K1_ATTR
__global__ K1_ATTR __launch_bounds__(512, 2) void k1(const u16* x, const u16* win, const u16* wsp, const u16* wout, char* ws) {
  __shared__ __attribute__((aligned(16))) char smem[K1_LDS];
  k1_body(smem, x, win, wsp, wout, ws);
}

#define K2_R 18
#define K2_PF 7
#define K2_TD 0
constexpr bool k2_touch_at(int tau) { return K2_TD > 0 && tau % 14 == 0 && tau / 7 + K2_TD <= 14; }
constexpr int K2_EPI = 5, K2_EPI_AT = 112 - K2_R + 2, K2_PRO = 16 + K2_R + (K2_TD > 0 ? 2 : 0);
constexpr int k2_apiece(int tau) {
  int s = tau / 7, f = tau % 7;
  int a = (s + 3) / 2 + ((s & 1) ? 0 : 1) - ((s & 1) ? 0 : 1);
  if (s & 1) { a = (s + 3) / 2; return (a <= 7 && (f & 1) == 0) ? 8 * a + f / 2 : -1; }
  a = (s + 2) / 2;
  return (a >= 2 && a <= 7 && f < 4) ? 8 * a + 4 + f : -1;
}
constexpr int k2_ops_after(int tau) { return (tau + K2_R < 112 ? 1 : 0) + (k2_apiece(tau) >= 0 ? 1 : 0) + (tau == K2_EPI_AT ? K2_EPI + K2_PF : 0) + (k2_touch_at(tau) ? 1 : 0); }
constexpr int k2_issued(int tau) { int n = K2_PRO; for (int i = 0; i < tau; i++) n += k2_ops_after(i); return n; }
constexpr int k2_pos_B(int n) { return n < 14 ? 8 + n + 1 : (n < K2_R ? 16 + n + 1 : k2_issued(n - K2_R) + 1); }
constexpr int k2_pos_A(int a) { return a == 0 ? 8 : (a == 1 ? 30 : k2_issued(7 * (2 * a - 2) + 4)); }
constexpr int k2_wait(int tau) {
  int need = k2_pos_B(tau);
  int s = tau / 7, f = tau % 7;
  if ((s & 1) && f == 2 && (s + 1) / 2 <= 7) { int a = k2_pos_A((s + 1) / 2); need = a > need ? a : need; }
  return k2_issued(tau) - need;
}
#define K2_MFMA mfma_bf
struct K2G { const char* wsp0; const char* wsp; const char* z2; const char* z1; const char* bsp; const char* pf; u32 aoff[2]; u32 boff; u32 adst; u32 dump; u32 tb; int arot; int w; };
template <int P> DEV void k2_a1(const K2G& g) {
  constexpr int SS = P / 8;
  const int i = (P % 8) ^ g.arot;
  dma16(g.wsp + SS * 128 + (size_t)i * 8 * 4096, g.aoff[P & 1], g.adst + (SS & 1) * 8192 + ((i ^ (2 * g.w)) * 1024));
}
template <int A, int B> DEV void k2_as(const K2G& g) { if constexpr (A < B) { k2_a1<A>(g); k2_as<A + 1, B>(g); } }
template <int N> DEV void k2_b1(const K2G& g, u32x4 (&bf)[K2_R]) {
 gload16<(N % 8 - 4) * 1024>(bf[N % K2_R], g.z2 + (N / 8) * 8192, g.boff); }
template <int A, int B> DEV void k2_bs(const K2G& g, u32x4 (&bf)[K2_R]) { if constexpr (A < B) { k2_b1<A>(g, bf); k2_bs<A + 1, B>(g, bf); } }
template <int I> DEV u32x4 k2_read_a(const char* ap0, const char* ap1, int t) {
  return *(const u32x4*)(((I & 1) ? ap1 : ap0) + ((I >> 1) & 1) * 8192 + t * 2048);
}
template <int TAU>
DEV void k2_group(const K2G& g, u32x4 (&bf)[K2_R], u32x4 (&af)[2][4], const char* ap0, const char* ap1, f32x4 (&acc)[4][7], u32x4 (&ep)[4], u32& bw, u32 (&tch)[2]) {
  constexpr int S = TAU / 7, F = TAU % 7;
  wait_vm<k2_wait(TAU)>();
  pin(bf[TAU % K2_R]);
  if constexpr (S < 15 && F >= 2 && F < 6) af[(S + 1) & 1][F - 2] = k2_read_a<S + 1>(ap0, ap1, F - 2);
  SB();
#pragma unroll
  for (int t = 0; t < 4; t++) acc[t][F] = K2_MFMA(bf[TAU % K2_R], af[S & 1][t], acc[t][F]);
  SB();
  if constexpr (TAU + K2_R < 112) k2_b1<TAU + K2_R>(g, bf);
  if constexpr (k2_apiece(TAU) >= 0) k2_a1<k2_apiece(TAU)>(g);
  if constexpr (k2_touch_at(TAU)) { keep(tch[(TAU / 14) & 1]); gload4(tch[(TAU / 14) & 1], g.z2 + (S + K2_TD) * 7168 - 4096, g.tb); }
  if constexpr (TAU == 111) { keep(tch[0]); keep(tch[1]); }
  if constexpr (TAU == K2_EPI_AT) {
    gload16<0>(ep[0], g.z1, g.boff); gload16<1024>(ep[1], g.z1, g.boff); gload16<2048>(ep[2], g.z1, g.boff); gload16<3072>(ep[3], g.z1, g.boff);
    gload4(bw, g.bsp, (g.boff >> 3) & 28u);
#pragma unroll
    for (int i = 0; i < K2_PF; i++) dma16(g.pf + i * 1024, g.boff, g.dump);
  }
  if constexpr (TAU < 111) k2_group<TAU + 1>(g, bf, af, ap0, ap1, acc, ep, bw, tch);
}

DEV void k2_gemm(char* smem, int b, const u16* wsp, const u16* bsp, const u16* wout, char* ws) {
  STAMP_DECL; STAMP(0);
  const int xcd = b & 7, idx = b >> 3;
  const int tp = (xcd >> 1) * 8 + (idx >> 2), fp = (xcd & 1) * 4 + (idx & 3);
  const int w = __builtin_amdgcn_readfirstlane(threadIdx.x >> 6), l = threadIdx.x & 63;
  const int rho = l & 15, q = l >> 4;
  const int tt = tp * 4 + w;
  char* const reg = smem + w * 16384;
  K2G g;
  g.wsp = (const char*)wsp + (size_t)tp * 64 * 4096 + w * 1024;
  g.wsp0 = (const char*)wsp + (size_t)tp * 64 * 4096 + w * 65536;
  g.z2 = ws + WS_Z2F + ((size_t)(fp * 64 + w * 16) * 7 + 4) * 1024;
  g.z1 = ws + WS_Z1F + ((size_t)tt * 28 + ((fp * 7) >> 1)) * 1024;
  g.bsp = (const char*)bsp + tt * 32;
  {
    int pc = (idx * 4 + w) * 7;
    pc = pc > 784 - 7 ? 784 - 7 : pc;
    g.pf = (const char*)wout + pc * 1024;
  }
  g.aoff[0] = (u32)((l >> 3) * 4096 + (((l & 7) ^ (l >> 4)) * 16));
  g.aoff[1] = (u32)((l >> 3) * 4096 + (((l & 7) ^ ((l >> 4) | 4)) * 16));
  g.boff = l * 16;
  g.adst = lds_addr(reg);
  g.dump = lds_addr(smem) + 65536 + w * 1024;
  g.w = w;
  g.arot = (idx & 3) * 2;
  u32x4 bf[K2_R], af[2][4], ep[4];
  u32 bw, tch[2];
  g.tb = (u32)(((idx >> 2) * 14 + (l < 14 ? l : 13)) * 128);
  tch[0] = tch[1] = 0u;
  k2_as<0, 8>(g); k2_bs<0, 14>(g, bf); k2_as<8, 16>(g); k2_bs<14, K2_R>(g, bf);
  const char* ap0 = reg + rho * 128 + (((0 + q) ^ (rho >> 1)) * 16);
  const char* ap1 = reg + rho * 128 + (((4 + q) ^ (rho >> 1)) * 16);
  f32x4 acc[4][7] = {};
  STAMP(1);
  wait_vm<K2_PRO - 8>();
  STAMP(2);
#pragma unroll
  for (int t = 0; t < 4; t++) af[0][t] = k2_read_a<0>(ap0, ap1, t);
  k2_group<0>(g, bf, af, ap0, ap1, acc, ep, bw, tch);
  STAMP(3);
#pragma unroll
  for (int f = 0; f < 7; f++) {
    *(f32x4*)(reg + f * 1024 + l * 16) = acc[1][f];
    *(f32x4*)(reg + (7 + f) * 1024 + l * 16) = acc[3][f];
  }
  bar();
  STAMP(4);
  char* const in1 = smem + (w ^ 1) * 16384;
#pragma unroll
  for (int f = 0; f < 7; f++) acc[2][f] += *(const f32x4*)(in1 + (7 + f) * 1024 + l * 16);
#pragma unroll
  for (int f = 0; f < 7; f++) *(f32x4*)(in1 + (7 + f) * 1024 + l * 16) = acc[2][f];
#pragma unroll
  for (int f = 0; f < 7; f++) acc[0][f] += *(const f32x4*)(in1 + f * 1024 + l * 16);
  bar();
  char* const in2 = smem + (w ^ 3) * 16384;
#pragma unroll
  for (int i = 0; i < 1; i++) wait_vm<K2_PF>();
#pragma unroll
  for (int i = 0; i < 4; i++) pin(ep[i]);
  asm volatile("" : "+v"(bw));
  const float bs = (rho & 1) ? bfhi(bw) : bflo(bw);
  STAMP(5);
  const int odd = fp & 1;
  u32x2 hv[8];
#pragma unroll
  for (int f = 0; f < 7; f++) {
    f32x4 v = acc[0][f] + *(const f32x4*)(in2 + (7 + f) * 1024 + l * 16);
    v = v + bs;
    const u32x4 ze = ep[f >> 1], zo = ep[(f + 1) >> 1];
    const u32 za = odd ? ((f & 1) ? zo.x : zo.z) : ((f & 1) ? ze.z : ze.x);
    const u32 zb = odd ? ((f & 1) ? zo.y : zo.w) : ((f & 1) ? ze.w : ze.y);
    hv[f] = (u32x2){pk_bf16(v.x * bflo(za), v.y * bfhi(za)), pk_bf16(v.z * bflo(zb), v.w * bfhi(zb))};
  }
  const u32 hb = (u32)(WS_HF + (tt * 28 + ((fp * 7) >> 1)) * 1024 + l * 16);
  if (odd) {
    gstore8(ws, hb + 8, hv[0]);
#pragma unroll
    for (int i = 0; i < 3; i++) gstore16(ws, hb + (i + 1) * 1024, (u32x4){hv[1 + 2 * i].x, hv[1 + 2 * i].y, hv[2 + 2 * i].x, hv[2 + 2 * i].y});
  } else {
#pragma unroll
    for (int i = 0; i < 3; i++) gstore16(ws, hb + i * 1024, (u32x4){hv[2 * i].x, hv[2 * i].y, hv[1 + 2 * i].x, hv[1 + 2 * i].y});
    gstore8(ws, hb + 3 * 1024, hv[6]);
  }
  STAMP(6); STAMP_FLUSH(1);
}

constexpr int KG_ROW = 272, KG_WAVE = 53 * KG_ROW;
constexpr int CV_CS = 4160, CV_T = 8 * CV_CS, CV_U = 2 * CV_T, CV_CMP = CV_U + 2 * 4608;
DEV f32x2 cexp_pow2(float gl, float fh, float fl, float p) {
  float m = __builtin_amdgcn_exp2f(gl * p);
  float r = __builtin_amdgcn_fractf(fh * p) + fl * p;
  return (f32x2){m * __builtin_amdgcn_cosf(r), m * __builtin_amdgcn_sinf(r)};
}
DEV float exp_acc(float x) {
  const float h = x * 1.44269504089f;
  const float lo = fmaf(x, 1.44269504089f, -h) + x * 1.925963e-8f;
  return __builtin_amdgcn_exp2f(h) * fmaf(lo, 0.69314718056f, 1.f);
}
struct KGen { f32x2 e1, e2, e4, e8, e16, e32, e64, e128, e256, e512, e1024, e2048, c0; };
DEV KGen kg_gens(float la, float ai, float cr, float ci, float dt, float sc) {
  const float zr = -rbf(exp_acc(la)) * dt, zi = ai * dt;
  const float gl = zr * 1.44269504089f;
  const float fh = zi * 0.15915494309f;
  const float fl = fmaf(zi, 0.15915494309f, -fh) + zi * 6.4206382e-9f;
  KGen k;
  k.c0 = (f32x2){cr * sc, ci * sc};
  k.e1 = cexp_pow2(gl, fh, fl, 1.f); k.e2 = csqr(k.e1); k.e4 = csqr(k.e2); k.e8 = csqr(k.e4);
  k.e16 = cexp_pow2(gl, fh, fl, 16.f); k.e32 = csqr(k.e16); k.e64 = csqr(k.e32); k.e128 = csqr(k.e64);
  k.e256 = cexp_pow2(gl, fh, fl, 256.f); k.e512 = csqr(k.e256); k.e1024 = csqr(k.e512); k.e2048 = csqr(k.e1024);
  return k;
}
DEV void kg_tables(const KGen& k, u32* tw) {
#define TW(row, v) tw[(row) * (KG_ROW / 4)] = (v)
  {
    f32x2 tb[16];
    tb[0] = (f32x2){128.f, 0.f};
    tb[1] = k.e8 * 128.f;
    tb[2] = k.e16 * 128.f; tb[3] = cmul(tb[1], k.e16);
    tb[4] = k.e32 * 128.f;
#pragma unroll
    for (int i = 1; i < 4; i++) tb[4 + i] = cmul(tb[i], k.e32);
    tb[8] = k.e64 * 128.f;
#pragma unroll
    for (int i = 1; i < 8; i++) tb[8 + i] = cmul(tb[i], k.e64);
    TW(0, pk_f16(128.f, 0.f));
    TW(16, pk_f16(k.e128.x * 128.f, k.e128.y * 128.f));
#pragma unroll
    for (int i = 1; i < 16; i++) { TW(i, pk_f16(tb[i].x, tb[i].y)); f32x2 u = cmul(tb[i], k.e128); TW(16 + i, pk_f16(u.x, u.y)); }
  }
  {
    f32x2 a[21];
    a[0] = k.c0; a[1] = cmul(k.c0, k.e1); a[2] = cmul(k.c0, k.e2); a[3] = cmul(a[1], k.e2);
#pragma unroll
    for (int i = 0; i < 4; i++) a[4 + i] = cmul(a[i], k.e4);
#pragma unroll
    for (int i = 0; i < 4; i++) a[8 + i] = cmul(a[2 * i], k.e256);
    a[12] = cmul(a[0], k.e512); a[13] = cmul(a[4], k.e512); a[14] = cmul(a[8], k.e512); a[15] = cmul(a[10], k.e512);
    a[16] = cmul(a[0], k.e1024); a[17] = cmul(a[8], k.e1024); a[18] = cmul(a[12], k.e1024); a[19] = cmul(a[14], k.e1024);
    a[20] = cmul(a[0], k.e2048);
#pragma unroll
    for (int i = 0; i < 21; i++) TW(32 + i, pk_f16(a[i].x, -a[i].y));
  }
#undef TW
}
template <int CTRL> DEV u32 dpp(u32 old, u32 src) { return __builtin_amdgcn_update_dpp(old, src, CTRL, 0xf, 0xf, false); }
template <int CTRL> DEV float dpp_add(float v) { return v + __uint_as_float(__builtin_amdgcn_update_dpp(0u, __float_as_uint(v), CTRL, 0xf, 0xf, true)); }
DEV void cv_frags(u32x4 (&A)[8], u32x4 (&B)[8], const char* pa, const char* pb) {
#pragma unroll
  for (int i = 0; i < 8; i++) {
    A[i] = *(const u32x4*)(pa - 512 * i);
    B[i] = *(const u32x4*)(pb + 512 * i);
  }
}
DEV void cv_mma(const u32x4 (&A)[8], const u32x4 (&B)[8], f32x4 (&acc)[8]) {
#pragma unroll
  for (int a = 0; a < 8; a++)
#pragma unroll
    for (int s = 7; s >= a; s--) acc[s] = mfma_h(A[a], B[s - a], acc[s]);
}
#define SSM_PRIO 0
DEV void k2_ssm(char* smem, int cb, const u16* lar, const u16* aim, const u16* cre, const u16* cim, const u16* ldt, const u16* dsk, char* ws) {
  STAMP_DECL; STAMP(0);
  __builtin_amdgcn_s_setprio(3);
  const int w = __builtin_amdgcn_readfirstlane(threadIdx.x >> 6), l = threadIdx.x & 63;
  const int ch = w >> 1, d = cb * 2 + ch, hw = w & 1, t = hw * 64 + l;
  const int rho = l & 15, q = l >> 4;
  const size_t o = (size_t)d * NMODE;
  const u32 vla = *(const u32*)(lar + o + hw * 128 + 2 * l), vai = *(const u32*)(aim + o + hw * 128 + 2 * l);
  const u32 vcr = *(const u32*)(cre + o + hw * 128 + 2 * l), vci = *(const u32*)(cim + o + hw * 128 + 2 * l);
  const u32 ocr = *(const u32*)(cre + o + (hw ^ 1) * 128 + 2 * l), oci = *(const u32*)(cim + o + (hw ^ 1) * 128 + 2 * l);
  const u32 vdt = ldt[d], vdk = dsk[d];
  char* const T = smem + ch * CV_T;
  char* const U = smem + CV_U + ch * 4608;
  u16* const cmp = (u16*)(smem + CV_CMP + ch * 1408);
#pragma unroll
  for (int i = 0; i < 2; i++) dma16(ws + WS_UT + (size_t)d * (SEQ * 2) + (hw * 2 + i) * 1024, l * 16, lds_addr(U) + 512 + (hw * 2 + i) * 1024);
  __builtin_amdgcn_s_setprio(SSM_PRIO);
  if (t < 32) *(u32x4*)(U + t * 16) = (u32x4){0u, 0u, 0u, 0u};
  u32 gi[2][8];
#pragma unroll
  for (int c = 0; c < 2; c++)
#pragma unroll
    for (int j = 0; j < 4; j++) {
      const int l0 = 2047 - (hw * 128 + c * 64 + l) * 8 - 2 * j, l1 = l0 - 1;
      const u32 bits = pk_bf16((float)l0, (float)l1);
      int i0 = (int)(bits & 0xffffu) - 0x4280, i1 = (int)(bits >> 16) - 0x4280;
      if (c == 1) { i0 = l0 < 128 ? l0 : i0; i1 = l1 < 128 ? l1 : i1; }
      gi[c][2 * j] = i0; gi[c][2 * j + 1] = i1;
    }
  const float dt = rbf(exp_acc(bf2f(vdt)));
  const float dk = bf2f(vdk);
  float S = fabsf(bflo(vcr)) + fabsf(bfhi(vcr)) + fabsf(bflo(vci)) + fabsf(bfhi(vci));
  float S2 = fabsf(bflo(ocr)) + fabsf(bfhi(ocr)) + fabsf(bflo(oci)) + fabsf(bfhi(oci));
  S = hw ? S2 + S : S + S2;
  S = dpp_add<0xb1>(S); S = dpp_add<0x4e>(S); S = dpp_add<0x141>(S); S = dpp_add<0x140>(S);
  S = xsum32(xsum16(S));
  int e = (int)((__float_as_uint(S) >> 23) & 255u) - 125;
  e = e < -100 ? -100 : (e > 100 ? 100 : e);
  e = __builtin_amdgcn_readfirstlane(e);
  const float sc = __uint_as_float((u32)(127 + 8 - e) << 23);
  const float ks = dt * __uint_as_float((u32)(127 + e - 14) << 23);
  STAMP(1);
  char* tab = smem + w * KG_WAVE;
  f32x16 kacc = {};
  {
    const int r = l & 31, h = l >> 5;
    const char* pa = tab + (32 + (r < 21 ? r : 20)) * KG_ROW + h * 16;
    const char* pb = tab + r * KG_ROW + h * 16;
    const KGen k0 = kg_gens(bflo(vla), bflo(vai), bflo(vcr), bflo(vci), dt, sc);
    STAMP(8);
    kg_tables(k0, (u32*)(tab + l * 4));
    SB();
    STAMP(9);
    u32x4 fa[8], fb[8];
#pragma unroll
    for (int kk = 0; kk < 8; kk++) { fa[kk] = *(const u32x4*)(pa + kk * 32); fb[kk] = *(const u32x4*)(pb + kk * 32); }
    SB();
    const KGen k1 = kg_gens(bfhi(vla), bfhi(vai), bfhi(vcr), bfhi(vci), dt, sc);
#pragma unroll
    for (int kk = 0; kk < 8; kk++) kacc = mfma32_h(fa[kk], fb[kk], kacc);
    STAMP(10);
    kg_tables(k1, (u32*)(tab + l * 4));
    STAMP(11);
#pragma unroll
    for (int kk = 0; kk < 8; kk++) kacc = mfma32_h(*(const u32x4*)(pa + kk * 32), *(const u32x4*)(pb + kk * 32), kacc);
    STAMP(12);
    float* give = (float*)tab + l;
    float keep[6];
#pragma unroll
    for (int i = 0; i < 6; i++) { give[i * 64] = hw ? kacc[i] : kacc[6 + i]; keep[i] = hw ? kacc[6 + i] : kacc[i]; }
    const int b1 = h ? 384 + 2 * r : 256 + 4 * r, b2 = h ? (r == 0 ? 640 : 672) : 512 + r;
    const int x0 = hw ? b1 + (h ? 64 : 2) : 4 * h + 8 * r;
    const int x1 = hw ? b2 : x0 + 2, d1 = hw ? (h ? (r == 0 ? 32 : 0) : 32) : 1;
    const int x2 = hw ? (h ? 672 : b2 + 64) : b1, d2 = hw ? (h ? 0 : 32) : 1;
    bar();
    const float* take = (const float*)(smem + (w ^ 1) * KG_WAVE) + l;
    const int xi[6] = {x0, x0 + 1, x1, x1 + d1, x2, x2 + d2};
#pragma unroll
    for (int i = 0; i < 6; i++) {
      const float o = take[i * 64];
      cmp[xi[i]] = __builtin_bit_cast(u16, (_Float16)(hw ? o + keep[i] : keep[i] + o));
    }
  }
  STAMP(2);
  bar();
  {
    u32 ra[3][4];
#pragma unroll
    for (int c = 0; c < 2; c++)
#pragma unroll
      for (int j = 0; j < 4; j++) ra[c][j] = (u32)cmp[gi[c][2 * j]] | ((u32)cmp[gi[c][2 * j + 1]] << 16);
    {
      const u32 k8 = hw ? 0u : cmp[512], k4 = hw ? 0u : cmp[511], k0 = hw ? 0u : cmp[510];
      ra[2][0] = k8 * 0x10001u; ra[2][1] = k4 * 0x10001u; ra[2][2] = k4 | (k0 << 16); ra[2][3] = k0 * 0x10001u;
    }
#pragma unroll
    for (int c = 0; c < 2; c++) {
      u32 dw[8], od[7];
#pragma unroll
      for (int j = 0; j < 4; j++) {
        dw[j] = ra[c][j];
        dw[4 + j] = dpp<0x130>(c == 0 ? dpp<0x134>(0u, ra[1][j]) : ra[2][j], ra[c][j]);
      }
#pragma unroll
      for (int i = 0; i < 7; i++) od[i] = __builtin_amdgcn_alignbit(dw[i + 1], dw[i], 16);
      char* tp = T + (hw * 128 + c * 64 + l) * 16;
#pragma unroll
      for (int k = 0; k < 4; k++) {
        *(u32x4*)(tp + (2 * k) * CV_CS) = (u32x4){dw[k], dw[k + 1], dw[k + 2], dw[k + 3]};
        *(u32x4*)(tp + (2 * k + 1) * CV_CS) = (u32x4){od[k], od[k + 1], od[k + 2], od[k + 3]};
      }
    }
    if (t < 8) *(u32x4*)(T + t * CV_CS + 4096) = (u32x4){0u, 0u, 0u, 0u};
  }
  wait_vm<0>();
  bar();
  STAMP(3);
  const char* pa = T + (7 - (rho & 7)) * CV_CS + (255 - (rho >> 3) + q) * 16 - 32 - 256 * hw;
  const char* pb = U + 512 + rho * 32 + q * 16 - 32 - 256 * hw;
  f32x4 acc[8] = {};
  {
    u32x4 A0[8], B0[8], A1[8], B1[8];
    cv_frags(A0, B0, pa, pb);
    SB(); cv_frags(A1, B1, pa - 64, pb - 64); SB();
    cv_mma(A0, B0, acc);
    SB(); cv_frags(A0, B0, pa - 128, pb - 128); SB();
    cv_mma(A1, B1, acc);
    SB(); cv_frags(A1, B1, pa - 192, pb - 192); SB();
    cv_mma(A0, B0, acc);
    cv_mma(A1, B1, acc);
  }
  STAMP(4);
  bar();
  {
    char* box = T + hw * 4096;
#pragma unroll
    for (int i = 0; i < 4; i++) {
      const f32x4 give = hw ? acc[i] : acc[4 + i];
      acc[i] = hw ? acc[4 + i] : acc[i];
      *(f32x4*)(box + i * 1024 + l * 16) = give;
    }
  }
  u32x2 ub[4];
#pragma unroll
  for (int i = 0; i < 4; i++) ub[i] = *(const u32x2*)(U + 512 + (256 * (4 * hw + i) + 16 * rho + 4 * q) * 2);
  bar();
  {
    const char* box = T + (hw ^ 1) * 4096;
    u32x2 yv[4];
#pragma unroll
    for (int i = 0; i < 4; i++) {
      f32x4 v = (acc[i] + *(const f32x4*)(box + i * 1024 + l * 16)) * ks;
      f16x2 u0 = as_h2(ub[i].x), u1 = as_h2(ub[i].y);
      yv[i] = (u32x2){pk_bf16(fmaf((float)u0.x, dk, v.x), fmaf((float)u0.y, dk, v.y)), pk_bf16(fmaf((float)u1.x, dk, v.z), fmaf((float)u1.y, dk, v.w))};
    }
#pragma unroll
    for (int i = 0; i < 4; i += 2) {
      auto sx = __builtin_amdgcn_permlane16_swap(yv[i].x, yv[i + 1].x, false, false);
      auto sy = __builtin_amdgcn_permlane16_swap(yv[i].y, yv[i + 1].y, false, false);
      const u32 off = (u32)(512 * (4 * hw + i + (q & 1)) + 32 * rho + 16 * (q >> 1));
      gstore16(ws, (u32)(WS_YT + d * (SEQ * 2)) + off, (u32x4){sx[0], sy[0], sx[1], sy[1]});
    }
  }
  STAMP(5); STAMP_FLUSH(1);
}

__global__ NOPK __launch_bounds__(256, 2) void k2(const u16* wsp, const u16* bsp, const u16* wout, const u16* lar, const u16* aim, const u16* cre,
                                             const u16* cim, const u16* ldt, const u16* dsk, char* ws) {
  __shared__ __attribute__((aligned(16))) char smem[81920];
  if (blockIdx.x < 256) k2_gemm(smem, blockIdx.x, wsp, bsp, wout, ws);
  else k2_ssm(smem, blockIdx.x - 256, lar, aim, cre, cim, ldt, dsk, ws);
}

#define K3_LEAD 3
struct K3G { const char* wo; const char* hf; u32 woff; u32 hoff[4]; u32 wdst; const char* yt; u32 yoff; u32 ydst; const char* xb; u32 xo; };
template <int I> DEV void k3_issue(const K3G& g, u32x4 (&hf)[7][4], u32x4 (&xv)[2]) {
  if constexpr (I < 7) {
#pragma unroll
    for (int i = 0; i < 4; i++) dma16(g.wo + (size_t)(I * 32 + i * 8) * (DM * 2), g.woff, g.wdst + (I * 4 + i) * 1024);
#pragma unroll
    for (int t = 0; t < 4; t++) gload16<0>(hf[I][t], g.hf + I * 1024, g.hoff[t]);
  } else if constexpr (I == 7) {
    dma16(g.yt, g.yoff, g.ydst);
    dma16(g.yt + 32 * (SEQ * 2), g.yoff, g.ydst + 1024);
    gload16<0>(xv[0], g.xb, g.xo);
    gload16<0>(xv[1], g.xb + 8 * (DM * 2), g.xo);
  }
}
constexpr int k3_cnt(int units) { return units <= 7 ? 8 * units : 60; }
template <int I> DEV void k3_step(const K3G& g, u32x4 (&hf)[7][4], u32x4 (&xv)[2], const char* const (&wp)[4], f32x4 (&acc)[4][4]) {
  wait_vm<k3_cnt(I + K3_LEAD) - 8 * (I + 1)>();
#pragma unroll
  for (int t = 0; t < 4; t++) pin(hf[I][t]);
  u32x4 wf[4];
#pragma unroll
  for (int p = 0; p < 4; p++) wf[p] = tr_frag(wp[p] + I * 4096, wp[p] + I * 4096 + 2048);
  k3_issue<I + K3_LEAD>(g, hf, xv);
#pragma unroll
  for (int p = 0; p < 4; p++)
#pragma unroll
    for (int t = 0; t < 4; t++) acc[t][p] = mfma_bf(wf[p], hf[I][t], acc[t][p]);
  if constexpr (I < 6) k3_step<I + 1>(g, hf, xv, wp, acc);
}
template <int A, int B> DEV void k3_pro(const K3G& g, u32x4 (&hf)[7][4], u32x4 (&xv)[2]) {
  if constexpr (A < B) { k3_issue<A>(g, hf, xv); k3_pro<A + 1, B>(g, hf, xv); }
}
__global__ __launch_bounds__(256, 1) void k3(const u16* x, const u16* wout, u16* out, char* ws) {
  __shared__ __attribute__((aligned(16))) char smem[4 * 28672 + 8192];
  STAMP_DECL; STAMP(0);
  const int b = blockIdx.x, xcd = b & 7, idx = b >> 3;
  const int tp = xcd * 4 + idx / 7, dp = idx % 7;
  const int w = __builtin_amdgcn_readfirstlane(threadIdx.x >> 6), l = threadIdx.x & 63;
  const int rho = l & 15, q = l >> 4, qq = (l >> 2) & 3, pp = l & 3;
  char* const reg = smem + w * 28672;
  char* const Y = smem + 4 * 28672;
  K3G g;
  g.wo = (const char*)wout + (size_t)(w * 224) * (DM * 2) + dp * 128;
  g.hf = ws + WS_HF + ((size_t)tp * 4 * 28 + w * 7) * 1024;
  g.woff = (u32)((l >> 3) * (DM * 2) + (((l & 7) ^ ((l >> 4) << 1)) * 16));
#pragma unroll
  for (int t = 0; t < 4; t++) g.hoff[t] = (u32)((t ^ w) * 28 * 1024 + l * 16);
  g.wdst = lds_addr(reg);
  g.yt = ws + WS_YT + (size_t)(dp * 64) * (SEQ * 2) + tp * 128 + w * 32;
  g.yoff = (u32)((l >> 1) * (SEQ * 2) + (l & 1) * 16);
  g.ydst = lds_addr(Y) + w * 2048;
  g.xo = (u32)((l >> 3) * (DM * 2) + (l & 7) * 16);
  const size_t xb = (size_t)(tp * 64 + w * 16) * (DM * 2) + dp * 128;
  g.xb = (const char*)x + xb;
  u32x4 hf[7][4], xv[2];
  k3_pro<0, K3_LEAD>(g, hf, xv);
  const char* wp[4];
  const int sg = ((q & 1) << 1) | (qq >> 1);
#pragma unroll
  for (int p = 0; p < 4; p++) wp[p] = reg + (4 * q + qq) * 128 + ((p ^ sg) * 32) + 8 * pp;
  f32x4 acc[4][4] = {};
  STAMP(1);
  k3_step<0>(g, hf, xv, wp, acc);
  STAMP(2);
#pragma unroll
  for (int t = 1; t < 4; t++)
#pragma unroll
    for (int p = 0; p < 4; p++) *(f32x4*)(reg + t * 4352 + rho * 272 + p * 64 + q * 16) = acc[t][p];
  wait_vm<0>();
  pin(xv[0]); pin(xv[1]);
#pragma unroll
  for (int p = 0; p < 4; p++) {
    u32x2 yv = tr_read(Y + w * 2048 + (16 * p + 4 * q + qq) * 32 + 8 * pp);
    *(f32x4*)(reg + rho * 272 + p * 64 + q * 16) = acc[0][p] + (f32x4){bflo(yv.x), bfhi(yv.x), bflo(yv.y), bfhi(yv.y)};
  }
  bar();
  STAMP(3);
#pragma unroll
  for (int i = 0; i < 2; i++) {
    f32x4 a = {bflo(xv[i].x), bfhi(xv[i].x), bflo(xv[i].y), bfhi(xv[i].y)}, c = {bflo(xv[i].z), bfhi(xv[i].z), bflo(xv[i].w), bfhi(xv[i].w)};
#pragma unroll
    for (int t = 0; t < 4; t++) {
      const char* rp = smem + (w ^ t) * 28672 + t * 4352 + (8 * i + (l >> 3)) * 272 + (l & 7) * 32;
      a += *(const f32x4*)(rp); c += *(const f32x4*)(rp + 16);
    }
    u32x4 o = {pk_bf16(a.x, a.y), pk_bf16(a.z, a.w), pk_bf16(c.x, c.y), pk_bf16(c.z, c.w)};
    gstore16(out, (u32)xb + i * 8 * (DM * 2) + g.xo, o);
  }
  STAMP(4); STAMP_FLUSH(2);
}

#define K1_GRID 256
#define K2_GRID 480
extern "C" void kernel_launch(void* const* d_in, const int* in_sizes, int n_in, void* d_out, int out_size, void* d_ws, size_t ws_size,
                              hipStream_t stream) {
  const u16* x = (const u16*)d_in[0];
  char* ws = (char*)d_ws;
  k1<<<K1_GRID, 512, 0, stream>>>(x, (const u16*)d_in[8], (const u16*)d_in[9], (const u16*)d_in[11], ws);
  k2<<<K2_GRID, 256, 0, stream>>>((const u16*)d_in[9], (const u16*)d_in[10], (const u16*)d_in[11], (const u16*)d_in[1], (const u16*)d_in[2],
                              (const u16*)d_in[3], (const u16*)d_in[4], (const u16*)d_in[5], (const u16*)d_in[6], ws);
  k3<<<224, 256, 0, stream>>>(x, (const u16*)d_in[11], (u16*)d_out, ws);
}
```

```cpp
#include <hip/hip_runtime.h>
#include <stdint.h>

typedef unsigned short u16;
typedef unsigned int u32;
typedef unsigned long long u64;
typedef __attribute__((ext_vector_type(4))) u32 u32x4;
typedef __attribute__((ext_vector_type(2))) u32 u32x2;
typedef __attribute__((ext_vector_type(4))) float f32x4;
typedef __attribute__((ext_vector_type(2))) float f32x2;
typedef __attribute__((ext_vector_type(16))) float f32x16;
typedef __attribute__((ext_vector_type(8))) __bf16 bf16x8;
typedef __attribute__((ext_vector_type(2))) __bf16 bf16x2;
typedef __attribute__((ext_vector_type(8))) _Float16 f16x8;
typedef __attribute__((ext_vector_type(2))) _Float16 f16x2;
typedef __attribute__((ext_vector_type(4))) short s16x4;
#define LDSAS __attribute__((address_space(3)))
#define GAS __attribute__((address_space(1)))
#define DEV __device__ __forceinline__
#if defined(__HIP_DEVICE_COMPILE__)
#define NOPK __attribute__((target("no-packed-fp32-ops")))
#else
#define NOPK
#endif
#define SB() __builtin_amdgcn_sched_barrier(0)

constexpr int SEQ = 2048, DM = 448, NMODE = 256, FF = 1792, FH = 896;

constexpr size_t WS_Z2F = 0;
constexpr size_t WS_Z1F = 4u << 20;
constexpr size_t WS_HF  = 8u << 20;
constexpr size_t WS_UT  = 12u << 20;
constexpr size_t WS_YT  = 14u << 20;
constexpr size_t WS_GR  = 16u << 20;
constexpr size_t WS_EP  = 17u << 20;

#define STAMP(i)
#define STAMP_DECL
#define STAMP_ARG
#define STAMP_PASS
#define STAMP_FLUSH(kern)
DEV float bf2f(u32 v) { return __uint_as_float(v << 16); }
DEV float bflo(u32 v) { return __uint_as_float(v << 16); }
DEV float bfhi(u32 v) { return __uint_as_float(v & 0xffff0000u); }
DEV u32 pk_bf16(float a, float b) { bf16x2 v = {(__bf16)a, (__bf16)b}; return __builtin_bit_cast(u32, v); }
DEV u32 pk_f16(float a, float b) { f16x2 v = {(_Float16)a, (_Float16)b}; return __builtin_bit_cast(u32, v); }
DEV f16x2 as_h2(u32 v) { return __builtin_bit_cast(f16x2, v); }
DEV float rbf(float a) { return __uint_as_float(pk_bf16(a, a) << 16); }
DEV f32x4 mfma_bf(u32x4 a, u32x4 b, f32x4 c) {
  return __builtin_amdgcn_mfma_f32_16x16x32_bf16(__builtin_bit_cast(bf16x8, a), __builtin_bit_cast(bf16x8, b), c, 0, 0, 0);
}
DEV f32x4 mfma_h(u32x4 a, u32x4 b, f32x4 c) {
  return __builtin_amdgcn_mfma_f32_16x16x32_f16(__builtin_bit_cast(f16x8, a), __builtin_bit_cast(f16x8, b), c, 0, 0, 0);
}
DEV f32x16 mfma32_h(u32x4 a, u32x4 b, f32x16 c) {
  return __builtin_amdgcn_mfma_f32_32x32x16_f16(__builtin_bit_cast(f16x8, a), __builtin_bit_cast(f16x8, b), c, 0, 0, 0);
}
DEV u32x2 tr_read(const char* p) {
  s16x4 v = __builtin_amdgcn_ds_read_tr16_b64_v4i16((LDSAS s16x4*)p);
  return __builtin_bit_cast(u32x2, v);
}
DEV u32x4 tr_frag(const char* p0, const char* p1) {
  u32x2 a = tr_read(p0), b = tr_read(p1);
  return (u32x4){a.x, a.y, b.x, b.y};
}
#define LDMOD ""
#define DMAMOD ""
DEV u32 lds_addr(const void* p) { return (u32)(uintptr_t)(LDSAS const char*)p; }
DEV void dma16(const void* sbase, u32 voff, u32 ldst) {
  asm volatile("s_mov_b32 m0, %2\n\ts_nop 0\n\tglobal_load_lds_dwordx4 %0, %1 " DMAMOD ::"v"(voff), "s"(sbase), "s"(ldst) : "memory");
}
template <int OFF> DEV void gload16(u32x4& d, const void* sbase, u32 voff) {
  asm volatile("global_load_dwordx4 %0, %1, %2 offset:%3 " LDMOD : "=v"(d) : "v"(voff), "s"(sbase), "i"(OFF) : "memory");
}
DEV void gstore16(void* sbase, u32 voff, u32x4 d) {
  asm volatile("global_store_dwordx4 %0, %1, %2 sc1\n\ts_nop 1" ::"v"(voff), "v"(d), "s"(sbase) : "memory");
}
DEV void gstore8(void* sbase, u32 voff, u32x2 d) {
  asm volatile("global_store_dwordx2 %0, %1, %2 sc1" ::"v"(voff), "v"(d), "s"(sbase) : "memory");
}
DEV void gstore16p(void* sbase, u32 voff, u32x4 d) {
  asm volatile("global_store_dwordx4 %0, %1, %2\n\ts_nop 1" ::"v"(voff), "v"(d), "s"(sbase) : "memory");
}
#define Z2STORE gstore16
DEV void gload4(u32& d, const void* sbase, u32 voff) {
  asm volatile("global_load_dword %0, %1, %2" : "=v"(d) : "v"(voff), "s"(sbase) : "memory");
}
template <int N> DEV void wait_vm() { asm volatile("s_waitcnt vmcnt(%0)" ::"i"(N) : "memory"); }
DEV void touch(u32& d, const void* p) { asm volatile("global_load_dword %0, %1, off" : "=v"(d) : "v"(p) : "memory"); }
DEV void keep(u32& a) { asm volatile("" : "+v"(a)); }
DEV void pin(u32x4& a) { asm volatile("" : "+v"(a)); }
DEV void bar() { asm volatile("s_waitcnt lgkmcnt(0)\n\ts_barrier" ::: "memory"); }
DEV float xsum16(float v) { auto r = __builtin_amdgcn_permlane16_swap(__float_as_uint(v), __float_as_uint(v), false, false); return __uint_as_float(r[0]) + __uint_as_float(r[1]); }
DEV float xsum32(float v) { auto r = __builtin_amdgcn_permlane32_swap(__float_as_uint(v), __float_as_uint(v), false, false); return __uint_as_float(r[0]) + __uint_as_float(r[1]); }
DEV f32x2 ln_scalars(float sum, float sq, float inv_n) {
  float m = sum * inv_n;
  float v = fmaxf(sq * inv_n - m * m, 0.f);
  float sd = rbf(__builtin_amdgcn_sqrtf(rbf(rbf(v) + 1e-5f)));
  return (f32x2){rbf(m), __builtin_amdgcn_rcpf(sd)};
}
DEV f32x2 gelu2(f32x2 x) {
  const float c1 = -2.3022081983f, c2 = -0.1029432396f;
  f32x2 t = x * x;
  t = t * c2 + c1;
  t = t * x;
  f32x2 e = {__builtin_amdgcn_exp2f(t.x), __builtin_amdgcn_exp2f(t.y)};
  e = e + 1.0f;
  f32x2 r = {__builtin_amdgcn_rcpf(e.x), __builtin_amdgcn_rcpf(e.y)};
  return x * r;
}
DEV f32x2 cmul(f32x2 a, f32x2 b) { return (f32x2){fmaf(a.x, b.x, -(a.y * b.y)), fmaf(a.x, b.y, a.y * b.x)}; }
DEV f32x2 csqr(f32x2 a) { return (f32x2){a.x * a.x - a.y * a.y, 2.f * a.x * a.y}; }

#define K1_PKACT
#define K1_PUBDELAY 8
#define TSTRIDE 4096u
#define K1_PRIO 2
#define K1_PF 4
constexpr int k1_slot(int u) { return u < 9 ? u : u - 9; }
constexpr int K1_X = 73728, K1_UT = K1_X + 65536, K1_CS = K1_UT + 32 * 272, K1_LN = K1_CS + 512, K1_LN1 = K1_LN + 1024, K1_CNT = K1_LN1 + 1024, K1_DUMP = K1_CNT + 64, K1_LDS = K1_DUMP + 4096;

typedef LDSAS char* lptr;
DEV lptr pinned(lptr p) { asm volatile("" : "+v"(p)); return p; }
DEV u32x4 lread16(lptr p) { return *(LDSAS u32x4*)p; }
DEV u32x4 ltr_frag(lptr p0, lptr p1) {
  s16x4 a = __builtin_amdgcn_ds_read_tr16_b64_v4i16((LDSAS s16x4*)p0), b = __builtin_amdgcn_ds_read_tr16_b64_v4i16((LDSAS s16x4*)p1);
  u32x2 x = __builtin_bit_cast(u32x2, a), y = __builtin_bit_cast(u32x2, b);
  return (u32x4){x.x, x.y, y.x, y.y};
}
DEV void mma(f32x4& c, const u32x4& a, const u32x4& b) { asm volatile("v_mfma_f32_16x16x32_bf16 %0, %1, %2, %0" : "+v"(c) : "v"(a), "v"(b)); }
DEV void mma0(f32x4& c, const u32x4& a, const u32x4& b) { asm volatile("v_mfma_f32_16x16x32_bf16 %0, %1, %2, 0" : "=&v"(c) : "v"(a), "v"(b)); }
template <bool FIRST> DEV void mmaf(f32x4& c, const u32x4& a, const u32x4& b) { if constexpr (FIRST) mma0(c, a, b); else mma(c, a, b); }
DEV void mma_drain() { SB(); asm volatile("s_nop 7\n\ts_nop 1" ::: "memory"); SB(); }
DEV void sbar() { asm volatile("s_barrier" ::: "memory"); }

struct K1L { const char* win; const char* xb; u32 xoff[2]; u32 woff[2]; u32 wdst; u32 xdst; int m0; };
template <int U> DEV void k1_unit(const K1L& g) {
  int line = U % 7 + g.m0; line = line >= 7 ? line - 7 : line;
  if constexpr (U < 7) {
#pragma unroll
    for (int i = 0; i < 4; i++) dma16(g.xb + line * 128 + (i >> 1) * 16 * (DM * 2), g.xoff[i & 1], g.xdst + (U & 3) * 16384 + i * 1024);
  }
#pragma unroll
  for (int i = 0; i < 2; i++) dma16(g.win + (size_t)line * 64 * (FF * 2) + (U < 7 ? FH * 2 : 0), g.woff[i], g.wdst + k1_slot(U) * 8192 + i * 1024);
}
struct K1C { lptr str[4]; lptr wp[4]; };
DEV void k1_xr(const K1C& g, u32x4 (&xf)[4], int M) {
#pragma unroll
  for (int i = 0; i < 4; i++) xf[i] = lread16(g.str[i] + (M & 3) * 16384);
}
template <int U> DEV u32x4 k1_wf(const K1C& g, int f) {
  if constexpr (k1_slot(U) < 8) return ltr_frag(g.wp[f & 3] + k1_slot(U) * 8192 + (f >> 2) * 4096, g.wp[f & 3] + k1_slot(U) * 8192 + (f >> 2) * 4096 + 512);
  else { lptr p = pinned(g.wp[f & 3] + 65536); return ltr_frag(p + (f >> 2) * 4096, p + (f >> 2) * 4096 + 512); }
}
template <int U> DEV void k1_main(const K1C& g, const u32x4 (&xf)[4], u32x4 (&wf)[8], f32x4 (&acc)[2][4], f32x4& cs, const u32x4& ones) {
#pragma unroll
  for (int f = 0; f < 8; f++) {
    if (U % 7 == 0 && f < 4) {
      if (f == 0) mma0(cs, wf[f], ones);
      mma0(acc[0][f & 3], wf[f], xf[0]);
      mma0(acc[1][f & 3], wf[f], xf[1]);
    } else {
      if ((f & 3) == 0) mma(cs, wf[f], ones);
      mma(acc[0][f & 3], wf[f], xf[(f >> 2) * 2]);
      mma(acc[1][f & 3], wf[f], xf[(f >> 2) * 2 + 1]);
    }
    SB(); if constexpr (U % 7 < 6) wf[f] = k1_wf<U + 1>(g, f); SB();
  }
}
template <int M>
DEV void k1_stage(const K1C& g, u32x4 (&xf)[7][4], u32x4 (&wf)[8], f32x4 (&acc)[2][4], f32x4& cs, f32x4 (&rsum)[2], f32x4 (&gram)[2], const u32x4& ones) {
  asm volatile("s_waitcnt lgkmcnt(15)\n\ts_barrier" ::: "memory");
  if constexpr (M < 6) k1_xr(g, xf[M + 1], M + 1);
  SB();
#pragma unroll
  for (int i = 0; i < 4; i++) {
    if (M == 0 && i < 2) { mma0(rsum[i & 1], ones, xf[M][i]); mma0(gram[i & 1], xf[M][i], xf[M][i]); }
    else { mma(rsum[i & 1], ones, xf[M][i]); mma(gram[i & 1], xf[M][i], xf[M][i]); }
  }
  k1_main<M>(g, xf[M], wf, acc, cs, ones);
  if constexpr (M < 6) k1_stage<M + 1>(g, xf, wf, acc, cs, rsum, gram, ones);
}
template <int M> DEV void k1_z1loop(const K1C& g, u32x4 (&xf)[7][4], u32x4 (&wf)[8], f32x4 (&acc)[2][4], f32x4& cs, const u32x4& ones) {
  k1_main<M + 7>(g, xf[M], wf, acc, cs, ones);
  if constexpr (M < 6) k1_z1loop<M + 1>(g, xf, wf, acc, cs, ones);
}
DEV void k1_act(f32x4 (&acc)[2][4], const char* csb, int w, const f32x2 (&ln1)[2]) {
#pragma unroll
  for (int p = 0; p < 4; p++) {
    const f32x4 c = *(const f32x4*)(csb + (p ^ w) * 64);
#pragma unroll
    for (int t = 0; t < 2; t++) {
      f32x2 a = (acc[t][p].xy - c.xy * ln1[t].x) * ln1[t].y;
      f32x2 b = (acc[t][p].zw - c.zw * ln1[t].x) * ln1[t].y;
      a = gelu2(a); b = gelu2(b);
      acc[t][p] = (f32x4){a.x, a.y, b.x, b.y};
    }
  }
}
DEV void act4_stats(f32x4& x, const f32x4& c, float m, float rs, float c1, float& s, float& s2) {
  float x0 = x.x, x1 = x.y, x2 = x.z, x3 = x.w, t0, t1, t2, t3;
  asm("v_fma_f32 %0, -%10, %14, %0\n\tv_fma_f32 %1, -%11, %14, %1\n\tv_fma_f32 %2, -%12, %14, %2\n\tv_fma_f32 %3, -%13, %14, %3\n\t"
      "v_mul_f32 %0, %0, %15\n\tv_mul_f32 %1, %1, %15\n\tv_mul_f32 %2, %2, %15\n\tv_mul_f32 %3, %3, %15\n\t"
      "v_mul_f32 %4, %0, %0\n\tv_mul_f32 %5, %1, %1\n\tv_mul_f32 %6, %2, %2\n\tv_mul_f32 %7, %3, %3\n\t"
      "v_fmamk_f32 %4, %4, 0xbdd2d3e8, %16\n\tv_fmamk_f32 %5, %5, 0xbdd2d3e8, %16\n\tv_fmamk_f32 %6, %6, 0xbdd2d3e8, %16\n\tv_fmamk_f32 %7, %7, 0xbdd2d3e8, %16\n\t"
      "v_mul_f32 %4, %4, %0\n\tv_mul_f32 %5, %5, %1\n\tv_mul_f32 %6, %6, %2\n\tv_mul_f32 %7, %7, %3\n\t"
      "v_exp_f32 %4, %4\n\tv_exp_f32 %5, %5\n\tv_exp_f32 %6, %6\n\tv_exp_f32 %7, %7\n\t"
      "v_add_f32 %4, 1.0, %4\n\tv_add_f32 %5, 1.0, %5\n\tv_add_f32 %6, 1.0, %6\n\tv_add_f32 %7, 1.0, %7\n\t"
      "v_rcp_f32 %4, %4\n\tv_rcp_f32 %5, %5\n\tv_rcp_f32 %6, %6\n\tv_rcp_f32 %7, %7\n\t"
      "v_mul_f32 %0, %0, %4\n\tv_mul_f32 %1, %1, %5\n\tv_mul_f32 %2, %2, %6\n\tv_mul_f32 %3, %3, %7\n\t"
      "v_add_f32 %4, %0, %1\n\tv_fmac_f32 %9, %0, %0\n\tv_add_f32 %5, %2, %3\n\tv_fmac_f32 %9, %1, %1\n\t"
      "v_add_f32 %8, %8, %4\n\tv_fmac_f32 %9, %2, %2\n\tv_add_f32 %8, %8, %5\n\tv_fmac_f32 %9, %3, %3"
      : "+v"(x0), "+v"(x1), "+v"(x2), "+v"(x3), "=&v"(t0), "=&v"(t1), "=&v"(t2), "=&v"(t3), "+v"(s), "+v"(s2)
      : "v"(c.x), "v"(c.y), "v"(c.z), "v"(c.w), "v"(m), "v"(rs), "v"(c1));
  x = (f32x4){x0, x1, x2, x3};
}
DEV int lane_id() { int l = __builtin_amdgcn_mbcnt_hi(~0u, __builtin_amdgcn_mbcnt_lo(~0u, 0u)); asm volatile("" : "+v"(l)); return l; }
DEV void quad_sync(lptr cnt, int l) {
  asm volatile("s_waitcnt lgkmcnt(0)" ::: "memory");
  if (l == 0) __hip_atomic_fetch_add((LDSAS u32*)cnt, 1u, __ATOMIC_RELAXED, __HIP_MEMORY_SCOPE_WORKGROUP);
  while (__builtin_amdgcn_readfirstlane(*(volatile LDSAS u32*)cnt) < 4u) __builtin_amdgcn_s_sleep(0);
  asm volatile("" ::: "memory");
}
DEV void lbar0() { asm volatile("s_waitcnt vmcnt(6) lgkmcnt(0)\n\ts_barrier" ::: "memory"); }

struct Extra { const char* p[8]; };
DEV void k1_body(char* smem, const u16* x, const u16* win, const u16* wsp, const u16* wout, char* ws, const Extra& ex) {
  STAMP_DECL; STAMP(0);
  const int b = blockIdx.x, xcd = b & 7, idx = b >> 3;
  const int w8 = __builtin_amdgcn_readfirstlane(threadIdx.x >> 6), l = threadIdx.x & 63, w = w8 & 3;
  if (b >= 224) {
    const int j = (idx - 28) * 8 + w8;
    const char* p = (const char*)wsp + ((size_t)xcd * 1024 + 112 * K1_PF) * 1024;
    constexpr int REST = 1024 - 112 * K1_PF;
    {
      u32 d0, d1, d2, d3;
      const int n = j * 64 + l;
      const char* p = n < 448 ? ex.p[0] + n * 4096 : (n < 672 ? ex.p[1 + (n - 448) / 56] + ((n - 448) % 56) * 4096 : ex.p[5 + (n % 3)]);
      asm volatile("" : "+v"(p));
      gload4(d0, ws, (u32)((n * TSTRIDE) % (18u << 20)));
      gload4(d1, ws, (u32)(((n + 2048) * TSTRIDE) % (18u << 20)));
      gload4(d2, ws, (u32)(((n + 4096) * TSTRIDE) % (18u << 20)));
      touch(d3, p);
      wait_vm<0>();
      keep(d0); keep(d1); keep(d2); keep(d3);
    }
#pragma unroll 1
    for (int i = j; i < REST; i += 32) dma16(p + (size_t)i * 1024, l * 16, lds_addr(smem) + w8 * 1024);
    const char* po = (const char*)wout + (size_t)(xcd * 98) * 1024;
#pragma unroll
    for (int k = 0; k < 4; k++) { int c = j + 32 * k; c = c < 98 ? c : 97; dma16(po + c * 1024, l * 16, lds_addr(smem) + w8 * 1024); }
    STAMP(1); STAMP_FLUSH(0);
    return;
  }
  const int i7 = (idx * 37) >> 8;
  const int panel = (xcd >> 1) * 4 + i7, sib = (xcd & 1) * 7 + idx - 7 * i7;
  const lptr lds = (lptr)smem;
  K1L g;
  if (w8 >= 4) {
    g.win = (const char*)win + (size_t)(16 * w) * (FF * 2) + sib * 128;
    g.xb = (const char*)x + (size_t)(panel * 128 + w * 32) * (DM * 2);
    g.xoff[0] = (u32)((l >> 3) * (DM * 2) + (((l & 7) ^ (l >> 4)) * 16));
    g.xoff[1] = (u32)((8 + (l >> 3)) * (DM * 2) + (((l & 7) ^ ((l >> 4) | 4)) * 16));
    g.woff[0] = (u32)((l >> 3) * (FF * 2) + (((l & 7) ^ (((l >> 4) & 1) << 1)) * 16));
    g.woff[1] = (u32)((8 + (l >> 3)) * (FF * 2) + (((l & 7) ^ (((l >> 4) & 1) << 1) ^ 4) * 16));
    g.xdst = lds_addr(smem) + K1_X + w * 4096;
    g.wdst = lds_addr(smem) + w * 2048;
    g.m0 = sib >> 1;
    k1_unit<0>(g); k1_unit<1>(g);
  }
  K1C c;
  {
    const int rho = l & 15, q = l >> 4, qq = (l >> 2) & 3, pp = l & 3;
#pragma unroll
    for (int i = 0; i < 4; i++) c.str[i] = pinned(lds + K1_X + w * 4096 + (i & 1) * 2048 + rho * 128 + (((4 * (i >> 1) + q) ^ (rho >> 1)) * 16));
    const int sg = (qq >> 1) | ((q & 1) << 1);
#pragma unroll
    for (int f = 0; f < 4; f++) c.wp[f] = lds + (8 * q + qq) * 128 + (((f ^ w) ^ sg) * 32) + 8 * pp;
  }
  u32x4 ones = {0x3f803f80u, 0x3f803f80u, 0x3f803f80u, 0x3f803f80u};
  pin(ones);
  u32x4 xf[7][4], wf[8];
  f32x4 acc[2][4], cs;
  if (w8 >= 4) {
    lbar0();
    k1_unit<2>(g); k1_xr(c, xf[0], 0); lbar0();
    k1_unit<3>(g); k1_xr(c, xf[1], 1); lbar0();
    k1_unit<4>(g); k1_xr(c, xf[2], 2); lbar0();
    k1_unit<5>(g); k1_xr(c, xf[3], 3); lbar0();
    k1_unit<6>(g); k1_xr(c, xf[4], 4); lbar0();
    k1_unit<7>(g); k1_unit<8>(g); k1_unit<9>(g); k1_xr(c, xf[5], 5); lbar0();
    k1_unit<10>(g); k1_unit<11>(g); k1_unit<12>(g); k1_xr(c, xf[6], 6);
    asm volatile("s_waitcnt lgkmcnt(0)\n\ts_barrier" ::: "memory");
    k1_unit<13>(g);
    STAMP(1);
#pragma unroll
    for (int i = 0; i < K1_PF; i++)
      dma16((const char*)wsp + ((size_t)xcd * 1024 + (idx * 4 + w) * K1_PF + i) * 1024, l * 16, lds_addr(smem) + K1_DUMP + w * 1024);
    wait_vm<K1_PF>(); sbar();
    STAMP(2);
    f32x2 ln1[2];
    {
    const int rho = l & 15, q = l >> 4;
#pragma unroll
    for (int t = 0; t < 2; t++) ln1[t] = *(const f32x2*)(smem + K1_LN1 + w * 256 + (t * 16 + rho) * 8);
#pragma unroll
    for (int f = 0; f < 8; f++) wf[f] = k1_wf<7>(c, f);
#pragma unroll
    for (int t = 0; t < 2; t++) {
      const u32x4 va = xf[0][t], vb = xf[0][2 + t];
      const u32 msk = (sib & 1) ? 0xffffffffu : 0u;
      const u32x4 v = (va & ~msk) | (vb & msk);
      u16* ub = (u16*)(smem + K1_UT + (8 * q) * 272 + (w * 32 + t * 16 + rho) * 2);
#pragma unroll
      for (int j = 0; j < 4; j++) {
        float a = rbf(rbf(bflo(v[j]) - ln1[t].x) * ln1[t].y), bb = rbf(rbf(bfhi(v[j]) - ln1[t].x) * ln1[t].y);
        u32 o = pk_f16(a, bb);
        ub[(2 * j) * 136] = (u16)o;
        ub[(2 * j + 1) * 136] = (u16)(o >> 16);
      }
    }
}
    SB();
    k1_z1loop<0>(c, xf, wf, acc, cs, ones);
    mma_drain();
    const int l = lane_id(), rho = l & 15, q = l >> 4;
    if (rho == 0) *(f32x4*)(smem + K1_CS + 256 + w * 64 + q * 16) = cs;
    STAMP(3);
    quad_sync(lds + K1_CNT, l);
    STAMP(4);
#pragma unroll
    for (int i = 0; i < 2; i++) {
      const u32x4 uo = *(const u32x4*)(smem + K1_UT + (w * 8 + i * 4 + q) * 272 + rho * 16);
      gstore16(ws, (u32)(WS_UT + (sib * 32 + w * 8 + i * 4 + q) * (SEQ * 2) + panel * 256 + rho * 16), uo);
    }
    k1_act(acc, smem + K1_CS + 256 + q * 16, w, ln1);
#pragma unroll
    for (int t = 0; t < 2; t++)
#pragma unroll
      for (int cc = 0; cc < 2; cc++) {
        u32x2 a = {pk_bf16(acc[t][2 * cc].x, acc[t][2 * cc].y), pk_bf16(acc[t][2 * cc].z, acc[t][2 * cc].w)};
        u32x2 bb = {pk_bf16(acc[t][2 * cc + 1].x, acc[t][2 * cc + 1].y), pk_bf16(acc[t][2 * cc + 1].z, acc[t][2 * cc + 1].w)};
        const u32x4 o = (w & 1) ? (u32x4){bb.x, bb.y, a.x, a.y} : (u32x4){a.x, a.y, bb.x, bb.y};
        gstore16(ws, (u32)(WS_Z1F + ((panel * 8 + w * 2 + t) * 28 + sib * 2 + (cc ^ (w >> 1))) * 1024 + l * 16), o);
      }
    STAMP(5); STAMP_FLUSH(0);
    return;
  }
  const int grp = panel * 4 + w;
  u32 tag;
  asm volatile("s_load_dword %0, %1, 0x0\n\ts_waitcnt lgkmcnt(0)" : "=s"(tag) : "s"(ws + WS_EP + grp * 256) : "memory");
  tag += 1u;
  f32x4 rsum[2], gram[2];
  if (threadIdx.x < 2) *(u32*)(smem + K1_CNT + threadIdx.x * 4) = 0u;
  bar();
  STAMP(2);
  k1_xr(c, xf[0], 0);
#pragma unroll
  for (int f = 0; f < 8; f++) wf[f] = k1_wf<0>(c, f);
  k1_stage<0>(c, xf, wf, acc, cs, rsum, gram, ones);
  mma_drain();
  const int rho = l & 15, q = l >> 4, qq = (l >> 2) & 3, pp = l & 3;
  if (rho == 0) *(f32x4*)(smem + K1_CS + w * 64 + q * 16) = cs;
  STAMP(3);
  f32x2 ln1[2];
#pragma unroll
  for (int t = 0; t < 2; t++) {
    float d = (rho & 2) ? ((rho & 1) ? gram[t].w : gram[t].z) : ((rho & 1) ? gram[t].y : gram[t].x);
    d = ((rho >> 2) == q) ? d : 0.f;
    d = xsum32(xsum16(d));
    ln1[t] = ln_scalars(rsum[t].x, d, 1.f / DM);
    if (q == 0) *(f32x2*)(smem + K1_LN1 + w * 256 + (t * 16 + rho) * 8) = ln1[t];
  }
  bar();
  __builtin_amdgcn_s_setprio(K1_PRIO);
  STAMP(4);
  GAS u64* gr = (GAS u64*)(ws + WS_GR) + (size_t)grp * 14 * 64;
  {
    float st[2][2] = {{0.f, 0.f}, {0.f, 0.f}};
    k1_act(acc, smem + K1_CS + q * 16, w, ln1);
#pragma unroll
    for (int t = 0; t < 2; t++) {
      f32x2 s = {0.f, 0.f}, s2 = {0.f, 0.f};
#pragma unroll
      for (int p = 0; p < 4; p++) {
        s += acc[t][p].xy; s += acc[t][p].zw;
        s2 += acc[t][p].xy * acc[t][p].xy; s2 += acc[t][p].zw * acc[t][p].zw;
      }
      st[t][0] = s.x + s.y; st[t][1] = s2.x + s2.y;
    }
#pragma unroll
    for (int t = 0; t < 2; t++) { st[t][0] = xsum32(xsum16(st[t][0])); st[t][1] = xsum32(xsum16(st[t][1])); }
    const float v = (q & 2) ? ((q & 1) ? st[1][1] : st[0][1]) : ((q & 1) ? st[1][0] : st[0][0]);
    __hip_atomic_store(gr + sib * 64 + l, ((u64)tag << 32) | __float_as_uint(v), __ATOMIC_RELAXED, __HIP_MEMORY_SCOPE_AGENT);
  }
  STAMP(5);
  __builtin_amdgcn_s_sleep(K1_PUBDELAY);
  STAMP(6);
  u64 rv[14];
#pragma unroll
  for (int i = 0; i < 14; i++) rv[i] = __hip_atomic_load(gr + i * 64 + l, __ATOMIC_RELAXED, __HIP_MEMORY_SCOPE_AGENT);
  STAMP(7);
  float tot;
  for (u32 spins = 0;; spins++) {
    bool ok = true; tot = 0.f;
#pragma unroll
    for (int i = 0; i < 14; i++) { ok &= (u32)(rv[i] >> 32) == tag; tot += __uint_as_float((u32)rv[i]); }
    if (__all(ok) || spins > (1u << 20)) break;
#pragma unroll
    for (int i = 0; i < 14; i++) rv[i] = __hip_atomic_load(gr + i * 64 + l, __ATOMIC_RELAXED, __HIP_MEMORY_SCOPE_AGENT);
  }
  STAMP(8);
  if (sib == 0 && l == 0) __hip_atomic_store((GAS u32*)(ws + WS_EP + grp * 256), tag, __ATOMIC_RELAXED, __HIP_MEMORY_SCOPE_AGENT);
  {
    auto bb = __builtin_amdgcn_permlane32_swap(__float_as_uint(tot), __float_as_uint(tot), false, false);
    const f32x2 lr = ln_scalars(__uint_as_float(bb[0]), __uint_as_float(bb[1]), 1.f / FH);
    auto lm = __builtin_amdgcn_permlane16_swap(__float_as_uint(lr.x), __float_as_uint(lr.x), false, false);
    auto ls = __builtin_amdgcn_permlane16_swap(__float_as_uint(lr.y), __float_as_uint(lr.y), false, false);
    char* const stg = smem + K1_X + w * 8192;
#pragma unroll
    for (int t = 0; t < 2; t++) {
      const float m2 = __uint_as_float(lm[t]), r2 = __uint_as_float(ls[t]);
#pragma unroll
      for (int p = 0; p < 4; p++) {
        f32x2 a = (acc[t][p].xy - m2) * r2, b2 = (acc[t][p].zw - m2) * r2;
        *(u32x2*)(stg + (16 * t + rho) * 136 + p * 32 + q * 8) = (u32x2){pk_bf16(a.x, a.y), pk_bf16(b2.x, b2.y)};
      }
    }
#pragma unroll
    for (int p = 0; p < 4; p++) {
      const char* rp = stg + (8 * q + qq) * 136 + p * 32 + pp * 8;
      u32x4 o = tr_frag(rp, rp + 4 * 136);
      const int ft = sib * 4 + (p ^ w);
      Z2STORE(ws, (u32)(WS_Z2F + (((ft / 7) * 64 + grp) * 7 + ft % 7) * 1024 + l * 16), o);
    }
  }
  STAMP(9); STAMP_FLUSH(0);
}
#define K1_ATTR
__global__ K1_ATTR __launch_bounds__(512, 2) void k1(const u16* x, const u16* win, const u16* wsp, const u16* wout, char* ws, Extra ex) {
  __shared__ __attribute__((aligned(16))) char smem[K1_LDS];
  k1_body(smem, x, win, wsp, wout, ws, ex);
}

#define K2_R 18
#define K2_PF 7
#define K2_TD 0
constexpr bool k2_touch_at(int tau) { return K2_TD > 0 && tau % 14 == 0 && tau / 7 + K2_TD <= 14; }
constexpr int K2_EPI = 5, K2_EPI_AT = 112 - K2_R + 2, K2_PRO = 16 + K2_R + (K2_TD > 0 ? 2 : 0);
constexpr int k2_apiece(int tau) {
  int s = tau / 7, f = tau % 7;
  int a = (s + 3) / 2 + ((s & 1) ? 0 : 1) - ((s & 1) ? 0 : 1);
  if (s & 1) { a = (s + 3) / 2; return (a <= 7 && (f & 1) == 0) ? 8 * a + f / 2 : -1; }
  a = (s + 2) / 2;
  return (a >= 2 && a <= 7 && f < 4) ? 8 * a + 4 + f : -1;
}
constexpr int k2_ops_after(int tau) { return (tau + K2_R < 112 ? 1 : 0) + (k2_apiece(tau) >= 0 ? 1 : 0) + (tau == K2_EPI_AT ? K2_EPI + K2_PF : 0) + (k2_touch_at(tau) ? 1 : 0); }
constexpr int k2_issued(int tau) { int n = K2_PRO; for (int i = 0; i < tau; i++) n += k2_ops_after(i); return n; }
constexpr int k2_pos_B(int n) { return n < 14 ? 8 + n + 1 : (n < K2_R ? 16 + n + 1 : k2_issued(n - K2_R) + 1); }
constexpr int k2_pos_A(int a) { return a == 0 ? 8 : (a == 1 ? 30 : k2_issued(7 * (2 * a - 2) + 4)); }
constexpr int k2_wait(int tau) {
  int need = k2_pos_B(tau);
  int s = tau / 7, f = tau % 7;
  if ((s & 1) && f == 2 && (s + 1) / 2 <= 7) { int a = k2_pos_A((s + 1) / 2); need = a > need ? a : need; }
  return k2_issued(tau) - need;
}
#define K2_MFMA mfma_bf
struct K2G { const char* wsp0; const char* wsp; const char* z2; const char* z1; const char* bsp; const char* pf; u32 aoff[2]; u32 boff; u32 adst; u32 dump; u32 tb; int arot; int w; };
template <int P> DEV void k2_a1(const K2G& g) {
  constexpr int SS = P / 8;
  const int i = (P % 8) ^ g.arot;
  dma16(g.wsp + SS * 128 + (size_t)i * 8 * 4096, g.aoff[P & 1], g.adst + (SS & 1) * 8192 + ((i ^ (2 * g.w)) * 1024));
}
template <int A, int B> DEV void k2_as(const K2G& g) { if constexpr (A < B) { k2_a1<A>(g); k2_as<A + 1, B>(g); } }
template <int N> DEV void k2_b1(const K2G& g, u32x4 (&bf)[K2_R]) {
 gload16<(N % 8 - 4) * 1024>(bf[N % K2_R], g.z2 + (N / 8) * 8192, g.boff); }
template <int A, int B> DEV void k2_bs(const K2G& g, u32x4 (&bf)[K2_R]) { if constexpr (A < B) { k2_b1<A>(g, bf); k2_bs<A + 1, B>(g, bf); } }
template <int I> DEV u32x4 k2_read_a(const char* ap0, const char* ap1, int t) {
  return *(const u32x4*)(((I & 1) ? ap1 : ap0) + ((I >> 1) & 1) * 8192 + t * 2048);
}
template <int TAU>
DEV void k2_group(const K2G& g, u32x4 (&bf)[K2_R], u32x4 (&af)[2][4], const char* ap0, const char* ap1, f32x4 (&acc)[4][7], u32x4 (&ep)[4], u32& bw, u32 (&tch)[2]) {
  constexpr int S = TAU / 7, F = TAU % 7;
  wait_vm<k2_wait(TAU)>();
  pin(bf[TAU % K2_R]);
  if constexpr (S < 15 && F >= 2 && F < 6) af[(S + 1) & 1][F - 2] = k2_read_a<S + 1>(ap0, ap1, F - 2);
  SB();
#pragma unroll
  for (int t = 0; t < 4; t++) acc[t][F] = K2_MFMA(bf[TAU % K2_R], af[S & 1][t], acc[t][F]);
  SB();
  if constexpr (TAU + K2_R < 112) k2_b1<TAU + K2_R>(g, bf);
  if constexpr (k2_apiece(TAU) >= 0) k2_a1<k2_apiece(TAU)>(g);
  if constexpr (k2_touch_at(TAU)) { keep(tch[(TAU / 14) & 1]); gload4(tch[(TAU / 14) & 1], g.z2 + (S + K2_TD) * 7168 - 4096, g.tb); }
  if constexpr (TAU == 111) { keep(tch[0]); keep(tch[1]); }
  if constexpr (TAU == K2_EPI_AT) {
    gload16<0>(ep[0], g.z1, g.boff); gload16<1024>(ep[1], g.z1, g.boff); gload16<2048>(ep[2], g.z1, g.boff); gload16<3072>(ep[3], g.z1, g.boff);
    gload4(bw, g.bsp, (g.boff >> 3) & 28u);
#pragma unroll
    for (int i = 0; i < K2_PF; i++) dma16(g.pf + i * 1024, g.boff, g.dump);
  }
  if constexpr (TAU < 111) k2_group<TAU + 1>(g, bf, af, ap0, ap1, acc, ep, bw, tch);
}

DEV void k2_gemm(char* smem, int b, const u16* wsp, const u16* bsp, const u16* wout, char* ws) {
  STAMP_DECL; STAMP(0);
  const int xcd = b & 7, idx = b >> 3;
  const int tp = (xcd >> 1) * 8 + (idx >> 2), fp = (xcd & 1) * 4 + (idx & 3);
  const int w = __builtin_amdgcn_readfirstlane(threadIdx.x >> 6), l = threadIdx.x & 63;
  const int rho = l & 15, q = l >> 4;
  const int tt = tp * 4 + w;
  char* const reg = smem + w * 16384;
  K2G g;
  g.wsp = (const char*)wsp + (size_t)tp * 64 * 4096 + w * 1024;
  g.wsp0 = (const char*)wsp + (size_t)tp * 64 * 4096 + w * 65536;
  g.z2 = ws + WS_Z2F + ((size_t)(fp * 64 + w * 16) * 7 + 4) * 1024;
  g.z1 = ws + WS_Z1F + ((size_t)tt * 28 + ((fp * 7) >> 1)) * 1024;
  g.bsp = (const char*)bsp + tt * 32;
  {
    int pc = (idx * 4 + w) * 7;
    pc = pc > 784 - 7 ? 784 - 7 : pc;
    g.pf = (const char*)wout + pc * 1024;
  }
  g.aoff[0] = (u32)((l >> 3) * 4096 + (((l & 7) ^ (l >> 4)) * 16));
  g.aoff[1] = (u32)((l >> 3) * 4096 + (((l & 7) ^ ((l >> 4) | 4)) * 16));
  g.boff = l * 16;
  g.adst = lds_addr(reg);
  g.dump = lds_addr(smem) + 65536 + w * 1024;
  g.w = w;
  g.arot = (idx & 3) * 2;
  u32x4 bf[K2_R], af[2][4], ep[4];
  u32 bw, tch[2];
  g.tb = (u32)(((idx >> 2) * 14 + (l < 14 ? l : 13)) * 128);
  tch[0] = tch[1] = 0u;
  k2_as<0, 8>(g); k2_bs<0, 14>(g, bf); k2_as<8, 16>(g); k2_bs<14, K2_R>(g, bf);
  const char* ap0 = reg + rho * 128 + (((0 + q) ^ (rho >> 1)) * 16);
  const char* ap1 = reg + rho * 128 + (((4 + q) ^ (rho >> 1)) * 16);
  f32x4 acc[4][7] = {};
  STAMP(1);
  wait_vm<K2_PRO - 8>();
  STAMP(2);
#pragma unroll
  for (int t = 0; t < 4; t++) af[0][t] = k2_read_a<0>(ap0, ap1, t);
  k2_group<0>(g, bf, af, ap0, ap1, acc, ep, bw, tch);
  STAMP(3);
#pragma unroll
  for (int f = 0; f < 7; f++) {
    *(f32x4*)(reg + f * 1024 + l * 16) = acc[1][f];
    *(f32x4*)(reg + (7 + f) * 1024 + l * 16) = acc[3][f];
  }
  bar();
  STAMP(4);
  char* const in1 = smem + (w ^ 1) * 16384;
#pragma unroll
  for (int f = 0; f < 7; f++) acc[2][f] += *(const f32x4*)(in1 + (7 + f) * 1024 + l * 16);
#pragma unroll
  for (int f = 0; f < 7; f++) *(f32x4*)(in1 + (7 + f) * 1024 + l * 16) = acc[2][f];
#pragma unroll
  for (int f = 0; f < 7; f++) acc[0][f] += *(const f32x4*)(in1 + f * 1024 + l * 16);
  bar();
  char* const in2 = smem + (w ^ 3) * 16384;
#pragma unroll
  for (int i = 0; i < 1; i++) wait_vm<K2_PF>();
#pragma unroll
  for (int i = 0; i < 4; i++) pin(ep[i]);
  asm volatile("" : "+v"(bw));
  const float bs = (rho & 1) ? bfhi(bw) : bflo(bw);
  STAMP(5);
  const int odd = fp & 1;
  u32x2 hv[8];
#pragma unroll
  for (int f = 0; f < 7; f++) {
    f32x4 v = acc[0][f] + *(const f32x4*)(in2 + (7 + f) * 1024 + l * 16);
    v = v + bs;
    const u32x4 ze = ep[f >> 1], zo = ep[(f + 1) >> 1];
    const u32 za = odd ? ((f & 1) ? zo.x : zo.z) : ((f & 1) ? ze.z : ze.x);
    const u32 zb = odd ? ((f & 1) ? zo.y : zo.w) : ((f & 1) ? ze.w : ze.y);
    hv[f] = (u32x2){pk_bf16(v.x * bflo(za), v.y * bfhi(za)), pk_bf16(v.z * bflo(zb), v.w * bfhi(zb))};
  }
  const u32 hb = (u32)(WS_HF + (tt * 28 + ((fp * 7) >> 1)) * 1024 + l * 16);
  if (odd) {
    gstore8(ws, hb + 8, hv[0]);
#pragma unroll
    for (int i = 0; i < 3; i++) gstore16(ws, hb + (i + 1) * 1024, (u32x4){hv[1 + 2 * i].x, hv[1 + 2 * i].y, hv[2 + 2 * i].x, hv[2 + 2 * i].y});
  } else {
#pragma unroll
    for (int i = 0; i < 3; i++) gstore16(ws, hb + i * 1024, (u32x4){hv[2 * i].x, hv[2 * i].y, hv[1 + 2 * i].x, hv[1 + 2 * i].y});
    gstore8(ws, hb + 3 * 1024, hv[6]);
  }
  STAMP(6); STAMP_FLUSH(1);
}

constexpr int KG_ROW = 272, KG_WAVE = 53 * KG_ROW;
constexpr int CV_CS = 4160, CV_T = 8 * CV_CS, CV_U = 2 * CV_T, CV_CMP = CV_U + 2 * 4608;
DEV f32x2 cexp_pow2(float gl, float fh, float fl, float p) {
  float m = __builtin_amdgcn_exp2f(gl * p);
  float r = __builtin_amdgcn_fractf(fh * p) + fl * p;
  return (f32x2){m * __builtin_amdgcn_cosf(r), m * __builtin_amdgcn_sinf(r)};
}
DEV float exp_acc(float x) {
  const float h = x * 1.44269504089f;
  const float lo = fmaf(x, 1.44269504089f, -h) + x * 1.925963e-8f;
  return __builtin_amdgcn_exp2f(h) * fmaf(lo, 0.69314718056f, 1.f);
}
struct KGen { f32x2 e1, e2, e4, e8, e16, e32, e64, e128, e256, e512, e1024, e2048, c0; };
DEV KGen kg_gens(float la, float ai, float cr, float ci, float dt, float sc) {
  const float zr = -rbf(exp_acc(la)) * dt, zi = ai * dt;
  const float gl = zr * 1.44269504089f;
  const float fh = zi * 0.15915494309f;
  const float fl = fmaf(zi, 0.15915494309f, -fh) + zi * 6.4206382e-9f;
  KGen k;
  k.c0 = (f32x2){cr * sc, ci * sc};
  k.e1 = cexp_pow2(gl, fh, fl, 1.f); k.e2 = csqr(k.e1); k.e4 = csqr(k.e2); k.e8 = csqr(k.e4);
  k.e16 = cexp_pow2(gl, fh, fl, 16.f); k.e32 = csqr(k.e16); k.e64 = csqr(k.e32); k.e128 = csqr(k.e64);
  k.e256 = cexp_pow2(gl, fh, fl, 256.f); k.e512 = csqr(k.e256); k.e1024 = csqr(k.e512); k.e2048 = csqr(k.e1024);
  return k;
}
DEV void kg_tables(const KGen& k, u32* tw) {
#define TW(row, v) tw[(row) * (KG_ROW / 4)] = (v)
  {
    f32x2 tb[16];
    tb[0] = (f32x2){128.f, 0.f};
    tb[1] = k.e8 * 128.f;
    tb[2] = k.e16 * 128.f; tb[3] = cmul(tb[1], k.e16);
    tb[4] = k.e32 * 128.f;
#pragma unroll
    for (int i = 1; i < 4; i++) tb[4 + i] = cmul(tb[i], k.e32);
    tb[8] = k.e64 * 128.f;
#pragma unroll
    for (int i = 1; i < 8; i++) tb[8 + i] = cmul(tb[i], k.e64);
    TW(0, pk_f16(128.f, 0.f));
    TW(16, pk_f16(k.e128.x * 128.f, k.e128.y * 128.f));
#pragma unroll
    for (int i = 1; i < 16; i++) { TW(i, pk_f16(tb[i].x, tb[i].y)); f32x2 u = cmul(tb[i], k.e128); TW(16 + i, pk_f16(u.x, u.y)); }
  }
  {
    f32x2 a[21];
    a[0] = k.c0; a[1] = cmul(k.c0, k.e1); a[2] = cmul(k.c0, k.e2); a[3] = cmul(a[1], k.e2);
#pragma unroll
    for (int i = 0; i < 4; i++) a[4 + i] = cmul(a[i], k.e4);
#pragma unroll
    for (int i = 0; i < 4; i++) a[8 + i] = cmul(a[2 * i], k.e256);
    a[12] = cmul(a[0], k.e512); a[13] = cmul(a[4], k.e512); a[14] = cmul(a[8], k.e512); a[15] = cmul(a[10], k.e512);
    a[16] = cmul(a[0], k.e1024); a[17] = cmul(a[8], k.e1024); a[18] = cmul(a[12], k.e1024); a[19] = cmul(a[14], k.e1024);
    a[20] = cmul(a[0], k.e2048);
#pragma unroll
    for (int i = 0; i < 21; i++) TW(32 + i, pk_f16(a[i].x, -a[i].y));
  }
#undef TW
}
template <int CTRL> DEV u32 dpp(u32 old, u32 src) { return __builtin_amdgcn_update_dpp(old, src, CTRL, 0xf, 0xf, false); }
template <int CTRL> DEV float dpp_add(float v) { return v + __uint_as_float(__builtin_amdgcn_update_dpp(0u, __float_as_uint(v), CTRL, 0xf, 0xf, true)); }
DEV void cv_frags(u32x4 (&A)[8], u32x4 (&B)[8], const char* pa, const char* pb) {
#pragma unroll
  for (int i = 0; i < 8; i++) {
    A[i] = *(const u32x4*)(pa - 512 * i);
    B[i] = *(const u32x4*)(pb + 512 * i);
  }
}
DEV void cv_mma(const u32x4 (&A)[8], const u32x4 (&B)[8], f32x4 (&acc)[8]) {
#pragma unroll
  for (int a = 0; a < 8; a++)
#pragma unroll
    for (int s = 7; s >= a; s--) acc[s] = mfma_h(A[a], B[s - a], acc[s]);
}
#define SSM_PRIO 0
DEV void k2_ssm(char* smem, int cb, const u16* lar, const u16* aim, const u16* cre, const u16* cim, const u16* ldt, const u16* dsk, char* ws) {
  STAMP_DECL; STAMP(0);
  __builtin_amdgcn_s_setprio(3);
  const int w = __builtin_amdgcn_readfirstlane(threadIdx.x >> 6), l = threadIdx.x & 63;
  const int ch = w >> 1, d = cb * 2 + ch, hw = w & 1, t = hw * 64 + l;
  const int rho = l & 15, q = l >> 4;
  const size_t o = (size_t)d * NMODE;
  const u32 vla = *(const u32*)(lar + o + hw * 128 + 2 * l), vai = *(const u32*)(aim + o + hw * 128 + 2 * l);
  const u32 vcr = *(const u32*)(cre + o + hw * 128 + 2 * l), vci = *(const u32*)(cim + o + hw * 128 + 2 * l);
  const u32 ocr = *(const u32*)(cre + o + (hw ^ 1) * 128 + 2 * l), oci = *(const u32*)(cim + o + (hw ^ 1) * 128 + 2 * l);
  const u32 vdt = ldt[d], vdk = dsk[d];
  char* const T = smem + ch * CV_T;
  char* const U = smem + CV_U + ch * 4608;
  u16* const cmp = (u16*)(smem + CV_CMP + ch * 1408);
#pragma unroll
  for (int i = 0; i < 2; i++) dma16(ws + WS_UT + (size_t)d * (SEQ * 2) + (hw * 2 + i) * 1024, l * 16, lds_addr(U) + 512 + (hw * 2 + i) * 1024);
  __builtin_amdgcn_s_setprio(SSM_PRIO);
  if (t < 32) *(u32x4*)(U + t * 16) = (u32x4){0u, 0u, 0u, 0u};
  u32 gi[2][8];
#pragma unroll
  for (int c = 0; c < 2; c++)
#pragma unroll
    for (int j = 0; j < 4; j++) {
      const int l0 = 2047 - (hw * 128 + c * 64 + l) * 8 - 2 * j, l1 = l0 - 1;
      const u32 bits = pk_bf16((float)l0, (float)l1);
      int i0 = (int)(bits & 0xffffu) - 0x4280, i1 = (int)(bits >> 16) - 0x4280;
      if (c == 1) { i0 = l0 < 128 ? l0 : i0; i1 = l1 < 128 ? l1 : i1; }
      gi[c][2 * j] = i0; gi[c][2 * j + 1] = i1;
    }
  const float dt = rbf(exp_acc(bf2f(vdt)));
  const float dk = bf2f(vdk);
  float S = fabsf(bflo(vcr)) + fabsf(bfhi(vcr)) + fabsf(bflo(vci)) + fabsf(bfhi(vci));
  float S2 = fabsf(bflo(ocr)) + fabsf(bfhi(ocr)) + fabsf(bflo(oci)) + fabsf(bfhi(oci));
  S = hw ? S2 + S : S + S2;
  S = dpp_add<0xb1>(S); S = dpp_add<0x4e>(S); S = dpp_add<0x141>(S); S = dpp_add<0x140>(S);
  S = xsum32(xsum16(S));
  int e = (int)((__float_as_uint(S) >> 23) & 255u) - 125;
  e = e < -100 ? -100 : (e > 100 ? 100 : e);
  e = __builtin_amdgcn_readfirstlane(e);
  const float sc = __uint_as_float((u32)(127 + 8 - e) << 23);
  const float ks = dt * __uint_as_float((u32)(127 + e - 14) << 23);
  STAMP(1);
  char* tab = smem + w * KG_WAVE;
  f32x16 kacc = {};
  {
    const int r = l & 31, h = l >> 5;
    const char* pa = tab + (32 + (r < 21 ? r : 20)) * KG_ROW + h * 16;
    const char* pb = tab + r * KG_ROW + h * 16;
    const KGen k0 = kg_gens(bflo(vla), bflo(vai), bflo(vcr), bflo(vci), dt, sc);
    STAMP(8);
    kg_tables(k0, (u32*)(tab + l * 4));
    SB();
    STAMP(9);
    u32x4 fa[8], fb[8];
#pragma unroll
    for (int kk = 0; kk < 8; kk++) { fa[kk] = *(const u32x4*)(pa + kk * 32); fb[kk] = *(const u32x4*)(pb + kk * 32); }
    SB();
    const KGen k1 = kg_gens(bfhi(vla), bfhi(vai), bfhi(vcr), bfhi(vci), dt, sc);
#pragma unroll
    for (int kk = 0; kk < 8; kk++) kacc = mfma32_h(fa[kk], fb[kk], kacc);
    STAMP(10);
    kg_tables(k1, (u32*)(tab + l * 4));
    STAMP(11);
#pragma unroll
    for (int kk = 0; kk < 8; kk++) kacc = mfma32_h(*(const u32x4*)(pa + kk * 32), *(const u32x4*)(pb + kk * 32), kacc);
    STAMP(12);
    float* give = (float*)tab + l;
    float keep[6];
#pragma unroll
    for (int i = 0; i < 6; i++) { give[i * 64] = hw ? kacc[i] : kacc[6 + i]; keep[i] = hw ? kacc[6 + i] : kacc[i]; }
    const int b1 = h ? 384 + 2 * r : 256 + 4 * r, b2 = h ? (r == 0 ? 640 : 672) : 512 + r;
    const int x0 = hw ? b1 + (h ? 64 : 2) : 4 * h + 8 * r;
    const int x1 = hw ? b2 : x0 + 2, d1 = hw ? (h ? (r == 0 ? 32 : 0) : 32) : 1;
    const int x2 = hw ? (h ? 672 : b2 + 64) : b1, d2 = hw ? (h ? 0 : 32) : 1;
    bar();
    const float* take = (const float*)(smem + (w ^ 1) * KG_WAVE) + l;
    const int xi[6] = {x0, x0 + 1, x1, x1 + d1, x2, x2 + d2};
#pragma unroll
    for (int i = 0; i < 6; i++) {
      const float o = take[i * 64];
      cmp[xi[i]] = __builtin_bit_cast(u16, (_Float16)(hw ? o + keep[i] : keep[i] + o));
    }
  }
  STAMP(2);
  bar();
  {
    u32 ra[3][4];
#pragma unroll
    for (int c = 0; c < 2; c++)
#pragma unroll
      for (int j = 0; j < 4; j++) ra[c][j] = (u32)cmp[gi[c][2 * j]] | ((u32)cmp[gi[c][2 * j + 1]] << 16);
    {
      const u32 k8 = hw ? 0u : cmp[512], k4 = hw ? 0u : cmp[511], k0 = hw ? 0u : cmp[510];
      ra[2][0] = k8 * 0x10001u; ra[2][1] = k4 * 0x10001u; ra[2][2] = k4 | (k0 << 16); ra[2][3] = k0 * 0x10001u;
    }
#pragma unroll
    for (int c = 0; c < 2; c++) {
      u32 dw[8], od[7];
#pragma unroll
      for (int j = 0; j < 4; j++) {
        dw[j] = ra[c][j];
        dw[4 + j] = dpp<0x130>(c == 0 ? dpp<0x134>(0u, ra[1][j]) : ra[2][j], ra[c][j]);
      }
#pragma unroll
      for (int i = 0; i < 7; i++) od[i] = __builtin_amdgcn_alignbit(dw[i + 1], dw[i], 16);
      char* tp = T + (hw * 128 + c * 64 + l) * 16;
#pragma unroll
      for (int k = 0; k < 4; k++) {
        *(u32x4*)(tp + (2 * k) * CV_CS) = (u32x4){dw[k], dw[k + 1], dw[k + 2], dw[k + 3]};
        *(u32x4*)(tp + (2 * k + 1) * CV_CS) = (u32x4){od[k], od[k + 1], od[k + 2], od[k + 3]};
      }
    }
    if (t < 8) *(u32x4*)(T + t * CV_CS + 4096) = (u32x4){0u, 0u, 0u, 0u};
  }
  wait_vm<0>();
  bar();
  STAMP(3);
  const char* pa = T + (7 - (rho & 7)) * CV_CS + (255 - (rho >> 3) + q) * 16 - 32 - 256 * hw;
  const char* pb = U + 512 + rho * 32 + q * 16 - 32 - 256 * hw;
  f32x4 acc[8] = {};
  {
    u32x4 A0[8], B0[8], A1[8], B1[8];
    cv_frags(A0, B0, pa, pb);
    SB(); cv_frags(A1, B1, pa - 64, pb - 64); SB();
    cv_mma(A0, B0, acc);
    SB(); cv_frags(A0, B0, pa - 128, pb - 128); SB();
    cv_mma(A1, B1, acc);
    SB(); cv_frags(A1, B1, pa - 192, pb - 192); SB();
    cv_mma(A0, B0, acc);
    cv_mma(A1, B1, acc);
  }
  STAMP(4);
  bar();
  {
    char* box = T + hw * 4096;
#pragma unroll
    for (int i = 0; i < 4; i++) {
      const f32x4 give = hw ? acc[i] : acc[4 + i];
      acc[i] = hw ? acc[4 + i] : acc[i];
      *(f32x4*)(box + i * 1024 + l * 16) = give;
    }
  }
  u32x2 ub[4];
#pragma unroll
  for (int i = 0; i < 4; i++) ub[i] = *(const u32x2*)(U + 512 + (256 * (4 * hw + i) + 16 * rho + 4 * q) * 2);
  bar();
  {
    const char* box = T + (hw ^ 1) * 4096;
    u32x2 yv[4];
#pragma unroll
    for (int i = 0; i < 4; i++) {
      f32x4 v = (acc[i] + *(const f32x4*)(box + i * 1024 + l * 16)) * ks;
      f16x2 u0 = as_h2(ub[i].x), u1 = as_h2(ub[i].y);
      yv[i] = (u32x2){pk_bf16(fmaf((float)u0.x, dk, v.x), fmaf((float)u0.y, dk, v.y)), pk_bf16(fmaf((float)u1.x, dk, v.z), fmaf((float)u1.y, dk, v.w))};
    }
#pragma unroll
    for (int i = 0; i < 4; i += 2) {
      auto sx = __builtin_amdgcn_permlane16_swap(yv[i].x, yv[i + 1].x, false, false);
      auto sy = __builtin_amdgcn_permlane16_swap(yv[i].y, yv[i + 1].y, false, false);
      const u32 off = (u32)(512 * (4 * hw + i + (q & 1)) + 32 * rho + 16 * (q >> 1));
      gstore16(ws, (u32)(WS_YT + d * (SEQ * 2)) + off, (u32x4){sx[0], sy[0], sx[1], sy[1]});
    }
  }
  STAMP(5); STAMP_FLUSH(1);
}

__global__ NOPK __launch_bounds__(256, 2) void k2(const u16* wsp, const u16* bsp, const u16* wout, const u16* lar, const u16* aim, const u16* cre,
                                             const u16* cim, const u16* ldt, const u16* dsk, char* ws) {
  __shared__ __attribute__((aligned(16))) char smem[81920];
  if (blockIdx.x < 256) k2_gemm(smem, blockIdx.x, wsp, bsp, wout, ws);
  else k2_ssm(smem, blockIdx.x - 256, lar, aim, cre, cim, ldt, dsk, ws);
}

#define K3_LEAD 3
struct K3G { const char* wo; const char* hf; u32 woff; u32 hoff[4]; u32 wdst; const char* yt; u32 yoff; u32 ydst; const char* xb; u32 xo; };
template <int I> DEV void k3_issue(const K3G& g, u32x4 (&hf)[7][4], u32x4 (&xv)[2]) {
  if constexpr (I < 7) {
#pragma unroll
    for (int i = 0; i < 4; i++) dma16(g.wo + (size_t)(I * 32 + i * 8) * (DM * 2), g.woff, g.wdst + (I * 4 + i) * 1024);
#pragma unroll
    for (int t = 0; t < 4; t++) gload16<0>(hf[I][t], g.hf + I * 1024, g.hoff[t]);
  } else if constexpr (I == 7) {
    dma16(g.yt, g.yoff, g.ydst);
    dma16(g.yt + 32 * (SEQ * 2), g.yoff, g.ydst + 1024);
    gload16<0>(xv[0], g.xb, g.xo);
    gload16<0>(xv[1], g.xb + 8 * (DM * 2), g.xo);
  }
}
constexpr int k3_cnt(int units) { return units <= 7 ? 8 * units : 60; }
template <int I> DEV void k3_step(const K3G& g, u32x4 (&hf)[7][4], u32x4 (&xv)[2], const char* const (&wp)[4], f32x4 (&acc)[4][4]) {
  wait_vm<k3_cnt(I + K3_LEAD) - 8 * (I + 1)>();
#pragma unroll
  for (int t = 0; t < 4; t++) pin(hf[I][t]);
  u32x4 wf[4];
#pragma unroll
  for (int p = 0; p < 4; p++) wf[p] = tr_frag(wp[p] + I * 4096, wp[p] + I * 4096 + 2048);
  k3_issue<I + K3_LEAD>(g, hf, xv);
#pragma unroll
  for (int p = 0; p < 4; p++)
#pragma unroll
    for (int t = 0; t < 4; t++) acc[t][p] = mfma_bf(wf[p], hf[I][t], acc[t][p]);
  if constexpr (I < 6) k3_step<I + 1>(g, hf, xv, wp, acc);
}
template <int A, int B> DEV void k3_pro(const K3G& g, u32x4 (&hf)[7][4], u32x4 (&xv)[2]) {
  if constexpr (A < B) { k3_issue<A>(g, hf, xv); k3_pro<A + 1, B>(g, hf, xv); }
}
__global__ __launch_bounds__(256, 1) void k3(const u16* x, const u16* wout, u16* out, char* ws) {
  __shared__ __attribute__((aligned(16))) char smem[4 * 28672 + 8192];
  STAMP_DECL; STAMP(0);
  const int b = blockIdx.x, xcd = b & 7, idx = b >> 3;
  const int tp = xcd * 4 + idx / 7, dp = idx % 7;
  const int w = __builtin_amdgcn_readfirstlane(threadIdx.x >> 6), l = threadIdx.x & 63;
  const int rho = l & 15, q = l >> 4, qq = (l >> 2) & 3, pp = l & 3;
  char* const reg = smem + w * 28672;
  char* const Y = smem + 4 * 28672;
  K3G g;
  g.wo = (const char*)wout + (size_t)(w * 224) * (DM * 2) + dp * 128;
  g.hf = ws + WS_HF + ((size_t)tp * 4 * 28 + w * 7) * 1024;
  g.woff = (u32)((l >> 3) * (DM * 2) + (((l & 7) ^ ((l >> 4) << 1)) * 16));
#pragma unroll
  for (int t = 0; t < 4; t++) g.hoff[t] = (u32)((t ^ w) * 28 * 1024 + l * 16);
  g.wdst = lds_addr(reg);
  g.yt = ws + WS_YT + (size_t)(dp * 64) * (SEQ * 2) + tp * 128 + w * 32;
  g.yoff = (u32)((l >> 1) * (SEQ * 2) + (l & 1) * 16);
  g.ydst = lds_addr(Y) + w * 2048;
  g.xo = (u32)((l >> 3) * (DM * 2) + (l & 7) * 16);
  const size_t xb = (size_t)(tp * 64 + w * 16) * (DM * 2) + dp * 128;
  g.xb = (const char*)x + xb;
  u32x4 hf[7][4], xv[2];
  k3_pro<0, K3_LEAD>(g, hf, xv);
  const char* wp[4];
  const int sg = ((q & 1) << 1) | (qq >> 1);
#pragma unroll
  for (int p = 0; p < 4; p++) wp[p] = reg + (4 * q + qq) * 128 + ((p ^ sg) * 32) + 8 * pp;
  f32x4 acc[4][4] = {};
  STAMP(1);
  k3_step<0>(g, hf, xv, wp, acc);
  STAMP(2);
#pragma unroll
  for (int t = 1; t < 4; t++)
#pragma unroll
    for (int p = 0; p < 4; p++) *(f32x4*)(reg + t * 4352 + rho * 272 + p * 64 + q * 16) = acc[t][p];
  wait_vm<0>();
  pin(xv[0]); pin(xv[1]);
#pragma unroll
  for (int p = 0; p < 4; p++) {
    u32x2 yv = tr_read(Y + w * 2048 + (16 * p + 4 * q + qq) * 32 + 8 * pp);
    *(f32x4*)(reg + rho * 272 + p * 64 + q * 16) = acc[0][p] + (f32x4){bflo(yv.x), bfhi(yv.x), bflo(yv.y), bfhi(yv.y)};
  }
  bar();
  STAMP(3);
#pragma unroll
  for (int i = 0; i < 2; i++) {
    f32x4 a = {bflo(xv[i].x), bfhi(xv[i].x), bflo(xv[i].y), bfhi(xv[i].y)}, c = {bflo(xv[i].z), bfhi(xv[i].z), bflo(xv[i].w), bfhi(xv[i].w)};
#pragma unroll
    for (int t = 0; t < 4; t++) {
      const char* rp = smem + (w ^ t) * 28672 + t * 4352 + (8 * i + (l >> 3)) * 272 + (l & 7) * 32;
      a += *(const f32x4*)(rp); c += *(const f32x4*)(rp + 16);
    }
    u32x4 o = {pk_bf16(a.x, a.y), pk_bf16(a.z, a.w), pk_bf16(c.x, c.y), pk_bf16(c.z, c.w)};
    gstore16(out, (u32)xb + i * 8 * (DM * 2) + g.xo, o);
  }
  STAMP(4); STAMP_FLUSH(2);
}

#define K1_GRID 256
#define K2_GRID 480
extern "C" void kernel_launch(void* const* d_in, const int* in_sizes, int n_in, void* d_out, int out_size, void* d_ws, size_t ws_size,
                              hipStream_t stream) {
  const u16* x = (const u16*)d_in[0];
  char* ws = (char*)d_ws;
  Extra ex = {{(const char*)d_out, (const char*)d_in[1], (const char*)d_in[2], (const char*)d_in[3], (const char*)d_in[4], (const char*)d_in[5], (const char*)d_in[6], (const char*)d_in[10]}};
  k1<<<K1_GRID, 512, 0, stream>>>(x, (const u16*)d_in[8], (const u16*)d_in[9], (const u16*)d_in[11], ws, ex);
  k2<<<K2_GRID, 256, 0, stream>>>((const u16*)d_in[9], (const u16*)d_in[10], (const u16*)d_in[11], (const u16*)d_in[1], (const u16*)d_in[2],
                              (const u16*)d_in[3], (const u16*)d_in[4], (const u16*)d_in[5], (const u16*)d_in[6], ws);
  k3<<<224, 256, 0, stream>>>(x, (const u16*)d_in[11], (u16*)d_out, ws);
}
```

```cpp
#include <hip/hip_runtime.h>
#include <stdint.h>

typedef unsigned short u16;
typedef unsigned int u32;
typedef unsigned long long u64;
typedef __attribute__((ext_vector_type(4))) u32 u32x4;
typedef __attribute__((ext_vector_type(2))) u32 u32x2;
typedef __attribute__((ext_vector_type(4))) float f32x4;
typedef __attribute__((ext_vector_type(2))) float f32x2;
typedef __attribute__((ext_vector_type(16))) float f32x16;
typedef __attribute__((ext_vector_type(8))) __bf16 bf16x8;
typedef __attribute__((ext_vector_type(2))) __bf16 bf16x2;
typedef __attribute__((ext_vector_type(8))) _Float16 f16x8;
typedef __attribute__((ext_vector_type(2))) _Float16 f16x2;
typedef __attribute__((ext_vector_type(4))) short s16x4;
#define LDSAS __attribute__((address_space(3)))
#define GAS __attribute__((address_space(1)))
#define DEV __device__ __forceinline__
#if defined(__HIP_DEVICE_COMPILE__)
#define NOPK __attribute__((target("no-packed-fp32-ops")))
#else
#define NOPK
#endif
#define SB() __builtin_amdgcn_sched_barrier(0)

constexpr int SEQ = 2048, DM = 448, NMODE = 256, FF = 1792, FH = 896;

constexpr size_t WS_Z2F = 0;
constexpr size_t WS_Z1F = 4u << 20;
constexpr size_t WS_HF  = 8u << 20;
constexpr size_t WS_UT  = 12u << 20;
constexpr size_t WS_YT  = 14u << 20;
constexpr size_t WS_GR  = 16u << 20;
constexpr size_t WS_EP  = 17u << 20;

#define STAMP(i)
#define STAMP_DECL
#define STAMP_ARG
#define STAMP_PASS
#define STAMP_FLUSH(kern)
DEV float bf2f(u32 v) { return __uint_as_float(v << 16); }
DEV float bflo(u32 v) { return __uint_as_float(v << 16); }
DEV float bfhi(u32 v) { return __uint_as_float(v & 0xffff0000u); }
DEV u32 pk_bf16(float a, float b) { bf16x2 v = {(__bf16)a, (__bf16)b}; return __builtin_bit_cast(u32, v); }
DEV u32 pk_f16(float a, float b) { f16x2 v = {(_Float16)a, (_Float16)b}; return __builtin_bit_cast(u32, v); }
DEV f16x2 as_h2(u32 v) { return __builtin_bit_cast(f16x2, v); }
DEV float rbf(float a) { return __uint_as_float(pk_bf16(a, a) << 16); }
DEV f32x4 mfma_bf(u32x4 a, u32x4 b, f32x4 c) {
  return __builtin_amdgcn_mfma_f32_16x16x32_bf16(__builtin_bit_cast(bf16x8, a), __builtin_bit_cast(bf16x8, b), c, 0, 0, 0);
}
DEV f32x4 mfma_h(u32x4 a, u32x4 b, f32x4 c) {
  return __builtin_amdgcn_mfma_f32_16x16x32_f16(__builtin_bit_cast(f16x8, a), __builtin_bit_cast(f16x8, b), c, 0, 0, 0);
}
DEV f32x16 mfma32_h(u32x4 a, u32x4 b, f32x16 c) {
  return __builtin_amdgcn_mfma_f32_32x32x16_f16(__builtin_bit_cast(f16x8, a), __builtin_bit_cast(f16x8, b), c, 0, 0, 0);
}
DEV u32x2 tr_read(const char* p) {
  s16x4 v = __builtin_amdgcn_ds_read_tr16_b64_v4i16((LDSAS s16x4*)p);
  return __builtin_bit_cast(u32x2, v);
}
DEV u32x4 tr_frag(const char* p0, const char* p1) {
  u32x2 a = tr_read(p0), b = tr_read(p1);
  return (u32x4){a.x, a.y, b.x, b.y};
}
#define LDMOD ""
#define DMAMOD ""
DEV u32 lds_addr(const void* p) { return (u32)(uintptr_t)(LDSAS const char*)p; }
DEV void dma16(const void* sbase, u32 voff, u32 ldst) {
  asm volatile("s_mov_b32 m0, %2\n\ts_nop 0\n\tglobal_load_lds_dwordx4 %0, %1 " DMAMOD ::"v"(voff), "s"(sbase), "s"(ldst) : "memory");
}
template <int OFF> DEV void gload16(u32x4& d, const void* sbase, u32 voff) {
  asm volatile("global_load_dwordx4 %0, %1, %2 offset:%3 " LDMOD : "=v"(d) : "v"(voff), "s"(sbase), "i"(OFF) : "memory");
}
DEV void gstore16(void* sbase, u32 voff, u32x4 d) {
  asm volatile("global_store_dwordx4 %0, %1, %2 sc1\n\ts_nop 1" ::"v"(voff), "v"(d), "s"(sbase) : "memory");
}
DEV void gstore8(void* sbase, u32 voff, u32x2 d) {
  asm volatile("global_store_dwordx2 %0, %1, %2 sc1" ::"v"(voff), "v"(d), "s"(sbase) : "memory");
}
DEV void gstore16p(void* sbase, u32 voff, u32x4 d) {
  asm volatile("global_store_dwordx4 %0, %1, %2\n\ts_nop 1" ::"v"(voff), "v"(d), "s"(sbase) : "memory");
}
#define Z2STORE gstore16
DEV void gload4(u32& d, const void* sbase, u32 voff) {
  asm volatile("global_load_dword %0, %1, %2" : "=v"(d) : "v"(voff), "s"(sbase) : "memory");
}
template <int N> DEV void wait_vm() { asm volatile("s_waitcnt vmcnt(%0)" ::"i"(N) : "memory"); }
DEV void touch(u32& d, const void* p) { asm volatile("global_load_dword %0, %1, off" : "=v"(d) : "v"(p) : "memory"); }
DEV void keep(u32& a) { asm volatile("" : "+v"(a)); }
DEV void pin(u32x4& a) { asm volatile("" : "+v"(a)); }
DEV void bar() { asm volatile("s_waitcnt lgkmcnt(0)\n\ts_barrier" ::: "memory"); }
DEV float xsum16(float v) { auto r = __builtin_amdgcn_permlane16_swap(__float_as_uint(v), __float_as_uint(v), false, false); return __uint_as_float(r[0]) + __uint_as_float(r[1]); }
DEV float xsum32(float v) { auto r = __builtin_amdgcn_permlane32_swap(__float_as_uint(v), __float_as_uint(v), false, false); return __uint_as_float(r[0]) + __uint_as_float(r[1]); }
DEV f32x2 ln_scalars(float sum, float sq, float inv_n) {
  float m = sum * inv_n;
  float v = fmaxf(sq * inv_n - m * m, 0.f);
  float sd = rbf(__builtin_amdgcn_sqrtf(rbf(rbf(v) + 1e-5f)));
  return (f32x2){rbf(m), __builtin_amdgcn_rcpf(sd)};
}
DEV f32x2 gelu2(f32x2 x) {
  const float c1 = -2.3022081983f, c2 = -0.1029432396f;
  f32x2 t = x * x;
  t = t * c2 + c1;
  t = t * x;
  f32x2 e = {__builtin_amdgcn_exp2f(t.x), __builtin_amdgcn_exp2f(t.y)};
  e = e + 1.0f;
  f32x2 r = {__builtin_amdgcn_rcpf(e.x), __builtin_amdgcn_rcpf(e.y)};
  return x * r;
}
DEV f32x2 cmul(f32x2 a, f32x2 b) { return (f32x2){fmaf(a.x, b.x, -(a.y * b.y)), fmaf(a.x, b.y, a.y * b.x)}; }
DEV f32x2 csqr(f32x2 a) { return (f32x2){a.x * a.x - a.y * a.y, 2.f * a.x * a.y}; }

#define K1_PKACT
#define K1_PUBDELAY 8
#define TSTRIDE 65536u
#define K1_PRIO 2
#define K1_PF 4
constexpr int k1_slot(int u) { return u < 9 ? u : u - 9; }
constexpr int K1_X = 73728, K1_UT = K1_X + 65536, K1_CS = K1_UT + 32 * 272, K1_LN = K1_CS + 512, K1_LN1 = K1_LN + 1024, K1_CNT = K1_LN1 + 1024, K1_DUMP = K1_CNT + 64, K1_LDS = K1_DUMP + 4096;

typedef LDSAS char* lptr;
DEV lptr pinned(lptr p) { asm volatile("" : "+v"(p)); return p; }
DEV u32x4 lread16(lptr p) { return *(LDSAS u32x4*)p; }
DEV u32x4 ltr_frag(lptr p0, lptr p1) {
  s16x4 a = __builtin_amdgcn_ds_read_tr16_b64_v4i16((LDSAS s16x4*)p0), b = __builtin_amdgcn_ds_read_tr16_b64_v4i16((LDSAS s16x4*)p1);
  u32x2 x = __builtin_bit_cast(u32x2, a), y = __builtin_bit_cast(u32x2, b);
  return (u32x4){x.x, x.y, y.x, y.y};
}
DEV void mma(f32x4& c, const u32x4& a, const u32x4& b) { asm volatile("v_mfma_f32_16x16x32_bf16 %0, %1, %2, %0" : "+v"(c) : "v"(a), "v"(b)); }
DEV void mma0(f32x4& c, const u32x4& a, const u32x4& b) { asm volatile("v_mfma_f32_16x16x32_bf16 %0, %1, %2, 0" : "=&v"(c) : "v"(a), "v"(b)); }
template <bool FIRST> DEV void mmaf(f32x4& c, const u32x4& a, const u32x4& b) { if constexpr (FIRST) mma0(c, a, b); else mma(c, a, b); }
DEV void mma_drain() { SB(); asm volatile("s_nop 7\n\ts_nop 1" ::: "memory"); SB(); }
DEV void sbar() { asm volatile("s_barrier" ::: "memory"); }

struct K1L { const char* win; const char* xb; u32 xoff[2]; u32 woff[2]; u32 wdst; u32 xdst; int m0; };
template <int U> DEV void k1_unit(const K1L& g) {
  int line = U % 7 + g.m0; line = line >= 7 ? line - 7 : line;
  if constexpr (U < 7) {
#pragma unroll
    for (int i = 0; i < 4; i++) dma16(g.xb + line * 128 + (i >> 1) * 16 * (DM * 2), g.xoff[i & 1], g.xdst + (U & 3) * 16384 + i * 1024);
  }
#pragma unroll
  for (int i = 0; i < 2; i++) dma16(g.win + (size_t)line * 64 * (FF * 2) + (U < 7 ? FH * 2 : 0), g.woff[i], g.wdst + k1_slot(U) * 8192 + i * 1024);
}
struct K1C { lptr str[4]; lptr wp[4]; };
DEV void k1_xr(const K1C& g, u32x4 (&xf)[4], int M) {
#pragma unroll
  for (int i = 0; i < 4; i++) xf[i] = lread16(g.str[i] + (M & 3) * 16384);
}
template <int U> DEV u32x4 k1_wf(const K1C& g, int f) {
  if constexpr (k1_slot(U) < 8) return ltr_frag(g.wp[f & 3] + k1_slot(U) * 8192 + (f >> 2) * 4096, g.wp[f & 3] + k1_slot(U) * 8192 + (f >> 2) * 4096 + 512);
  else { lptr p = pinned(g.wp[f & 3] + 65536); return ltr_frag(p + (f >> 2) * 4096, p + (f >> 2) * 4096 + 512); }
}
template <int U> DEV void k1_main(const K1C& g, const u32x4 (&xf)[4], u32x4 (&wf)[8], f32x4 (&acc)[2][4], f32x4& cs, const u32x4& ones) {
#pragma unroll
  for (int f = 0; f < 8; f++) {
    if (U % 7 == 0 && f < 4) {
      if (f == 0) mma0(cs, wf[f], ones);
      mma0(acc[0][f & 3], wf[f], xf[0]);
      mma0(acc[1][f & 3], wf[f], xf[1]);
    } else {
      if ((f & 3) == 0) mma(cs, wf[f], ones);
      mma(acc[0][f & 3], wf[f], xf[(f >> 2) * 2]);
      mma(acc[1][f & 3], wf[f], xf[(f >> 2) * 2 + 1]);
    }
    SB(); if constexpr (U % 7 < 6) wf[f] = k1_wf<U + 1>(g, f); SB();
  }
}
template <int M>
DEV void k1_stage(const K1C& g, u32x4 (&xf)[7][4], u32x4 (&wf)[8], f32x4 (&acc)[2][4], f32x4& cs, f32x4 (&rsum)[2], f32x4 (&gram)[2], const u32x4& ones) {
  asm volatile("s_waitcnt lgkmcnt(15)\n\ts_barrier" ::: "memory");
  if constexpr (M < 6) k1_xr(g, xf[M + 1], M + 1);
  SB();
#pragma unroll
  for (int i = 0; i < 4; i++) {
    if (M == 0 && i < 2) { mma0(rsum[i & 1], ones, xf[M][i]); mma0(gram[i & 1], xf[M][i], xf[M][i]); }
    else { mma(rsum[i & 1], ones, xf[M][i]); mma(gram[i & 1], xf[M][i], xf[M][i]); }
  }
  k1_main<M>(g, xf[M], wf, acc, cs, ones);
  if constexpr (M < 6) k1_stage<M + 1>(g, xf, wf, acc, cs, rsum, gram, ones);
}
template <int M>
DEV void k1_z1loop(const K1C& g, u32x4 (&xf)[7][4], u32x4 (&wf)[8], f32x4 (&acc)[2][4], f32x4& cs, const u32x4& ones, const char* pf, u32 l16, u32 dump) {
  if constexpr (M + 1 < K1_PF) dma16(pf + (M + 1) * 1024, l16, dump);
  k1_main<M + 7>(g, xf[M], wf, acc, cs, ones);
  if constexpr (M < 6) k1_z1loop<M + 1>(g, xf, wf, acc, cs, ones, pf, l16, dump);
}
DEV void k1_act(f32x4 (&acc)[2][4], const char* csb, int w, const f32x2 (&ln1)[2]) {
#pragma unroll
  for (int p = 0; p < 4; p++) {
    const f32x4 c = *(const f32x4*)(csb + (p ^ w) * 64);
#pragma unroll
    for (int t = 0; t < 2; t++) {
      f32x2 a = (acc[t][p].xy - c.xy * ln1[t].x) * ln1[t].y;
      f32x2 b = (acc[t][p].zw - c.zw * ln1[t].x) * ln1[t].y;
      a = gelu2(a); b = gelu2(b);
      acc[t][p] = (f32x4){a.x, a.y, b.x, b.y};
    }
  }
}
DEV void act4_stats(f32x4& x, const f32x4& c, float m, float rs, float c1, float& s, float& s2) {
  float x0 = x.x, x1 = x.y, x2 = x.z, x3 = x.w, t0, t1, t2, t3;
  asm("v_fma_f32 %0, -%10, %14, %0\n\tv_fma_f32 %1, -%11, %14, %1\n\tv_fma_f32 %2, -%12, %14, %2\n\tv_fma_f32 %3, -%13, %14, %3\n\t"
      "v_mul_f32 %0, %0, %15\n\tv_mul_f32 %1, %1, %15\n\tv_mul_f32 %2, %2, %15\n\tv_mul_f32 %3, %3, %15\n\t"
      "v_mul_f32 %4, %0, %0\n\tv_mul_f32 %5, %1, %1\n\tv_mul_f32 %6, %2, %2\n\tv_mul_f32 %7, %3, %3\n\t"
      "v_fmamk_f32 %4, %4, 0xbdd2d3e8, %16\n\tv_fmamk_f32 %5, %5, 0xbdd2d3e8, %16\n\tv_fmamk_f32 %6, %6, 0xbdd2d3e8, %16\n\tv_fmamk_f32 %7, %7, 0xbdd2d3e8, %16\n\t"
      "v_mul_f32 %4, %4, %0\n\tv_mul_f32 %5, %5, %1\n\tv_mul_f32 %6, %6, %2\n\tv_mul_f32 %7, %7, %3\n\t"
      "v_exp_f32 %4, %4\n\tv_exp_f32 %5, %5\n\tv_exp_f32 %6, %6\n\tv_exp_f32 %7, %7\n\t"
      "v_add_f32 %4, 1.0, %4\n\tv_add_f32 %5, 1.0, %5\n\tv_add_f32 %6, 1.0, %6\n\tv_add_f32 %7, 1.0, %7\n\t"
      "v_rcp_f32 %4, %4\n\tv_rcp_f32 %5, %5\n\tv_rcp_f32 %6, %6\n\tv_rcp_f32 %7, %7\n\t"
      "v_mul_f32 %0, %0, %4\n\tv_mul_f32 %1, %1, %5\n\tv_mul_f32 %2, %2, %6\n\tv_mul_f32 %3, %3, %7\n\t"
      "v_add_f32 %4, %0, %1\n\tv_fmac_f32 %9, %0, %0\n\tv_add_f32 %5, %2, %3\n\tv_fmac_f32 %9, %1, %1\n\t"
      "v_add_f32 %8, %8, %4\n\tv_fmac_f32 %9, %2, %2\n\tv_add_f32 %8, %8, %5\n\tv_fmac_f32 %9, %3, %3"
      : "+v"(x0), "+v"(x1), "+v"(x2), "+v"(x3), "=&v"(t0), "=&v"(t1), "=&v"(t2), "=&v"(t3), "+v"(s), "+v"(s2)
      : "v"(c.x), "v"(c.y), "v"(c.z), "v"(c.w), "v"(m), "v"(rs), "v"(c1));
  x = (f32x4){x0, x1, x2, x3};
}
DEV int lane_id() { int l = __builtin_amdgcn_mbcnt_hi(~0u, __builtin_amdgcn_mbcnt_lo(~0u, 0u)); asm volatile("" : "+v"(l)); return l; }
DEV void quad_sync(lptr cnt, int l) {
  asm volatile("s_waitcnt lgkmcnt(0)" ::: "memory");
  if (l == 0) __hip_atomic_fetch_add((LDSAS u32*)cnt, 1u, __ATOMIC_RELAXED, __HIP_MEMORY_SCOPE_WORKGROUP);
  while (__builtin_amdgcn_readfirstlane(*(volatile LDSAS u32*)cnt) < 4u) __builtin_amdgcn_s_sleep(0);
  asm volatile("" ::: "memory");
}
DEV void lbar0() { asm volatile("s_waitcnt vmcnt(6) lgkmcnt(0)\n\ts_barrier" ::: "memory"); }

struct Extra { const char* p[8]; };
DEV void k1_body(char* smem, const u16* x, const u16* win, const u16* wsp, const u16* wout, char* ws, const Extra& ex) {
  STAMP_DECL; STAMP(0);
  const int b = blockIdx.x, xcd = b & 7, idx = b >> 3;
  const int w8 = __builtin_amdgcn_readfirstlane(threadIdx.x >> 6), l = threadIdx.x & 63, w = w8 & 3;
  if (b >= 224) {
    const int j = (idx - 28) * 8 + w8;
    const char* p = (const char*)wsp + ((size_t)xcd * 1024 + 112 * K1_PF) * 1024;
    constexpr int REST = 1024 - 112 * K1_PF;
    u32 dc;
    {
      u64 c0, c1;
      asm volatile("s_getpc_b64 vcc\n\ts_add_u32 vcc_lo, vcc_lo, k1@rel32@lo+4\n\ts_addc_u32 vcc_hi, vcc_hi, k1@rel32@hi+12\n\ts_mov_b64 %0, vcc" : "=s"(c0) :: "scc", "vcc");
      asm volatile("s_getpc_b64 vcc\n\ts_add_u32 vcc_lo, vcc_lo, k3_code_end@rel32@lo+4\n\ts_addc_u32 vcc_hi, vcc_hi, k3_code_end@rel32@hi+12\n\ts_mov_b64 %0, vcc" : "=s"(c1) :: "scc", "vcc");
      const u32 len = (u32)(c1 - c0) & ~3u, o = (u32)(j * 64 + l) * 128u;
      gload4(dc, (const void*)c0, j < 8 ? (o < len ? o : len - 4u) : 0u);
    }
    {
      u32 d0, d1, d2, d3;
      const int n = j * 64 + l;
      const char* p = n < 448 ? ex.p[0] + n * 4096 : (n < 672 ? ex.p[1 + (n - 448) / 56] + ((n - 448) % 56) * 4096 : ex.p[5 + (n % 3)]);
      asm volatile("" : "+v"(p));
      gload4(d0, ws, (u32)((n * TSTRIDE) % (18u << 20)));
      gload4(d1, ws, (u32)(((n + 2048) * TSTRIDE) % (18u << 20)));
      gload4(d2, ws, (u32)(((n + 4096) * TSTRIDE) % (18u << 20)));
      touch(d3, p);
      wait_vm<0>();
      keep(d0); keep(d1); keep(d2); keep(d3);
      keep(dc);
    }
    const char* po = (const char*)wout + (size_t)(xcd * 98) * 1024;
#pragma unroll
    for (int k = 0; k < 4; k++) { int c = j + 32 * k; c = c < 98 ? c : 97; dma16(po + c * 1024, l * 16, lds_addr(smem) + w8 * 1024); }
#pragma unroll 1
    for (int i = j; i < REST; i += 32) {
      dma16(p + (size_t)i * 1024, l * 16, lds_addr(smem) + w8 * 1024);
    }
    STAMP(1); STAMP_FLUSH(0);
    return;
  }
  const int i7 = (idx * 37) >> 8;
  const int panel = (xcd >> 1) * 4 + i7, sib = (xcd & 1) * 7 + idx - 7 * i7;
  const lptr lds = (lptr)smem;
  K1L g;
  if (w8 >= 4) {
    g.win = (const char*)win + (size_t)(16 * w) * (FF * 2) + sib * 128;
    g.xb = (const char*)x + (size_t)(panel * 128 + w * 32) * (DM * 2);
    g.xoff[0] = (u32)((l >> 3) * (DM * 2) + (((l & 7) ^ (l >> 4)) * 16));
    g.xoff[1] = (u32)((8 + (l >> 3)) * (DM * 2) + (((l & 7) ^ ((l >> 4) | 4)) * 16));
    g.woff[0] = (u32)((l >> 3) * (FF * 2) + (((l & 7) ^ (((l >> 4) & 1) << 1)) * 16));
    g.woff[1] = (u32)((8 + (l >> 3)) * (FF * 2) + (((l & 7) ^ (((l >> 4) & 1) << 1) ^ 4) * 16));
    g.xdst = lds_addr(smem) + K1_X + w * 4096;
    g.wdst = lds_addr(smem) + w * 2048;
    g.m0 = sib >> 1;
    k1_unit<0>(g); k1_unit<1>(g);
  }
  K1C c;
  {
    const int rho = l & 15, q = l >> 4, qq = (l >> 2) & 3, pp = l & 3;
#pragma unroll
    for (int i = 0; i < 4; i++) c.str[i] = pinned(lds + K1_X + w * 4096 + (i & 1) * 2048 + rho * 128 + (((4 * (i >> 1) + q) ^ (rho >> 1)) * 16));
    const int sg = (qq >> 1) | ((q & 1) << 1);
#pragma unroll
    for (int f = 0; f < 4; f++) c.wp[f] = lds + (8 * q + qq) * 128 + (((f ^ w) ^ sg) * 32) + 8 * pp;
  }
  u32x4 ones = {0x3f803f80u, 0x3f803f80u, 0x3f803f80u, 0x3f803f80u};
  pin(ones);
  u32x4 xf[7][4], wf[8];
  f32x4 acc[2][4], cs;
  if (w8 >= 4) {
    lbar0();
    k1_unit<2>(g); k1_xr(c, xf[0], 0); lbar0();
    k1_unit<3>(g); k1_xr(c, xf[1], 1); lbar0();
    k1_unit<4>(g); k1_xr(c, xf[2], 2); lbar0();
    k1_unit<5>(g); k1_xr(c, xf[3], 3); lbar0();
    k1_unit<6>(g); k1_xr(c, xf[4], 4); lbar0();
    k1_unit<7>(g); k1_unit<8>(g); k1_unit<9>(g); k1_xr(c, xf[5], 5); lbar0();
    k1_unit<10>(g); k1_unit<11>(g); k1_unit<12>(g); k1_xr(c, xf[6], 6);
    asm volatile("s_waitcnt lgkmcnt(0)\n\ts_barrier" ::: "memory");
    k1_unit<13>(g);
    STAMP(1);
    const char* pf = (const char*)wsp + ((size_t)xcd * 1024 + (idx * 4 + w) * K1_PF) * 1024;
    const u32 l16 = l * 16, dump = lds_addr(smem) + K1_DUMP + w * 1024;
    if constexpr (K1_PF > 0) dma16(pf, l16, dump);
    wait_vm<(K1_PF > 0 ? 1 : 0)>(); sbar();
    STAMP(2);
    f32x2 ln1[2];
    {
    const int rho = l & 15, q = l >> 4;
#pragma unroll
    for (int t = 0; t < 2; t++) ln1[t] = *(const f32x2*)(smem + K1_LN1 + w * 256 + (t * 16 + rho) * 8);
#pragma unroll
    for (int f = 0; f < 8; f++) wf[f] = k1_wf<7>(c, f);
#pragma unroll
    for (int t = 0; t < 2; t++) {
      const u32x4 va = xf[0][t], vb = xf[0][2 + t];
      const u32 msk = (sib & 1) ? 0xffffffffu : 0u;
      const u32x4 v = (va & ~msk) | (vb & msk);
      u16* ub = (u16*)(smem + K1_UT + (8 * q) * 272 + (w * 32 + t * 16 + rho) * 2);
#pragma unroll
      for (int j = 0; j < 4; j++) {
        float a = rbf(rbf(bflo(v[j]) - ln1[t].x) * ln1[t].y), bb = rbf(rbf(bfhi(v[j]) - ln1[t].x) * ln1[t].y);
        u32 o = pk_f16(a, bb);
        ub[(2 * j) * 136] = (u16)o;
        ub[(2 * j + 1) * 136] = (u16)(o >> 16);
      }
    }
}
    SB();
    k1_z1loop<0>(c, xf, wf, acc, cs, ones, pf, l16, dump);
    mma_drain();
    const int l = lane_id(), rho = l & 15, q = l >> 4;
    if (rho == 0) *(f32x4*)(smem + K1_CS + 256 + w * 64 + q * 16) = cs;
    STAMP(3);
    quad_sync(lds + K1_CNT, l);
    STAMP(4);
#pragma unroll
    for (int i = 0; i < 2; i++) {
      const u32x4 uo = *(const u32x4*)(smem + K1_UT + (w * 8 + i * 4 + q) * 272 + rho * 16);
      gstore16(ws, (u32)(WS_UT + (sib * 32 + w * 8 + i * 4 + q) * (SEQ * 2) + panel * 256 + rho * 16), uo);
    }
    k1_act(acc, smem + K1_CS + 256 + q * 16, w, ln1);
#pragma unroll
    for (int t = 0; t < 2; t++)
#pragma unroll
      for (int cc = 0; cc < 2; cc++) {
        u32x2 a = {pk_bf16(acc[t][2 * cc].x, acc[t][2 * cc].y), pk_bf16(acc[t][2 * cc].z, acc[t][2 * cc].w)};
        u32x2 bb = {pk_bf16(acc[t][2 * cc + 1].x, acc[t][2 * cc + 1].y), pk_bf16(acc[t][2 * cc + 1].z, acc[t][2 * cc + 1].w)};
        const u32x4 o = (w & 1) ? (u32x4){bb.x, bb.y, a.x, a.y} : (u32x4){a.x, a.y, bb.x, bb.y};
        gstore16(ws, (u32)(WS_Z1F + ((panel * 8 + w * 2 + t) * 28 + sib * 2 + (cc ^ (w >> 1))) * 1024 + l * 16), o);
      }
    STAMP(5); STAMP_FLUSH(0);
    return;
  }
  const int grp = panel * 4 + w;
  u32 tch;
  {
    const int h = l >> 5, n = (w & 1) * 32 + (l & 31);
    (void)h;
    const int wl = panel * 56 + (n < 56 ? n : 55);
    const u32 xo = (u32)(panel * 128 * (DM * 2) + (sib * 64 + n) * 128);
    const u32 wo = (u32)((wl >> 1) * (FF * 2) + ((wl & 1) ? 0 : FH * 2) + sib * 128);
    gload4(tch, w < 2 ? (const void*)x : (const void*)win, w < 2 ? xo : wo);
  }
  u32 tag;
  asm volatile("s_load_dword %0, %1, 0x0\n\ts_waitcnt lgkmcnt(0)" : "=s"(tag) : "s"(ws + WS_EP + grp * 256) : "memory");
  tag += 1u;
  f32x4 rsum[2], gram[2];
  if (threadIdx.x < 2) *(u32*)(smem + K1_CNT + threadIdx.x * 4) = 0u;
  bar();
  STAMP(2);
  k1_xr(c, xf[0], 0);
#pragma unroll
  for (int f = 0; f < 8; f++) wf[f] = k1_wf<0>(c, f);
  k1_stage<0>(c, xf, wf, acc, cs, rsum, gram, ones);
  mma_drain();
  wait_vm<0>(); keep(tch);
  const int rho = l & 15, q = l >> 4, qq = (l >> 2) & 3, pp = l & 3;
  if (rho == 0) *(f32x4*)(smem + K1_CS + w * 64 + q * 16) = cs;
  STAMP(3);
  f32x2 ln1[2];
#pragma unroll
  for (int t = 0; t < 2; t++) {
    float d = (rho & 2) ? ((rho & 1) ? gram[t].w : gram[t].z) : ((rho & 1) ? gram[t].y : gram[t].x);
    d = ((rho >> 2) == q) ? d : 0.f;
    d = xsum32(xsum16(d));
    ln1[t] = ln_scalars(rsum[t].x, d, 1.f / DM);
    if (q == 0) *(f32x2*)(smem + K1_LN1 + w * 256 + (t * 16 + rho) * 8) = ln1[t];
  }
  bar();
  __builtin_amdgcn_s_setprio(K1_PRIO);
  STAMP(4);
  GAS u64* gr = (GAS u64*)(ws + WS_GR) + (size_t)grp * 14 * 64;
  {
    float st[2][2] = {{0.f, 0.f}, {0.f, 0.f}};
    k1_act(acc, smem + K1_CS + q * 16, w, ln1);
#pragma unroll
    for (int t = 0; t < 2; t++) {
      f32x2 s = {0.f, 0.f}, s2 = {0.f, 0.f};
#pragma unroll
      for (int p = 0; p < 4; p++) {
        s += acc[t][p].xy; s += acc[t][p].zw;
        s2 += acc[t][p].xy * acc[t][p].xy; s2 += acc[t][p].zw * acc[t][p].zw;
      }
      st[t][0] = s.x + s.y; st[t][1] = s2.x + s2.y;
    }
#pragma unroll
    for (int t = 0; t < 2; t++) { st[t][0] = xsum32(xsum16(st[t][0])); st[t][1] = xsum32(xsum16(st[t][1])); }
    const float v = (q & 2) ? ((q & 1) ? st[1][1] : st[0][1]) : ((q & 1) ? st[1][0] : st[0][0]);
    __hip_atomic_store(gr + sib * 64 + l, ((u64)tag << 32) | __float_as_uint(v), __ATOMIC_RELAXED, __HIP_MEMORY_SCOPE_AGENT);
  }
  STAMP(5);
  __builtin_amdgcn_s_sleep(K1_PUBDELAY);
  STAMP(6);
  u64 rv[14];
#pragma unroll
  for (int i = 0; i < 14; i++) rv[i] = __hip_atomic_load(gr + i * 64 + l, __ATOMIC_RELAXED, __HIP_MEMORY_SCOPE_AGENT);
  STAMP(7);
  float tot;
  for (u32 spins = 0;; spins++) {
    bool ok = true; tot = 0.f;
#pragma unroll
    for (int i = 0; i < 14; i++) { ok &= (u32)(rv[i] >> 32) == tag; tot += __uint_as_float((u32)rv[i]); }
    if (__all(ok) || spins > (1u << 20)) break;
#pragma unroll
    for (int i = 0; i < 14; i++) rv[i] = __hip_atomic_load(gr + i * 64 + l, __ATOMIC_RELAXED, __HIP_MEMORY_SCOPE_AGENT);
  }
  STAMP(8);
  if (sib == 0 && l == 0) __hip_atomic_store((GAS u32*)(ws + WS_EP + grp * 256), tag, __ATOMIC_RELAXED, __HIP_MEMORY_SCOPE_AGENT);
  {
    auto bb = __builtin_amdgcn_permlane32_swap(__float_as_uint(tot), __float_as_uint(tot), false, false);
    const f32x2 lr = ln_scalars(__uint_as_float(bb[0]), __uint_as_float(bb[1]), 1.f / FH);
    auto lm = __builtin_amdgcn_permlane16_swap(__float_as_uint(lr.x), __float_as_uint(lr.x), false, false);
    auto ls = __builtin_amdgcn_permlane16_swap(__float_as_uint(lr.y), __float_as_uint(lr.y), false, false);
    char* const stg = smem + K1_X + w * 8192;
#pragma unroll
    for (int t = 0; t < 2; t++) {
      const float m2 = __uint_as_float(lm[t]), r2 = __uint_as_float(ls[t]);
#pragma unroll
      for (int p = 0; p < 4; p++) {
        f32x2 a = (acc[t][p].xy - m2) * r2, b2 = (acc[t][p].zw - m2) * r2;
        *(u32x2*)(stg + (16 * t + rho) * 136 + p * 32 + q * 8) = (u32x2){pk_bf16(a.x, a.y), pk_bf16(b2.x, b2.y)};
      }
    }
#pragma unroll
    for (int p = 0; p < 4; p++) {
      const char* rp = stg + (8 * q + qq) * 136 + p * 32 + pp * 8;
      u32x4 o = tr_frag(rp, rp + 4 * 136);
      const int ft = sib * 4 + (p ^ w);
      Z2STORE(ws, (u32)(WS_Z2F + (((ft / 7) * 64 + grp) * 7 + ft % 7) * 1024 + l * 16), o);
    }
  }
  STAMP(9); STAMP_FLUSH(0);
}
#define K1_ATTR
extern "C" __global__ K1_ATTR __launch_bounds__(512, 2) void k1(const u16* x, const u16* win, const u16* wsp, const u16* wout, char* ws, Extra ex) {
  __shared__ __attribute__((aligned(16))) char smem[K1_LDS];
  k1_body(smem, x, win, wsp, wout, ws, ex);
}

#define K2_R 18
#define K2_PF 7
#define K2_TD 0
constexpr bool k2_touch_at(int tau) { return K2_TD > 0 && tau % 14 == 0 && tau / 7 + K2_TD <= 14; }
constexpr int K2_EPI = 5, K2_EPI_AT = 112 - K2_R + 2, K2_PRO = 16 + K2_R + (K2_TD > 0 ? 2 : 0);
constexpr int k2_apiece(int tau) {
  int s = tau / 7, f = tau % 7;
  int a = (s + 3) / 2 + ((s & 1) ? 0 : 1) - ((s & 1) ? 0 : 1);
  if (s & 1) { a = (s + 3) / 2; return (a <= 7 && (f & 1) == 0) ? 8 * a + f / 2 : -1; }
  a = (s + 2) / 2;
  return (a >= 2 && a <= 7 && f < 4) ? 8 * a + 4 + f : -1;
}
constexpr int k2_ops_after(int tau) { return (tau + K2_R < 112 ? 1 : 0) + (k2_apiece(tau) >= 0 ? 1 : 0) + (tau == K2_EPI_AT ? K2_EPI + K2_PF : 0) + (k2_touch_at(tau) ? 1 : 0); }
constexpr int k2_issued(int tau) { int n = K2_PRO; for (int i = 0; i < tau; i++) n += k2_ops_after(i); return n; }
constexpr int k2_pos_B(int n) { return n < 14 ? 8 + n + 1 : (n < K2_R ? 16 + n + 1 : k2_issued(n - K2_R) + 1); }
constexpr int k2_pos_A(int a) { return a == 0 ? 8 : (a == 1 ? 30 : k2_issued(7 * (2 * a - 2) + 4)); }
constexpr int k2_wait(int tau) {
  int need = k2_pos_B(tau);
  int s = tau / 7, f = tau % 7;
  if ((s & 1) && f == 2 && (s + 1) / 2 <= 7) { int a = k2_pos_A((s + 1) / 2); need = a > need ? a : need; }
  return k2_issued(tau) - need;
}
#define K2_MFMA mfma_bf
struct K2G { const char* wsp0; const char* wsp; const char* z2; const char* z1; const char* bsp; const char* pf; u32 aoff[2]; u32 boff; u32 adst; u32 dump; u32 tb; int arot; int w; };
template <int P> DEV void k2_a1(const K2G& g) {
  constexpr int SS = P / 8;
  const int i = (P % 8) ^ g.arot;
  dma16(g.wsp + SS * 128 + (size_t)i * 8 * 4096, g.aoff[P & 1], g.adst + (SS & 1) * 8192 + ((i ^ (2 * g.w)) * 1024));
}
template <int A, int B> DEV void k2_as(const K2G& g) { if constexpr (A < B) { k2_a1<A>(g); k2_as<A + 1, B>(g); } }
template <int N> DEV void k2_b1(const K2G& g, u32x4 (&bf)[K2_R]) {
 gload16<(N % 8 - 4) * 1024>(bf[N % K2_R], g.z2 + (N / 8) * 8192, g.boff); }
template <int A, int B> DEV void k2_bs(const K2G& g, u32x4 (&bf)[K2_R]) { if constexpr (A < B) { k2_b1<A>(g, bf); k2_bs<A + 1, B>(g, bf); } }
template <int I> DEV u32x4 k2_read_a(const char* ap0, const char* ap1, int t) {
  return *(const u32x4*)(((I & 1) ? ap1 : ap0) + ((I >> 1) & 1) * 8192 + t * 2048);
}
template <int TAU>
DEV void k2_group(const K2G& g, u32x4 (&bf)[K2_R], u32x4 (&af)[2][4], const char* ap0, const char* ap1, f32x4 (&acc)[4][7], u32x4 (&ep)[4], u32& bw, u32 (&tch)[2]) {
  constexpr int S = TAU / 7, F = TAU % 7;
  wait_vm<k2_wait(TAU)>();
  pin(bf[TAU % K2_R]);
  if constexpr (S < 15 && F >= 2 && F < 6) af[(S + 1) & 1][F - 2] = k2_read_a<S + 1>(ap0, ap1, F - 2);
  SB();
#pragma unroll
  for (int t = 0; t < 4; t++) acc[t][F] = K2_MFMA(bf[TAU % K2_R], af[S & 1][t], acc[t][F]);
  SB();
  if constexpr (TAU + K2_R < 112) k2_b1<TAU + K2_R>(g, bf);
  if constexpr (k2_apiece(TAU) >= 0) k2_a1<k2_apiece(TAU)>(g);
  if constexpr (k2_touch_at(TAU)) { keep(tch[(TAU / 14) & 1]); gload4(tch[(TAU / 14) & 1], g.z2 + (S + K2_TD) * 7168 - 4096, g.tb); }
  if constexpr (TAU == 111) { keep(tch[0]); keep(tch[1]); }
  if constexpr (TAU == K2_EPI_AT) {
    gload16<0>(ep[0], g.z1, g.boff); gload16<1024>(ep[1], g.z1, g.boff); gload16<2048>(ep[2], g.z1, g.boff); gload16<3072>(ep[3], g.z1, g.boff);
    gload4(bw, g.bsp, (g.boff >> 3) & 28u);
#pragma unroll
    for (int i = 0; i < K2_PF; i++) dma16(g.pf + i * 1024, g.boff, g.dump);
  }
  if constexpr (TAU < 111) k2_group<TAU + 1>(g, bf, af, ap0, ap1, acc, ep, bw, tch);
}

DEV void k2_gemm(char* smem, int b, const u16* wsp, const u16* bsp, const u16* wout, char* ws) {
  STAMP_DECL; STAMP(0);
  const int xcd = b & 7, idx = b >> 3;
  const int tp = (xcd >> 1) * 8 + (idx >> 2), fp = (xcd & 1) * 4 + (idx & 3);
  const int w = __builtin_amdgcn_readfirstlane(threadIdx.x >> 6), l = threadIdx.x & 63;
  const int rho = l & 15, q = l >> 4;
  const int tt = tp * 4 + w;
  char* const reg = smem + w * 16384;
  K2G g;
  g.wsp = (const char*)wsp + (size_t)tp * 64 * 4096 + w * 1024;
  g.wsp0 = (const char*)wsp + (size_t)tp * 64 * 4096 + w * 65536;
  g.z2 = ws + WS_Z2F + ((size_t)(fp * 64 + w * 16) * 7 + 4) * 1024;
  g.z1 = ws + WS_Z1F + ((size_t)tt * 28 + ((fp * 7) >> 1)) * 1024;
  g.bsp = (const char*)bsp + tt * 32;
  {
    int pc = (idx * 4 + w) * 7;
    pc = pc > 784 - 7 ? 784 - 7 : pc;
    g.pf = (const char*)wout + pc * 1024;
  }
  g.aoff[0] = (u32)((l >> 3) * 4096 + (((l & 7) ^ (l >> 4)) * 16));
  g.aoff[1] = (u32)((l >> 3) * 4096 + (((l & 7) ^ ((l >> 4) | 4)) * 16));
  g.boff = l * 16;
  g.adst = lds_addr(reg);
  g.dump = lds_addr(smem) + 65536 + w * 1024;
  g.w = w;
  g.arot = (idx & 3) * 2;
  u32x4 bf[K2_R], af[2][4], ep[4];
  u32 bw, tch[2];
  g.tb = (u32)(((idx >> 2) * 14 + (l < 14 ? l : 13)) * 128);
  tch[0] = tch[1] = 0u;
  k2_as<0, 8>(g); k2_bs<0, 14>(g, bf); k2_as<8, 16>(g); k2_bs<14, K2_R>(g, bf);
  const char* ap0 = reg + rho * 128 + (((0 + q) ^ (rho >> 1)) * 16);
  const char* ap1 = reg + rho * 128 + (((4 + q) ^ (rho >> 1)) * 16);
  f32x4 acc[4][7] = {};
  STAMP(1);
  wait_vm<K2_PRO - 8>();
  STAMP(2);
#pragma unroll
  for (int t = 0; t < 4; t++) af[0][t] = k2_read_a<0>(ap0, ap1, t);
  k2_group<0>(g, bf, af, ap0, ap1, acc, ep, bw, tch);
  STAMP(3);
#pragma unroll
  for (int f = 0; f < 7; f++) {
    *(f32x4*)(reg + f * 1024 + l * 16) = acc[1][f];
    *(f32x4*)(reg + (7 + f) * 1024 + l * 16) = acc[3][f];
  }
  bar();
  STAMP(4);
  char* const in1 = smem + (w ^ 1) * 16384;
#pragma unroll
  for (int f = 0; f < 7; f++) acc[2][f] += *(const f32x4*)(in1 + (7 + f) * 1024 + l * 16);
#pragma unroll
  for (int f = 0; f < 7; f++) *(f32x4*)(in1 + (7 + f) * 1024 + l * 16) = acc[2][f];
#pragma unroll
  for (int f = 0; f < 7; f++) acc[0][f] += *(const f32x4*)(in1 + f * 1024 + l * 16);
  bar();
  char* const in2 = smem + (w ^ 3) * 16384;
#pragma unroll
  for (int i = 0; i < 1; i++) wait_vm<K2_PF>();
#pragma unroll
  for (int i = 0; i < 4; i++) pin(ep[i]);
  asm volatile("" : "+v"(bw));
  const float bs = (rho & 1) ? bfhi(bw) : bflo(bw);
  STAMP(5);
  const int odd = fp & 1;
  u32x2 hv[8];
#pragma unroll
  for (int f = 0; f < 7; f++) {
    f32x4 v = acc[0][f] + *(const f32x4*)(in2 + (7 + f) * 1024 + l * 16);
    v = v + bs;
    const u32x4 ze = ep[f >> 1], zo = ep[(f + 1) >> 1];
    const u32 za = odd ? ((f & 1) ? zo.x : zo.z) : ((f & 1) ? ze.z : ze.x);
    const u32 zb = odd ? ((f & 1) ? zo.y : zo.w) : ((f & 1) ? ze.w : ze.y);
    hv[f] = (u32x2){pk_bf16(v.x * bflo(za), v.y * bfhi(za)), pk_bf16(v.z * bflo(zb), v.w * bfhi(zb))};
  }
  const u32 hb = (u32)(WS_HF + (tt * 28 + ((fp * 7) >> 1)) * 1024 + l * 16);
  if (odd) {
    gstore8(ws, hb + 8, hv[0]);
#pragma unroll
    for (int i = 0; i < 3; i++) gstore16(ws, hb + (i + 1) * 1024, (u32x4){hv[1 + 2 * i].x, hv[1 + 2 * i].y, hv[2 + 2 * i].x, hv[2 + 2 * i].y});
  } else {
#pragma unroll
    for (int i = 0; i < 3; i++) gstore16(ws, hb + i * 1024, (u32x4){hv[2 * i].x, hv[2 * i].y, hv[1 + 2 * i].x, hv[1 + 2 * i].y});
    gstore8(ws, hb + 3 * 1024, hv[6]);
  }
  STAMP(6); STAMP_FLUSH(1);
}

constexpr int KG_ROW = 272, KG_WAVE = 53 * KG_ROW;
constexpr int CV_CS = 4160, CV_T = 8 * CV_CS, CV_U = 2 * CV_T, CV_CMP = CV_U + 2 * 4608;
DEV f32x2 cexp_pow2(float gl, float fh, float fl, float p) {
  float m = __builtin_amdgcn_exp2f(gl * p);
  float r = __builtin_amdgcn_fractf(fh * p) + fl * p;
  return (f32x2){m * __builtin_amdgcn_cosf(r), m * __builtin_amdgcn_sinf(r)};
}
DEV float exp_acc(float x) {
  const float h = x * 1.44269504089f;
  const float lo = fmaf(x, 1.44269504089f, -h) + x * 1.925963e-8f;
  return __builtin_amdgcn_exp2f(h) * fmaf(lo, 0.69314718056f, 1.f);
}
struct KGen { f32x2 e1, e2, e4, e8, e16, e32, e64, e128, e256, e512, e1024, e2048, c0; };
DEV KGen kg_gens(float la, float ai, float cr, float ci, float dt, float sc) {
  const float zr = -rbf(exp_acc(la)) * dt, zi = ai * dt;
  const float gl = zr * 1.44269504089f;
  const float fh = zi * 0.15915494309f;
  const float fl = fmaf(zi, 0.15915494309f, -fh) + zi * 6.4206382e-9f;
  KGen k;
  k.c0 = (f32x2){cr * sc, ci * sc};
  k.e1 = cexp_pow2(gl, fh, fl, 1.f); k.e2 = csqr(k.e1); k.e4 = csqr(k.e2); k.e8 = csqr(k.e4);
  k.e16 = cexp_pow2(gl, fh, fl, 16.f); k.e32 = csqr(k.e16); k.e64 = csqr(k.e32); k.e128 = csqr(k.e64);
  k.e256 = cexp_pow2(gl, fh, fl, 256.f); k.e512 = csqr(k.e256); k.e1024 = csqr(k.e512); k.e2048 = csqr(k.e1024);
  return k;
}
DEV void kg_tables(const KGen& k, u32* tw) {
#define TW(row, v) tw[(row) * (KG_ROW / 4)] = (v)
  {
    f32x2 tb[16];
    tb[0] = (f32x2){128.f, 0.f};
    tb[1] = k.e8 * 128.f;
    tb[2] = k.e16 * 128.f; tb[3] = cmul(tb[1], k.e16);
    tb[4] = k.e32 * 128.f;
#pragma unroll
    for (int i = 1; i < 4; i++) tb[4 + i] = cmul(tb[i], k.e32);
    tb[8] = k.e64 * 128.f;
#pragma unroll
    for (int i = 1; i < 8; i++) tb[8 + i] = cmul(tb[i], k.e64);
    TW(0, pk_f16(128.f, 0.f));
    TW(16, pk_f16(k.e128.x * 128.f, k.e128.y * 128.f));
#pragma unroll
    for (int i = 1; i < 16; i++) { TW(i, pk_f16(tb[i].x, tb[i].y)); f32x2 u = cmul(tb[i], k.e128); TW(16 + i, pk_f16(u.x, u.y)); }
  }
  {
    f32x2 a[21];
    a[0] = k.c0; a[1] = cmul(k.c0, k.e1); a[2] = cmul(k.c0, k.e2); a[3] = cmul(a[1], k.e2);
#pragma unroll
    for (int i = 0; i < 4; i++) a[4 + i] = cmul(a[i], k.e4);
#pragma unroll
    for (int i = 0; i < 4; i++) a[8 + i] = cmul(a[2 * i], k.e256);
    a[12] = cmul(a[0], k.e512); a[13] = cmul(a[4], k.e512); a[14] = cmul(a[8], k.e512); a[15] = cmul(a[10], k.e512);
    a[16] = cmul(a[0], k.e1024); a[17] = cmul(a[8], k.e1024); a[18] = cmul(a[12], k.e1024); a[19] = cmul(a[14], k.e1024);
    a[20] = cmul(a[0], k.e2048);
#pragma unroll
    for (int i = 0; i < 21; i++) TW(32 + i, pk_f16(a[i].x, -a[i].y));
  }
#undef TW
}
template <int CTRL> DEV u32 dpp(u32 old, u32 src) { return __builtin_amdgcn_update_dpp(old, src, CTRL, 0xf, 0xf, false); }
template <int CTRL> DEV float dpp_add(float v) { return v + __uint_as_float(__builtin_amdgcn_update_dpp(0u, __float_as_uint(v), CTRL, 0xf, 0xf, true)); }
DEV void cv_frags(u32x4 (&A)[8], u32x4 (&B)[8], const char* pa, const char* pb) {
#pragma unroll
  for (int i = 0; i < 8; i++) {
    A[i] = *(const u32x4*)(pa - 512 * i);
    B[i] = *(const u32x4*)(pb + 512 * i);
  }
}
DEV void cv_mma(const u32x4 (&A)[8], const u32x4 (&B)[8], f32x4 (&acc)[8]) {
#pragma unroll
  for (int a = 0; a < 8; a++)
#pragma unroll
    for (int s = 7; s >= a; s--) acc[s] = mfma_h(A[a], B[s - a], acc[s]);
}
#define SSM_PRIO 0
DEV void k2_ssm(char* smem, int cb, const u16* lar, const u16* aim, const u16* cre, const u16* cim, const u16* ldt, const u16* dsk, char* ws) {
  STAMP_DECL; STAMP(0);
  __builtin_amdgcn_s_setprio(3);
  const int w = __builtin_amdgcn_readfirstlane(threadIdx.x >> 6), l = threadIdx.x & 63;
  const int ch = w >> 1, d = cb * 2 + ch, hw = w & 1, t = hw * 64 + l;
  const int rho = l & 15, q = l >> 4;
  const size_t o = (size_t)d * NMODE;
  const u32 vla = *(const u32*)(lar + o + hw * 128 + 2 * l), vai = *(const u32*)(aim + o + hw * 128 + 2 * l);
  const u32 vcr = *(const u32*)(cre + o + hw * 128 + 2 * l), vci = *(const u32*)(cim + o + hw * 128 + 2 * l);
  const u32 ocr = *(const u32*)(cre + o + (hw ^ 1) * 128 + 2 * l), oci = *(const u32*)(cim + o + (hw ^ 1) * 128 + 2 * l);
  const u32 vdt = ldt[d], vdk = dsk[d];
  char* const T = smem + ch * CV_T;
  char* const U = smem + CV_U + ch * 4608;
  u16* const cmp = (u16*)(smem + CV_CMP + ch * 1408);
#pragma unroll
  for (int i = 0; i < 2; i++) dma16(ws + WS_UT + (size_t)d * (SEQ * 2) + (hw * 2 + i) * 1024, l * 16, lds_addr(U) + 512 + (hw * 2 + i) * 1024);
  __builtin_amdgcn_s_setprio(SSM_PRIO);
  if (t < 32) *(u32x4*)(U + t * 16) = (u32x4){0u, 0u, 0u, 0u};
  u32 gi[2][8];
#pragma unroll
  for (int c = 0; c < 2; c++)
#pragma unroll
    for (int j = 0; j < 4; j++) {
      const int l0 = 2047 - (hw * 128 + c * 64 + l) * 8 - 2 * j, l1 = l0 - 1;
      const u32 bits = pk_bf16((float)l0, (float)l1);
      int i0 = (int)(bits & 0xffffu) - 0x4280, i1 = (int)(bits >> 16) - 0x4280;
      if (c == 1) { i0 = l0 < 128 ? l0 : i0; i1 = l1 < 128 ? l1 : i1; }
      gi[c][2 * j] = i0; gi[c][2 * j + 1] = i1;
    }
  const float dt = rbf(exp_acc(bf2f(vdt)));
  const float dk = bf2f(vdk);
  float S = fabsf(bflo(vcr)) + fabsf(bfhi(vcr)) + fabsf(bflo(vci)) + fabsf(bfhi(vci));
  float S2 = fabsf(bflo(ocr)) + fabsf(bfhi(ocr)) + fabsf(bflo(oci)) + fabsf(bfhi(oci));
  S = hw ? S2 + S : S + S2;
  S = dpp_add<0xb1>(S); S = dpp_add<0x4e>(S); S = dpp_add<0x141>(S); S = dpp_add<0x140>(S);
  S = xsum32(xsum16(S));
  int e = (int)((__float_as_uint(S) >> 23) & 255u) - 125;
  e = e < -100 ? -100 : (e > 100 ? 100 : e);
  e = __builtin_amdgcn_readfirstlane(e);
  const float sc = __uint_as_float((u32)(127 + 8 - e) << 23);
  const float ks = dt * __uint_as_float((u32)(127 + e - 14) << 23);
  STAMP(1);
  char* tab = smem + w * KG_WAVE;
  f32x16 kacc = {};
  {
    const int r = l & 31, h = l >> 5;
    const char* pa = tab + (32 + (r < 21 ? r : 20)) * KG_ROW + h * 16;
    const char* pb = tab + r * KG_ROW + h * 16;
    const KGen k0 = kg_gens(bflo(vla), bflo(vai), bflo(vcr), bflo(vci), dt, sc);
    STAMP(8);
    kg_tables(k0, (u32*)(tab + l * 4));
    SB();
    STAMP(9);
    u32x4 fa[8], fb[8];
#pragma unroll
    for (int kk = 0; kk < 8; kk++) { fa[kk] = *(const u32x4*)(pa + kk * 32); fb[kk] = *(const u32x4*)(pb + kk * 32); }
    SB();
    const KGen k1 = kg_gens(bfhi(vla), bfhi(vai), bfhi(vcr), bfhi(vci), dt, sc);
#pragma unroll
    for (int kk = 0; kk < 8; kk++) kacc = mfma32_h(fa[kk], fb[kk], kacc);
    STAMP(10);
    kg_tables(k1, (u32*)(tab + l * 4));
    STAMP(11);
#pragma unroll
    for (int kk = 0; kk < 8; kk++) kacc = mfma32_h(*(const u32x4*)(pa + kk * 32), *(const u32x4*)(pb + kk * 32), kacc);
    STAMP(12);
    float* give = (float*)tab + l;
    float keep[6];
#pragma unroll
    for (int i = 0; i < 6; i++) { give[i * 64] = hw ? kacc[i] : kacc[6 + i]; keep[i] = hw ? kacc[6 + i] : kacc[i]; }
    const int b1 = h ? 384 + 2 * r : 256 + 4 * r, b2 = h ? (r == 0 ? 640 : 672) : 512 + r;
    const int x0 = hw ? b1 + (h ? 64 : 2) : 4 * h + 8 * r;
    const int x1 = hw ? b2 : x0 + 2, d1 = hw ? (h ? (r == 0 ? 32 : 0) : 32) : 1;
    const int x2 = hw ? (h ? 672 : b2 + 64) : b1, d2 = hw ? (h ? 0 : 32) : 1;
    bar();
    const float* take = (const float*)(smem + (w ^ 1) * KG_WAVE) + l;
    const int xi[6] = {x0, x0 + 1, x1, x1 + d1, x2, x2 + d2};
#pragma unroll
    for (int i = 0; i < 6; i++) {
      const float o = take[i * 64];
      cmp[xi[i]] = __builtin_bit_cast(u16, (_Float16)(hw ? o + keep[i] : keep[i] + o));
    }
  }
  STAMP(2);
  bar();
  {
    u32 ra[3][4];
#pragma unroll
    for (int c = 0; c < 2; c++)
#pragma unroll
      for (int j = 0; j < 4; j++) ra[c][j] = (u32)cmp[gi[c][2 * j]] | ((u32)cmp[gi[c][2 * j + 1]] << 16);
    {
      const u32 k8 = hw ? 0u : cmp[512], k4 = hw ? 0u : cmp[511], k0 = hw ? 0u : cmp[510];
      ra[2][0] = k8 * 0x10001u; ra[2][1] = k4 * 0x10001u; ra[2][2] = k4 | (k0 << 16); ra[2][3] = k0 * 0x10001u;
    }
#pragma unroll
    for (int c = 0; c < 2; c++) {
      u32 dw[8], od[7];
#pragma unroll
      for (int j = 0; j < 4; j++) {
        dw[j] = ra[c][j];
        dw[4 + j] = dpp<0x130>(c == 0 ? dpp<0x134>(0u, ra[1][j]) : ra[2][j], ra[c][j]);
      }
#pragma unroll
      for (int i = 0; i < 7; i++) od[i] = __builtin_amdgcn_alignbit(dw[i + 1], dw[i], 16);
      char* tp = T + (hw * 128 + c * 64 + l) * 16;
#pragma unroll
      for (int k = 0; k < 4; k++) {
        *(u32x4*)(tp + (2 * k) * CV_CS) = (u32x4){dw[k], dw[k + 1], dw[k + 2], dw[k + 3]};
        *(u32x4*)(tp + (2 * k + 1) * CV_CS) = (u32x4){od[k], od[k + 1], od[k + 2], od[k + 3]};
      }
    }
    if (t < 8) *(u32x4*)(T + t * CV_CS + 4096) = (u32x4){0u, 0u, 0u, 0u};
  }
  wait_vm<0>();
  bar();
  STAMP(3);
  const char* pa = T + (7 - (rho & 7)) * CV_CS + (255 - (rho >> 3) + q) * 16 - 32 - 256 * hw;
  const char* pb = U + 512 + rho * 32 + q * 16 - 32 - 256 * hw;
  f32x4 acc[8] = {};
  {
    u32x4 A0[8], B0[8], A1[8], B1[8];
    cv_frags(A0, B0, pa, pb);
    SB(); cv_frags(A1, B1, pa - 64, pb - 64); SB();
    cv_mma(A0, B0, acc);
    SB(); cv_frags(A0, B0, pa - 128, pb - 128); SB();
    cv_mma(A1, B1, acc);
    SB(); cv_frags(A1, B1, pa - 192, pb - 192); SB();
    cv_mma(A0, B0, acc);
    cv_mma(A1, B1, acc);
  }
  STAMP(4);
  bar();
  {
    char* box = T + hw * 4096;
#pragma unroll
    for (int i = 0; i < 4; i++) {
      const f32x4 give = hw ? acc[i] : acc[4 + i];
      acc[i] = hw ? acc[4 + i] : acc[i];
      *(f32x4*)(box + i * 1024 + l * 16) = give;
    }
  }
  u32x2 ub[4];
#pragma unroll
  for (int i = 0; i < 4; i++) ub[i] = *(const u32x2*)(U + 512 + (256 * (4 * hw + i) + 16 * rho + 4 * q) * 2);
  bar();
  {
    const char* box = T + (hw ^ 1) * 4096;
    u32x2 yv[4];
#pragma unroll
    for (int i = 0; i < 4; i++) {
      f32x4 v = (acc[i] + *(const f32x4*)(box + i * 1024 + l * 16)) * ks;
      f16x2 u0 = as_h2(ub[i].x), u1 = as_h2(ub[i].y);
      yv[i] = (u32x2){pk_bf16(fmaf((float)u0.x, dk, v.x), fmaf((float)u0.y, dk, v.y)), pk_bf16(fmaf((float)u1.x, dk, v.z), fmaf((float)u1.y, dk, v.w))};
    }
#pragma unroll
    for (int i = 0; i < 4; i += 2) {
      auto sx = __builtin_amdgcn_permlane16_swap(yv[i].x, yv[i + 1].x, false, false);
      auto sy = __builtin_amdgcn_permlane16_swap(yv[i].y, yv[i + 1].y, false, false);
      const u32 off = (u32)(512 * (4 * hw + i + (q & 1)) + 32 * rho + 16 * (q >> 1));
      gstore16(ws, (u32)(WS_YT + d * (SEQ * 2)) + off, (u32x4){sx[0], sy[0], sx[1], sy[1]});
    }
  }
  STAMP(5); STAMP_FLUSH(1);
}

extern "C" __global__ NOPK __launch_bounds__(256, 2) void k2(const u16* wsp, const u16* bsp, const u16* wout, const u16* lar, const u16* aim, const u16* cre,
                                             const u16* cim, const u16* ldt, const u16* dsk, char* ws) {
  __shared__ __attribute__((aligned(16))) char smem[81920];
  if (blockIdx.x < 256) k2_gemm(smem, blockIdx.x, wsp, bsp, wout, ws);
  else k2_ssm(smem, blockIdx.x - 256, lar, aim, cre, cim, ldt, dsk, ws);
}

#define K3_LEAD 3
struct K3G { const char* wo; const char* hf; u32 woff; u32 hoff[4]; u32 wdst; const char* yt; u32 yoff; u32 ydst; const char* xb; u32 xo; };
template <int I> DEV void k3_issue(const K3G& g, u32x4 (&hf)[7][4], u32x4 (&xv)[2]) {
  if constexpr (I < 7) {
#pragma unroll
    for (int i = 0; i < 4; i++) dma16(g.wo + (size_t)(I * 32 + i * 8) * (DM * 2), g.woff, g.wdst + (I * 4 + i) * 1024);
#pragma unroll
    for (int t = 0; t < 4; t++) gload16<0>(hf[I][t], g.hf + I * 1024, g.hoff[t]);
  } else if constexpr (I == 7) {
    dma16(g.yt, g.yoff, g.ydst);
    dma16(g.yt + 32 * (SEQ * 2), g.yoff, g.ydst + 1024);
    gload16<0>(xv[0], g.xb, g.xo);
    gload16<0>(xv[1], g.xb + 8 * (DM * 2), g.xo);
  }
}
constexpr int k3_cnt(int units) { return units <= 7 ? 8 * units : 60; }
template <int I> DEV void k3_step(const K3G& g, u32x4 (&hf)[7][4], u32x4 (&xv)[2], const char* const (&wp)[4], f32x4 (&acc)[4][4]) {
  wait_vm<k3_cnt(I + K3_LEAD) - 8 * (I + 1)>();
#pragma unroll
  for (int t = 0; t < 4; t++) pin(hf[I][t]);
  u32x4 wf[4];
#pragma unroll
  for (int p = 0; p < 4; p++) wf[p] = tr_frag(wp[p] + I * 4096, wp[p] + I * 4096 + 2048);
  k3_issue<I + K3_LEAD>(g, hf, xv);
#pragma unroll
  for (int p = 0; p < 4; p++)
#pragma unroll
    for (int t = 0; t < 4; t++) acc[t][p] = mfma_bf(wf[p], hf[I][t], acc[t][p]);
  if constexpr (I < 6) k3_step<I + 1>(g, hf, xv, wp, acc);
}
template <int A, int B> DEV void k3_pro(const K3G& g, u32x4 (&hf)[7][4], u32x4 (&xv)[2]) {
  if constexpr (A < B) { k3_issue<A>(g, hf, xv); k3_pro<A + 1, B>(g, hf, xv); }
}
extern "C" __global__ __launch_bounds__(256, 1) void k3(const u16* x, const u16* wout, u16* out, char* ws) {
  __shared__ __attribute__((aligned(16))) char smem[4 * 28672 + 8192];
  STAMP_DECL; STAMP(0);
  const int b = blockIdx.x, xcd = b & 7, idx = b >> 3;
  const int tp = xcd * 4 + idx / 7, dp = idx % 7;
  const int w = __builtin_amdgcn_readfirstlane(threadIdx.x >> 6), l = threadIdx.x & 63;
  const int rho = l & 15, q = l >> 4, qq = (l >> 2) & 3, pp = l & 3;
  char* const reg = smem + w * 28672;
  char* const Y = smem + 4 * 28672;
  K3G g;
  g.wo = (const char*)wout + (size_t)(w * 224) * (DM * 2) + dp * 128;
  g.hf = ws + WS_HF + ((size_t)tp * 4 * 28 + w * 7) * 1024;
  g.woff = (u32)((l >> 3) * (DM * 2) + (((l & 7) ^ ((l >> 4) << 1)) * 16));
#pragma unroll
  for (int t = 0; t < 4; t++) g.hoff[t] = (u32)((t ^ w) * 28 * 1024 + l * 16);
  g.wdst = lds_addr(reg);
  g.yt = ws + WS_YT + (size_t)(dp * 64) * (SEQ * 2) + tp * 128 + w * 32;
  g.yoff = (u32)((l >> 1) * (SEQ * 2) + (l & 1) * 16);
  g.ydst = lds_addr(Y) + w * 2048;
  g.xo = (u32)((l >> 3) * (DM * 2) + (l & 7) * 16);
  const size_t xb = (size_t)(tp * 64 + w * 16) * (DM * 2) + dp * 128;
  g.xb = (const char*)x + xb;
  u32x4 hf[7][4], xv[2];
  k3_pro<0, K3_LEAD>(g, hf, xv);
  const char* wp[4];
  const int sg = ((q & 1) << 1) | (qq >> 1);
#pragma unroll
  for (int p = 0; p < 4; p++) wp[p] = reg + (4 * q + qq) * 128 + ((p ^ sg) * 32) + 8 * pp;
  f32x4 acc[4][4] = {};
  STAMP(1);
  k3_step<0>(g, hf, xv, wp, acc);
  STAMP(2);
#pragma unroll
  for (int t = 1; t < 4; t++)
#pragma unroll
    for (int p = 0; p < 4; p++) *(f32x4*)(reg + t * 4352 + rho * 272 + p * 64 + q * 16) = acc[t][p];
  wait_vm<0>();
  pin(xv[0]); pin(xv[1]);
#pragma unroll
  for (int p = 0; p < 4; p++) {
    u32x2 yv = tr_read(Y + w * 2048 + (16 * p + 4 * q + qq) * 32 + 8 * pp);
    *(f32x4*)(reg + rho * 272 + p * 64 + q * 16) = acc[0][p] + (f32x4){bflo(yv.x), bfhi(yv.x), bflo(yv.y), bfhi(yv.y)};
  }
  bar();
  STAMP(3);
#pragma unroll
  for (int i = 0; i < 2; i++) {
    f32x4 a = {bflo(xv[i].x), bfhi(xv[i].x), bflo(xv[i].y), bfhi(xv[i].y)}, c = {bflo(xv[i].z), bfhi(xv[i].z), bflo(xv[i].w), bfhi(xv[i].w)};
#pragma unroll
    for (int t = 0; t < 4; t++) {
      const char* rp = smem + (w ^ t) * 28672 + t * 4352 + (8 * i + (l >> 3)) * 272 + (l & 7) * 32;
      a += *(const f32x4*)(rp); c += *(const f32x4*)(rp + 16);
    }
    u32x4 o = {pk_bf16(a.x, a.y), pk_bf16(a.z, a.w), pk_bf16(c.x, c.y), pk_bf16(c.z, c.w)};
    gstore16(out, (u32)xb + i * 8 * (DM * 2) + g.xo, o);
  }
  STAMP(4); STAMP_FLUSH(2);
  asm volatile("k3_code_end:");
}

#define K1_GRID 256
#define K2_GRID 480
extern "C" void kernel_launch(void* const* d_in, const int* in_sizes, int n_in, void* d_out, int out_size, void* d_ws, size_t ws_size,
                              hipStream_t stream) {
  const u16* x = (const u16*)d_in[0];
  char* ws = (char*)d_ws;
  Extra ex = {{(const char*)d_out, (const char*)d_in[1], (const char*)d_in[2], (const char*)d_in[3], (const char*)d_in[4], (const char*)d_in[5], (const char*)d_in[6], (const char*)d_in[10]}};
  k1<<<K1_GRID, 512, 0, stream>>>(x, (const u16*)d_in[8], (const u16*)d_in[9], (const u16*)d_in[11], ws, ex);
  k2<<<K2_GRID, 256, 0, stream>>>((const u16*)d_in[9], (const u16*)d_in[10], (const u16*)d_in[11], (const u16*)d_in[1], (const u16*)d_in[2],
                              (const u16*)d_in[3], (const u16*)d_in[4], (const u16*)d_in[5], (const u16*)d_in[6], ws);
  k3<<<224, 256, 0, stream>>>(x, (const u16*)d_in[11], (u16*)d_out, ws);
}
```

```cpp
#define DIAG 0
#include <hip/hip_runtime.h>
#include <stdint.h>

typedef unsigned short u16;
typedef unsigned int u32;
typedef unsigned long long u64;
typedef __attribute__((ext_vector_type(4))) u32 u32x4;
typedef __attribute__((ext_vector_type(2))) u32 u32x2;
typedef __attribute__((ext_vector_type(4))) float f32x4;
typedef __attribute__((ext_vector_type(2))) float f32x2;
typedef __attribute__((ext_vector_type(16))) float f32x16;
typedef __attribute__((ext_vector_type(8))) __bf16 bf16x8;
typedef __attribute__((ext_vector_type(2))) __bf16 bf16x2;
typedef __attribute__((ext_vector_type(8))) _Float16 f16x8;
typedef __attribute__((ext_vector_type(2))) _Float16 f16x2;
typedef __attribute__((ext_vector_type(4))) short s16x4;
#define LDSAS __attribute__((address_space(3)))
#define GAS __attribute__((address_space(1)))
#define DEV __device__ __forceinline__
#if defined(__HIP_DEVICE_COMPILE__)
#define NOPK __attribute__((target("no-packed-fp32-ops")))
#else
#define NOPK
#endif
#define SB() __builtin_amdgcn_sched_barrier(0)

constexpr int SEQ = 2048, DM = 448, NMODE = 256, FF = 1792, FH = 896;

constexpr size_t WS_Z2F = 0;
constexpr size_t WS_Z1F = 4u << 20;
constexpr size_t WS_HF  = 8u << 20;
constexpr size_t WS_UT  = 12u << 20;
constexpr size_t WS_YT  = 14u << 20;
constexpr size_t WS_GR  = 16u << 20;
constexpr size_t WS_EP  = 17u << 20;

#define STAMP(i)
#define STAMP_DECL
#define STAMP_ARG
#define STAMP_PASS
#define STAMP_FLUSH(kern)
DEV float bf2f(u32 v) { return __uint_as_float(v << 16); }
DEV float bflo(u32 v) { return __uint_as_float(v << 16); }
DEV float bfhi(u32 v) { return __uint_as_float(v & 0xffff0000u); }
DEV u32 pk_bf16(float a, float b) { bf16x2 v = {(__bf16)a, (__bf16)b}; return __builtin_bit_cast(u32, v); }
DEV u32 pk_f16(float a, float b) { f16x2 v = {(_Float16)a, (_Float16)b}; return __builtin_bit_cast(u32, v); }
DEV f16x2 as_h2(u32 v) { return __builtin_bit_cast(f16x2, v); }
DEV float rbf(float a) { return __uint_as_float(pk_bf16(a, a) << 16); }
DEV f32x4 mfma_bf(u32x4 a, u32x4 b, f32x4 c) {
  return __builtin_amdgcn_mfma_f32_16x16x32_bf16(__builtin_bit_cast(bf16x8, a), __builtin_bit_cast(bf16x8, b), c, 0, 0, 0);
}
DEV f32x4 mfma_h(u32x4 a, u32x4 b, f32x4 c) {
  return __builtin_amdgcn_mfma_f32_16x16x32_f16(__builtin_bit_cast(f16x8, a), __builtin_bit_cast(f16x8, b), c, 0, 0, 0);
}
DEV f32x16 mfma32_h(u32x4 a, u32x4 b, f32x16 c) {
  return __builtin_amdgcn_mfma_f32_32x32x16_f16(__builtin_bit_cast(f16x8, a), __builtin_bit_cast(f16x8, b), c, 0, 0, 0);
}
DEV u32x2 tr_read(const char* p) {
  s16x4 v = __builtin_amdgcn_ds_read_tr16_b64_v4i16((LDSAS s16x4*)p);
  return __builtin_bit_cast(u32x2, v);
}
DEV u32x4 tr_frag(const char* p0, const char* p1) {
  u32x2 a = tr_read(p0), b = tr_read(p1);
  return (u32x4){a.x, a.y, b.x, b.y};
}
#define LDMOD ""
#define DMAMOD ""
DEV u32 lds_addr(const void* p) { return (u32)(uintptr_t)(LDSAS const char*)p; }
DEV void dma16(const void* sbase, u32 voff, u32 ldst) {
  asm volatile("s_mov_b32 m0, %2\n\ts_nop 0\n\tglobal_load_lds_dwordx4 %0, %1 " DMAMOD ::"v"(voff), "s"(sbase), "s"(ldst) : "memory");
}
template <int OFF> DEV void gload16(u32x4& d, const void* sbase, u32 voff) {
  asm volatile("global_load_dwordx4 %0, %1, %2 offset:%3 " LDMOD : "=v"(d) : "v"(voff), "s"(sbase), "i"(OFF) : "memory");
}
DEV void gstore16(void* sbase, u32 voff, u32x4 d) {
  asm volatile("global_store_dwordx4 %0, %1, %2 sc1\n\ts_nop 1" ::"v"(voff), "v"(d), "s"(sbase) : "memory");
}
DEV void gstore8(void* sbase, u32 voff, u32x2 d) {
  asm volatile("global_store_dwordx2 %0, %1, %2 sc1" ::"v"(voff), "v"(d), "s"(sbase) : "memory");
}
DEV void gstore16p(void* sbase, u32 voff, u32x4 d) {
  asm volatile("global_store_dwordx4 %0, %1, %2\n\ts_nop 1" ::"v"(voff), "v"(d), "s"(sbase) : "memory");
}
#define Z2STORE gstore16
DEV void gload4(u32& d, const void* sbase, u32 voff) {
  asm volatile("global_load_dword %0, %1, %2" : "=v"(d) : "v"(voff), "s"(sbase) : "memory");
}
template <int N> DEV void wait_vm() { asm volatile("s_waitcnt vmcnt(%0)" ::"i"(N) : "memory"); }
DEV void touch(u32& d, const void* p) { asm volatile("global_load_dword %0, %1, off" : "=v"(d) : "v"(p) : "memory"); }
DEV void keep(u32& a) { asm volatile("" : "+v"(a)); }
DEV void pin(u32x4& a) { asm volatile("" : "+v"(a)); }
DEV void bar() { asm volatile("s_waitcnt lgkmcnt(0)\n\ts_barrier" ::: "memory"); }
DEV float xsum16(float v) { auto r = __builtin_amdgcn_permlane16_swap(__float_as_uint(v), __float_as_uint(v), false, false); return __uint_as_float(r[0]) + __uint_as_float(r[1]); }
DEV float xsum32(float v) { auto r = __builtin_amdgcn_permlane32_swap(__float_as_uint(v), __float_as_uint(v), false, false); return __uint_as_float(r[0]) + __uint_as_float(r[1]); }
DEV f32x2 ln_scalars(float sum, float sq, float inv_n) {
  float m = sum * inv_n;
  float v = fmaxf(sq * inv_n - m * m, 0.f);
  float sd = rbf(__builtin_amdgcn_sqrtf(rbf(rbf(v) + 1e-5f)));
  return (f32x2){rbf(m), __builtin_amdgcn_rcpf(sd)};
}
DEV f32x2 gelu2(f32x2 x) {
  const float c1 = -2.3022081983f, c2 = -0.1029432396f;
  f32x2 t = x * x;
  t = t * c2 + c1;
  t = t * x;
  f32x2 e = {__builtin_amdgcn_exp2f(t.x), __builtin_amdgcn_exp2f(t.y)};
  e = e + 1.0f;
  f32x2 r = {__builtin_amdgcn_rcpf(e.x), __builtin_amdgcn_rcpf(e.y)};
  return x * r;
}
DEV f32x2 cmul(f32x2 a, f32x2 b) { return (f32x2){fmaf(a.x, b.x, -(a.y * b.y)), fmaf(a.x, b.y, a.y * b.x)}; }
DEV f32x2 csqr(f32x2 a) { return (f32x2){a.x * a.x - a.y * a.y, 2.f * a.x * a.y}; }

#define K1_PKACT
#define K1_PUBDELAY 8
#define TSTRIDE 65536u
#define K1_PF_STOP 11000u
#define K1_PRIO 2
#define K1_PF 4
constexpr int k1_slot(int u) { return u < 9 ? u : u - 9; }
constexpr int K1_X = 73728, K1_UT = K1_X + 65536, K1_CS = K1_UT + 32 * 272, K1_LN = K1_CS + 512, K1_LN1 = K1_LN + 1024, K1_CNT = K1_LN1 + 1024, K1_DUMP = K1_CNT + 64, K1_LDS = K1_DUMP + 4096;

typedef LDSAS char* lptr;
DEV lptr pinned(lptr p) { asm volatile("" : "+v"(p)); return p; }
DEV u32x4 lread16(lptr p) { return *(LDSAS u32x4*)p; }
DEV u32x4 ltr_frag(lptr p0, lptr p1) {
  s16x4 a = __builtin_amdgcn_ds_read_tr16_b64_v4i16((LDSAS s16x4*)p0), b = __builtin_amdgcn_ds_read_tr16_b64_v4i16((LDSAS s16x4*)p1);
  u32x2 x = __builtin_bit_cast(u32x2, a), y = __builtin_bit_cast(u32x2, b);
  return (u32x4){x.x, x.y, y.x, y.y};
}
DEV void mma(f32x4& c, const u32x4& a, const u32x4& b) { asm volatile("v_mfma_f32_16x16x32_bf16 %0, %1, %2, %0" : "+v"(c) : "v"(a), "v"(b)); }
DEV void mma0(f32x4& c, const u32x4& a, const u32x4& b) { asm volatile("v_mfma_f32_16x16x32_bf16 %0, %1, %2, 0" : "=&v"(c) : "v"(a), "v"(b)); }
template <bool FIRST> DEV void mmaf(f32x4& c, const u32x4& a, const u32x4& b) { if constexpr (FIRST) mma0(c, a, b); else mma(c, a, b); }
DEV void mma_drain() { SB(); asm volatile("s_nop 7\n\ts_nop 1" ::: "memory"); SB(); }
DEV void sbar() { asm volatile("s_barrier" ::: "memory"); }

struct K1L { const char* win; const char* xb; u32 xoff[2]; u32 woff[2]; u32 wdst; u32 xdst; int m0; };
template <int U> DEV void k1_unit(const K1L& g) {
  int line = U % 7 + g.m0; line = line >= 7 ? line - 7 : line;
  if constexpr (U < 7) {
#pragma unroll
    for (int i = 0; i < 4; i++) dma16(g.xb + line * 128 + (i >> 1) * 16 * (DM * 2), g.xoff[i & 1], g.xdst + (U & 3) * 16384 + i * 1024);
  }
#pragma unroll
  for (int i = 0; i < 2; i++) dma16(g.win + (size_t)line * 64 * (FF * 2) + (U < 7 ? FH * 2 : 0), g.woff[i], g.wdst + k1_slot(U) * 8192 + i * 1024);
}
struct K1C { lptr str[4]; lptr wp[4]; };
DEV void k1_xr(const K1C& g, u32x4 (&xf)[4], int M) {
#pragma unroll
  for (int i = 0; i < 4; i++) xf[i] = lread16(g.str[i] + (M & 3) * 16384);
}
template <int U> DEV u32x4 k1_wf(const K1C& g, int f) {
  if constexpr (k1_slot(U) < 8) return ltr_frag(g.wp[f & 3] + k1_slot(U) * 8192 + (f >> 2) * 4096, g.wp[f & 3] + k1_slot(U) * 8192 + (f >> 2) * 4096 + 512);
  else { lptr p = pinned(g.wp[f & 3] + 65536); return ltr_frag(p + (f >> 2) * 4096, p + (f >> 2) * 4096 + 512); }
}
template <int U> DEV void k1_main(const K1C& g, const u32x4 (&xf)[4], u32x4 (&wf)[8], f32x4 (&acc)[2][4], f32x4& cs, const u32x4& ones) {
#pragma unroll
  for (int f = 0; f < 8; f++) {
    if (U % 7 == 0 && f < 4) {
      if (f == 0) mma0(cs, wf[f], ones);
      mma0(acc[0][f & 3], wf[f], xf[0]);
      mma0(acc[1][f & 3], wf[f], xf[1]);
    } else {
      if ((f & 3) == 0) mma(cs, wf[f], ones);
      mma(acc[0][f & 3], wf[f], xf[(f >> 2) * 2]);
      mma(acc[1][f & 3], wf[f], xf[(f >> 2) * 2 + 1]);
    }
    SB(); if constexpr (U % 7 < 6) wf[f] = k1_wf<U + 1>(g, f); SB();
  }
}
template <int M>
DEV void k1_stage(const K1C& g, u32x4 (&xf)[7][4], u32x4 (&wf)[8], f32x4 (&acc)[2][4], f32x4& cs, f32x4 (&rsum)[2], f32x4 (&gram)[2], const u32x4& ones) {
  asm volatile("s_waitcnt lgkmcnt(15)\n\ts_barrier" ::: "memory");
  if constexpr (M < 6) k1_xr(g, xf[M + 1], M + 1);
  SB();
#pragma unroll
  for (int i = 0; i < 4; i++) {
    if (M == 0 && i < 2) { mma0(rsum[i & 1], ones, xf[M][i]); mma0(gram[i & 1], xf[M][i], xf[M][i]); }
    else { mma(rsum[i & 1], ones, xf[M][i]); mma(gram[i & 1], xf[M][i], xf[M][i]); }
  }
  k1_main<M>(g, xf[M], wf, acc, cs, ones);
  if constexpr (M < 6) k1_stage<M + 1>(g, xf, wf, acc, cs, rsum, gram, ones);
}
template <int M>
DEV void k1_z1loop(const K1C& g, u32x4 (&xf)[7][4], u32x4 (&wf)[8], f32x4 (&acc)[2][4], f32x4& cs, const u32x4& ones, const char* pf, u32 l16, u32 dump) {
  if constexpr (M + 1 < K1_PF) dma16(pf + (M + 1) * 1024, l16, dump);
  k1_main<M + 7>(g, xf[M], wf, acc, cs, ones);
  if constexpr (M < 6) k1_z1loop<M + 1>(g, xf, wf, acc, cs, ones, pf, l16, dump);
}
DEV void k1_act(f32x4 (&acc)[2][4], const char* csb, int w, const f32x2 (&ln1)[2]) {
#pragma unroll
  for (int p = 0; p < 4; p++) {
    const f32x4 c = *(const f32x4*)(csb + (p ^ w) * 64);
#pragma unroll
    for (int t = 0; t < 2; t++) {
      f32x2 a = (acc[t][p].xy - c.xy * ln1[t].x) * ln1[t].y;
      f32x2 b = (acc[t][p].zw - c.zw * ln1[t].x) * ln1[t].y;
      a = gelu2(a); b = gelu2(b);
      acc[t][p] = (f32x4){a.x, a.y, b.x, b.y};
    }
  }
}
DEV void act4_stats(f32x4& x, const f32x4& c, float m, float rs, float c1, float& s, float& s2) {
  float x0 = x.x, x1 = x.y, x2 = x.z, x3 = x.w, t0, t1, t2, t3;
  asm("v_fma_f32 %0, -%10, %14, %0\n\tv_fma_f32 %1, -%11, %14, %1\n\tv_fma_f32 %2, -%12, %14, %2\n\tv_fma_f32 %3, -%13, %14, %3\n\t"
      "v_mul_f32 %0, %0, %15\n\tv_mul_f32 %1, %1, %15\n\tv_mul_f32 %2, %2, %15\n\tv_mul_f32 %3, %3, %15\n\t"
      "v_mul_f32 %4, %0, %0\n\tv_mul_f32 %5, %1, %1\n\tv_mul_f32 %6, %2, %2\n\tv_mul_f32 %7, %3, %3\n\t"
      "v_fmamk_f32 %4, %4, 0xbdd2d3e8, %16\n\tv_fmamk_f32 %5, %5, 0xbdd2d3e8, %16\n\tv_fmamk_f32 %6, %6, 0xbdd2d3e8, %16\n\tv_fmamk_f32 %7, %7, 0xbdd2d3e8, %16\n\t"
      "v_mul_f32 %4, %4, %0\n\tv_mul_f32 %5, %5, %1\n\tv_mul_f32 %6, %6, %2\n\tv_mul_f32 %7, %7, %3\n\t"
      "v_exp_f32 %4, %4\n\tv_exp_f32 %5, %5\n\tv_exp_f32 %6, %6\n\tv_exp_f32 %7, %7\n\t"
      "v_add_f32 %4, 1.0, %4\n\tv_add_f32 %5, 1.0, %5\n\tv_add_f32 %6, 1.0, %6\n\tv_add_f32 %7, 1.0, %7\n\t"
      "v_rcp_f32 %4, %4\n\tv_rcp_f32 %5, %5\n\tv_rcp_f32 %6, %6\n\tv_rcp_f32 %7, %7\n\t"
      "v_mul_f32 %0, %0, %4\n\tv_mul_f32 %1, %1, %5\n\tv_mul_f32 %2, %2, %6\n\tv_mul_f32 %3, %3, %7\n\t"
      "v_add_f32 %4, %0, %1\n\tv_fmac_f32 %9, %0, %0\n\tv_add_f32 %5, %2, %3\n\tv_fmac_f32 %9, %1, %1\n\t"
      "v_add_f32 %8, %8, %4\n\tv_fmac_f32 %9, %2, %2\n\tv_add_f32 %8, %8, %5\n\tv_fmac_f32 %9, %3, %3"
      : "+v"(x0), "+v"(x1), "+v"(x2), "+v"(x3), "=&v"(t0), "=&v"(t1), "=&v"(t2), "=&v"(t3), "+v"(s), "+v"(s2)
      : "v"(c.x), "v"(c.y), "v"(c.z), "v"(c.w), "v"(m), "v"(rs), "v"(c1));
  x = (f32x4){x0, x1, x2, x3};
}
DEV int lane_id() { int l = __builtin_amdgcn_mbcnt_hi(~0u, __builtin_amdgcn_mbcnt_lo(~0u, 0u)); asm volatile("" : "+v"(l)); return l; }
DEV void quad_sync(lptr cnt, int l) {
  asm volatile("s_waitcnt lgkmcnt(0)" ::: "memory");
  if (l == 0) __hip_atomic_fetch_add((LDSAS u32*)cnt, 1u, __ATOMIC_RELAXED, __HIP_MEMORY_SCOPE_WORKGROUP);
  while (__builtin_amdgcn_readfirstlane(*(volatile LDSAS u32*)cnt) < 4u) __builtin_amdgcn_s_sleep(0);
  asm volatile("" ::: "memory");
}
DEV void lbar0() { asm volatile("s_waitcnt vmcnt(6) lgkmcnt(0)\n\ts_barrier" ::: "memory"); }

struct Extra { const char* p[8]; };
DEV void k1_body(char* smem, const u16* x, const u16* win, const u16* wsp, const u16* wout, char* ws, const Extra& ex) {
  STAMP_DECL; STAMP(0);
  const int b = blockIdx.x, xcd = b & 7, idx = b >> 3;
  const int w8 = __builtin_amdgcn_readfirstlane(threadIdx.x >> 6), l = threadIdx.x & 63, w = w8 & 3;
  if (b >= 224) {
    const int j = (idx - 28) * 8 + w8;
    const u32 t_start = (u32)__builtin_amdgcn_s_memtime();
    const char* p = (const char*)wsp + ((size_t)xcd * 1024 + 112 * K1_PF) * 1024;
    constexpr int REST = 1024 - 112 * K1_PF;
    u32 dc;
    {
      u64 c0, c1;
      asm volatile("s_getpc_b64 vcc\n\ts_add_u32 vcc_lo, vcc_lo, k1@rel32@lo+4\n\ts_addc_u32 vcc_hi, vcc_hi, k1@rel32@hi+12\n\ts_mov_b64 %0, vcc" : "=s"(c0) :: "scc", "vcc");
      asm volatile("s_getpc_b64 vcc\n\ts_add_u32 vcc_lo, vcc_lo, k3_code_end@rel32@lo+4\n\ts_addc_u32 vcc_hi, vcc_hi, k3_code_end@rel32@hi+12\n\ts_mov_b64 %0, vcc" : "=s"(c1) :: "scc", "vcc");
      const u32 len = (u32)(c1 - c0) & ~3u, o = (u32)(j * 64 + l) * 128u;
      gload4(dc, (const void*)c0, j < 8 ? (o < len ? o : len - 4u) : 0u);
    }
    {
      u32 d0, d1, d2, d3;
      const int n = j * 64 + l;
      const char* p = n < 448 ? ex.p[0] + n * 4096 : (n < 672 ? ex.p[1 + (n - 448) / 56] + ((n - 448) % 56) * 4096 : ex.p[5 + (n % 3)]);
      asm volatile("" : "+v"(p));
      gload4(d0, ws, (u32)((n * TSTRIDE) % (18u << 20)));
      gload4(d1, ws, (u32)(((n + 2048) * TSTRIDE) % (18u << 20)));
      gload4(d2, ws, (u32)(((n + 4096) * TSTRIDE) % (18u << 20)));
      touch(d3, p);
      wait_vm<0>();
      keep(d0); keep(d1); keep(d2); keep(d3);
      keep(dc);
    }
    const char* po = (const char*)wout + (size_t)(xcd * 98) * 1024;
#pragma unroll
    for (int k = 0; k < 4; k++) { int c = j + 32 * k; c = c < 98 ? c : 97; dma16(po + c * 1024, l * 16, lds_addr(smem) + w8 * 1024); }
#pragma unroll 1
    for (int i = j; i < REST; i += 32) {
      dma16(p + (size_t)i * 1024, l * 16, lds_addr(smem) + w8 * 1024);
      if ((u32)__builtin_amdgcn_s_memtime() - t_start > K1_PF_STOP) break;
    }
    STAMP(1); STAMP_FLUSH(0);
    return;
  }
  const int i7 = (idx * 37) >> 8;
  const int panel = (xcd >> 1) * 4 + i7, sib = (xcd & 1) * 7 + idx - 7 * i7;
  const lptr lds = (lptr)smem;
  K1L g;
  if (w8 >= 4) {
    g.win = (const char*)win + (size_t)(16 * w) * (FF * 2) + sib * 128;
    g.xb = (const char*)x + (size_t)(panel * 128 + w * 32) * (DM * 2);
    g.xoff[0] = (u32)((l >> 3) * (DM * 2) + (((l & 7) ^ (l >> 4)) * 16));
    g.xoff[1] = (u32)((8 + (l >> 3)) * (DM * 2) + (((l & 7) ^ ((l >> 4) | 4)) * 16));
    g.woff[0] = (u32)((l >> 3) * (FF * 2) + (((l & 7) ^ (((l >> 4) & 1) << 1)) * 16));
    g.woff[1] = (u32)((8 + (l >> 3)) * (FF * 2) + (((l & 7) ^ (((l >> 4) & 1) << 1) ^ 4) * 16));
    g.xdst = lds_addr(smem) + K1_X + w * 4096;
    g.wdst = lds_addr(smem) + w * 2048;
    g.m0 = sib >> 1;
    k1_unit<0>(g); k1_unit<1>(g);
  }
  K1C c;
  {
    const int rho = l & 15, q = l >> 4, qq = (l >> 2) & 3, pp = l & 3;
#pragma unroll
    for (int i = 0; i < 4; i++) c.str[i] = pinned(lds + K1_X + w * 4096 + (i & 1) * 2048 + rho * 128 + (((4 * (i >> 1) + q) ^ (rho >> 1)) * 16));
    const int sg = (qq >> 1) | ((q & 1) << 1);
#pragma unroll
    for (int f = 0; f < 4; f++) c.wp[f] = lds + (8 * q + qq) * 128 + (((f ^ w) ^ sg) * 32) + 8 * pp;
  }
  u32x4 ones = {0x3f803f80u, 0x3f803f80u, 0x3f803f80u, 0x3f803f80u};
  pin(ones);
  u32x4 xf[7][4], wf[8];
  f32x4 acc[2][4], cs;
  if (w8 >= 4) {
    lbar0();
    k1_unit<2>(g); k1_xr(c, xf[0], 0); lbar0();
    k1_unit<3>(g); k1_xr(c, xf[1], 1); lbar0();
    k1_unit<4>(g); k1_xr(c, xf[2], 2); lbar0();
    k1_unit<5>(g); k1_xr(c, xf[3], 3); lbar0();
    k1_unit<6>(g); k1_xr(c, xf[4], 4); lbar0();
    k1_unit<7>(g); k1_unit<8>(g); k1_unit<9>(g); k1_xr(c, xf[5], 5); lbar0();
    k1_unit<10>(g); k1_unit<11>(g); k1_unit<12>(g); k1_xr(c, xf[6], 6);
    asm volatile("s_waitcnt lgkmcnt(0)\n\ts_barrier" ::: "memory");
    k1_unit<13>(g);
    STAMP(1);
    const char* pf = (const char*)wsp + ((size_t)xcd * 1024 + (idx * 4 + w) * K1_PF) * 1024;
    const u32 l16 = l * 16, dump = lds_addr(smem) + K1_DUMP + w * 1024;
    if constexpr (K1_PF > 0) dma16(pf, l16, dump);
    wait_vm<(K1_PF > 0 ? 1 : 0)>(); sbar();
    STAMP(2);
    f32x2 ln1[2];
    {
    const int rho = l & 15, q = l >> 4;
#pragma unroll
    for (int t = 0; t < 2; t++) ln1[t] = *(const f32x2*)(smem + K1_LN1 + w * 256 + (t * 16 + rho) * 8);
#pragma unroll
    for (int f = 0; f < 8; f++) wf[f] = k1_wf<7>(c, f);
#pragma unroll
    for (int t = 0; t < 2; t++) {
      const u32x4 va = xf[0][t], vb = xf[0][2 + t];
      const u32 msk = (sib & 1) ? 0xffffffffu : 0u;
      const u32x4 v = (va & ~msk) | (vb & msk);
      u16* ub = (u16*)(smem + K1_UT + (8 * q) * 272 + (w * 32 + t * 16 + rho) * 2);
#pragma unroll
      for (int j = 0; j < 4; j++) {
        float a = rbf(rbf(bflo(v[j]) - ln1[t].x) * ln1[t].y), bb = rbf(rbf(bfhi(v[j]) - ln1[t].x) * ln1[t].y);
        u32 o = pk_f16(a, bb);
        ub[(2 * j) * 136] = (u16)o;
        ub[(2 * j + 1) * 136] = (u16)(o >> 16);
      }
    }
}
    SB();
    k1_z1loop<0>(c, xf, wf, acc, cs, ones, pf, l16, dump);
    mma_drain();
    const int l = lane_id(), rho = l & 15, q = l >> 4;
    if (rho == 0) *(f32x4*)(smem + K1_CS + 256 + w * 64 + q * 16) = cs;
    STAMP(3);
    quad_sync(lds + K1_CNT, l);
    STAMP(4);
#pragma unroll
    for (int i = 0; i < 2; i++) {
      const u32x4 uo = *(const u32x4*)(smem + K1_UT + (w * 8 + i * 4 + q) * 272 + rho * 16);
      gstore16(ws, (u32)(WS_UT + (sib * 32 + w * 8 + i * 4 + q) * (SEQ * 2) + panel * 256 + rho * 16), uo);
    }
    k1_act(acc, smem + K1_CS + 256 + q * 16, w, ln1);
#pragma unroll
    for (int t = 0; t < 2; t++)
#pragma unroll
      for (int cc = 0; cc < 2; cc++) {
        u32x2 a = {pk_bf16(acc[t][2 * cc].x, acc[t][2 * cc].y), pk_bf16(acc[t][2 * cc].z, acc[t][2 * cc].w)};
        u32x2 bb = {pk_bf16(acc[t][2 * cc + 1].x, acc[t][2 * cc + 1].y), pk_bf16(acc[t][2 * cc + 1].z, acc[t][2 * cc + 1].w)};
        const u32x4 o = (w & 1) ? (u32x4){bb.x, bb.y, a.x, a.y} : (u32x4){a.x, a.y, bb.x, bb.y};
        gstore16(ws, (u32)(WS_Z1F + ((panel * 8 + w * 2 + t) * 28 + sib * 2 + (cc ^ (w >> 1))) * 1024 + l * 16), o);
      }
    STAMP(5); STAMP_FLUSH(0);
    return;
  }
  const int grp = panel * 4 + w;
  u32 tch;
  {
    const int h = l >> 5, n = (w & 1) * 32 + (l & 31);
    (void)h;
    const int wl = panel * 56 + (n < 56 ? n : 55);
    const u32 xo = (u32)(panel * 128 * (DM * 2) + (sib * 64 + n) * 128);
    const u32 wo = (u32)((wl >> 1) * (FF * 2) + ((wl & 1) ? 0 : FH * 2) + sib * 128);
    gload4(tch, w < 2 ? (const void*)x : (const void*)win, w < 2 ? xo : wo);
  }
  u32 tag;
  asm volatile("s_load_dword %0, %1, 0x0\n\ts_waitcnt lgkmcnt(0)" : "=s"(tag) : "s"(ws + WS_EP + grp * 256) : "memory");
  tag += 1u;
  f32x4 rsum[2], gram[2];
  if (threadIdx.x < 2) *(u32*)(smem + K1_CNT + threadIdx.x * 4) = 0u;
  bar();
  STAMP(2);
  k1_xr(c, xf[0], 0);
#pragma unroll
  for (int f = 0; f < 8; f++) wf[f] = k1_wf<0>(c, f);
  k1_stage<0>(c, xf, wf, acc, cs, rsum, gram, ones);
  mma_drain();
  wait_vm<0>(); keep(tch);
  const int rho = l & 15, q = l >> 4, qq = (l >> 2) & 3, pp = l & 3;
  if (rho == 0) *(f32x4*)(smem + K1_CS + w * 64 + q * 16) = cs;
  STAMP(3);
  f32x2 ln1[2];
#pragma unroll
  for (int t = 0; t < 2; t++) {
    float d = (rho & 2) ? ((rho & 1) ? gram[t].w : gram[t].z) : ((rho & 1) ? gram[t].y : gram[t].x);
    d = ((rho >> 2) == q) ? d : 0.f;
    d = xsum32(xsum16(d));
    ln1[t] = ln_scalars(rsum[t].x, d, 1.f / DM);
    if (q == 0) *(f32x2*)(smem + K1_LN1 + w * 256 + (t * 16 + rho) * 8) = ln1[t];
  }
  bar();
  __builtin_amdgcn_s_setprio(K1_PRIO);
  STAMP(4);
  GAS u64* gr = (GAS u64*)(ws + WS_GR) + (size_t)grp * 14 * 64;
  {
    float st[2][2] = {{0.f, 0.f}, {0.f, 0.f}};
    k1_act(acc, smem + K1_CS + q * 16, w, ln1);
#pragma unroll
    for (int t = 0; t < 2; t++) {
      f32x2 s = {0.f, 0.f}, s2 = {0.f, 0.f};
#pragma unroll
      for (int p = 0; p < 4; p++) {
        s += acc[t][p].xy; s += acc[t][p].zw;
        s2 += acc[t][p].xy * acc[t][p].xy; s2 += acc[t][p].zw * acc[t][p].zw;
      }
      st[t][0] = s.x + s.y; st[t][1] = s2.x + s2.y;
    }
#pragma unroll
    for (int t = 0; t < 2; t++) { st[t][0] = xsum32(xsum16(st[t][0])); st[t][1] = xsum32(xsum16(st[t][1])); }
    const float v = (q & 2) ? ((q & 1) ? st[1][1] : st[0][1]) : ((q & 1) ? st[1][0] : st[0][0]);
    __hip_atomic_store(gr + sib * 64 + l, ((u64)tag << 32) | __float_as_uint(v), __ATOMIC_RELAXED, __HIP_MEMORY_SCOPE_AGENT);
  }
  STAMP(5);
  __builtin_amdgcn_s_sleep(K1_PUBDELAY);
  STAMP(6);
  u64 rv[14];
#pragma unroll
  for (int i = 0; i < 14; i++) rv[i] = __hip_atomic_load(gr + i * 64 + l, __ATOMIC_RELAXED, __HIP_MEMORY_SCOPE_AGENT);
  STAMP(7);
  float tot;
  for (u32 spins = 0;; spins++) {
    bool ok = true; tot = 0.f;
#pragma unroll
    for (int i = 0; i < 14; i++) { ok &= (u32)(rv[i] >> 32) == tag; tot += __uint_as_float((u32)rv[i]); }
    if (__all(ok) || spins > (1u << 20)) break;
#pragma unroll
    for (int i = 0; i < 14; i++) rv[i] = __hip_atomic_load(gr + i * 64 + l, __ATOMIC_RELAXED, __HIP_MEMORY_SCOPE_AGENT);
  }
  STAMP(8);
  if (sib == 0 && l == 0) __hip_atomic_store((GAS u32*)(ws + WS_EP + grp * 256), tag, __ATOMIC_RELAXED, __HIP_MEMORY_SCOPE_AGENT);
  {
    auto bb = __builtin_amdgcn_permlane32_swap(__float_as_uint(tot), __float_as_uint(tot), false, false);
    const f32x2 lr = ln_scalars(__uint_as_float(bb[0]), __uint_as_float(bb[1]), 1.f / FH);
    auto lm = __builtin_amdgcn_permlane16_swap(__float_as_uint(lr.x), __float_as_uint(lr.x), false, false);
    auto ls = __builtin_amdgcn_permlane16_swap(__float_as_uint(lr.y), __float_as_uint(lr.y), false, false);
    char* const stg = smem + K1_X + w * 8192;
#pragma unroll
    for (int t = 0; t < 2; t++) {
      const float m2 = __uint_as_float(lm[t]), r2 = __uint_as_float(ls[t]);
#pragma unroll
      for (int p = 0; p < 4; p++) {
        f32x2 a = (acc[t][p].xy - m2) * r2, b2 = (acc[t][p].zw - m2) * r2;
        *(u32x2*)(stg + (16 * t + rho) * 136 + p * 32 + q * 8) = (u32x2){pk_bf16(a.x, a.y), pk_bf16(b2.x, b2.y)};
      }
    }
#pragma unroll
    for (int p = 0; p < 4; p++) {
      const char* rp = stg + (8 * q + qq) * 136 + p * 32 + pp * 8;
      u32x4 o = tr_frag(rp, rp + 4 * 136);
      const int ft = sib * 4 + (p ^ w);
      Z2STORE(ws, (u32)(WS_Z2F + (((ft / 7) * 64 + grp) * 7 + ft % 7) * 1024 + l * 16), o);
    }
  }
  STAMP(9); STAMP_FLUSH(0);
}
#define K1_ATTR
extern "C" __global__ K1_ATTR __launch_bounds__(512, 2) void k1(const u16* x, const u16* win, const u16* wsp, const u16* wout, char* ws, Extra ex) {
  __shared__ __attribute__((aligned(16))) char smem[K1_LDS];
  if (blockIdx.x == 0 && threadIdx.x == 0) __hip_atomic_store((GAS u64*)(ws + (40u << 20)), (u64)__builtin_amdgcn_s_memrealtime(), __ATOMIC_RELAXED, __HIP_MEMORY_SCOPE_AGENT);
  k1_body(smem, x, win, wsp, wout, ws, ex);
}

#define K2_R 18
#define K2_PF 7
#define K2_TD 0
constexpr bool k2_touch_at(int tau) { return K2_TD > 0 && tau % 14 == 0 && tau / 7 + K2_TD <= 14; }
constexpr int K2_EPI = 5, K2_EPI_AT = 112 - K2_R + 2, K2_PRO = 16 + K2_R + (K2_TD > 0 ? 2 : 0);
constexpr int k2_apiece(int tau) {
  int s = tau / 7, f = tau % 7;
  int a = (s + 3) / 2 + ((s & 1) ? 0 : 1) - ((s & 1) ? 0 : 1);
  if (s & 1) { a = (s + 3) / 2; return (a <= 7 && (f & 1) == 0) ? 8 * a + f / 2 : -1; }
  a = (s + 2) / 2;
  return (a >= 2 && a <= 7 && f < 4) ? 8 * a + 4 + f : -1;
}
constexpr int k2_ops_after(int tau) { return (tau + K2_R < 112 ? 1 : 0) + (k2_apiece(tau) >= 0 ? 1 : 0) + (tau == K2_EPI_AT ? K2_EPI + K2_PF : 0) + (k2_touch_at(tau) ? 1 : 0); }
constexpr int k2_issued(int tau) { int n = K2_PRO; for (int i = 0; i < tau; i++) n += k2_ops_after(i); return n; }
constexpr int k2_pos_B(int n) { return n < 14 ? 8 + n + 1 : (n < K2_R ? 16 + n + 1 : k2_issued(n - K2_R) + 1); }
constexpr int k2_pos_A(int a) { return a == 0 ? 8 : (a == 1 ? 30 : k2_issued(7 * (2 * a - 2) + 4)); }
constexpr int k2_wait(int tau) {
  int need = k2_pos_B(tau);
  int s = tau / 7, f = tau % 7;
  if ((s & 1) && f == 2 && (s + 1) / 2 <= 7) { int a = k2_pos_A((s + 1) / 2); need = a > need ? a : need; }
  return k2_issued(tau) - need;
}
#define K2_MFMA mfma_bf
struct K2G { const char* wsp0; const char* wsp; const char* z2; const char* z1; const char* bsp; const char* pf; u32 aoff[2]; u32 boff; u32 adst; u32 dump; u32 tb; int arot; int w; };
template <int P> DEV void k2_a1(const K2G& g) {
  constexpr int SS = P / 8;
  const int i = (P % 8) ^ g.arot;
  dma16(g.wsp + SS * 128 + (size_t)i * 8 * 4096, g.aoff[P & 1], g.adst + (SS & 1) * 8192 + ((i ^ (2 * g.w)) * 1024));
}
template <int A, int B> DEV void k2_as(const K2G& g) { if constexpr (A < B) { k2_a1<A>(g); k2_as<A + 1, B>(g); } }
template <int N> DEV void k2_b1(const K2G& g, u32x4 (&bf)[K2_R]) {
 gload16<(N % 8 - 4) * 1024>(bf[N % K2_R], g.z2 + (N / 8) * 8192, g.boff); }
template <int A, int B> DEV void k2_bs(const K2G& g, u32x4 (&bf)[K2_R]) { if constexpr (A < B) { k2_b1<A>(g, bf); k2_bs<A + 1, B>(g, bf); } }
template <int I> DEV u32x4 k2_read_a(const char* ap0, const char* ap1, int t) {
  return *(const u32x4*)(((I & 1) ? ap1 : ap0) + ((I >> 1) & 1) * 8192 + t * 2048);
}
template <int TAU>
DEV void k2_group(const K2G& g, u32x4 (&bf)[K2_R], u32x4 (&af)[2][4], const char* ap0, const char* ap1, f32x4 (&acc)[4][7], u32x4 (&ep)[4], u32& bw, u32 (&tch)[2]) {
  constexpr int S = TAU / 7, F = TAU % 7;
  wait_vm<k2_wait(TAU)>();
  pin(bf[TAU % K2_R]);
  if constexpr (S < 15 && F >= 2 && F < 6) af[(S + 1) & 1][F - 2] = k2_read_a<S + 1>(ap0, ap1, F - 2);
  SB();
#pragma unroll
  for (int t = 0; t < 4; t++) acc[t][F] = K2_MFMA(bf[TAU % K2_R], af[S & 1][t], acc[t][F]);
  SB();
  if constexpr (TAU + K2_R < 112) k2_b1<TAU + K2_R>(g, bf);
  if constexpr (k2_apiece(TAU) >= 0) k2_a1<k2_apiece(TAU)>(g);
  if constexpr (k2_touch_at(TAU)) { keep(tch[(TAU / 14) & 1]); gload4(tch[(TAU / 14) & 1], g.z2 + (S + K2_TD) * 7168 - 4096, g.tb); }
  if constexpr (TAU == 111) { keep(tch[0]); keep(tch[1]); }
  if constexpr (TAU == K2_EPI_AT) {
    gload16<0>(ep[0], g.z1, g.boff); gload16<1024>(ep[1], g.z1, g.boff); gload16<2048>(ep[2], g.z1, g.boff); gload16<3072>(ep[3], g.z1, g.boff);
    gload4(bw, g.bsp, (g.boff >> 3) & 28u);
#pragma unroll
    for (int i = 0; i < K2_PF; i++) dma16(g.pf + i * 1024, g.boff, g.dump);
  }
  if constexpr (TAU < 111) k2_group<TAU + 1>(g, bf, af, ap0, ap1, acc, ep, bw, tch);
}

DEV void k2_gemm(char* smem, int b, const u16* wsp, const u16* bsp, const u16* wout, char* ws) {
  STAMP_DECL; STAMP(0);
  const int xcd = b & 7, idx = b >> 3;
  const int tp = (xcd >> 1) * 8 + (idx >> 2), fp = (xcd & 1) * 4 + (idx & 3);
  const int w = __builtin_amdgcn_readfirstlane(threadIdx.x >> 6), l = threadIdx.x & 63;
  const int rho = l & 15, q = l >> 4;
  const int tt = tp * 4 + w;
  char* const reg = smem + w * 16384;
  K2G g;
  g.wsp = (const char*)wsp + (size_t)tp * 64 * 4096 + w * 1024;
  g.wsp0 = (const char*)wsp + (size_t)tp * 64 * 4096 + w * 65536;
  g.z2 = ws + WS_Z2F + ((size_t)(fp * 64 + w * 16) * 7 + 4) * 1024;
  g.z1 = ws + WS_Z1F + ((size_t)tt * 28 + ((fp * 7) >> 1)) * 1024;
  g.bsp = (const char*)bsp + tt * 32;
  {
    int pc = (idx * 4 + w) * 7;
    pc = pc > 784 - 7 ? 784 - 7 : pc;
    g.pf = (const char*)wout + pc * 1024;
  }
  g.aoff[0] = (u32)((l >> 3) * 4096 + (((l & 7) ^ (l >> 4)) * 16));
  g.aoff[1] = (u32)((l >> 3) * 4096 + (((l & 7) ^ ((l >> 4) | 4)) * 16));
  g.boff = l * 16;
  g.adst = lds_addr(reg);
  g.dump = lds_addr(smem) + 65536 + w * 1024;
  g.w = w;
  g.arot = (idx & 3) * 2;
  u32x4 bf[K2_R], af[2][4], ep[4];
  u32 bw, tch[2];
  g.tb = (u32)(((idx >> 2) * 14 + (l < 14 ? l : 13)) * 128);
  tch[0] = tch[1] = 0u;
  k2_as<0, 8>(g); k2_bs<0, 14>(g, bf); k2_as<8, 16>(g); k2_bs<14, K2_R>(g, bf);
  const char* ap0 = reg + rho * 128 + (((0 + q) ^ (rho >> 1)) * 16);
  const char* ap1 = reg + rho * 128 + (((4 + q) ^ (rho >> 1)) * 16);
  f32x4 acc[4][7] = {};
  STAMP(1);
  wait_vm<K2_PRO - 8>();
  STAMP(2);
#pragma unroll
  for (int t = 0; t < 4; t++) af[0][t] = k2_read_a<0>(ap0, ap1, t);
  k2_group<0>(g, bf, af, ap0, ap1, acc, ep, bw, tch);
  STAMP(3);
#pragma unroll
  for (int f = 0; f < 7; f++) {
    *(f32x4*)(reg + f * 1024 + l * 16) = acc[1][f];
    *(f32x4*)(reg + (7 + f) * 1024 + l * 16) = acc[3][f];
  }
  bar();
  STAMP(4);
  char* const in1 = smem + (w ^ 1) * 16384;
#pragma unroll
  for (int f = 0; f < 7; f++) acc[2][f] += *(const f32x4*)(in1 + (7 + f) * 1024 + l * 16);
#pragma unroll
  for (int f = 0; f < 7; f++) *(f32x4*)(in1 + (7 + f) * 1024 + l * 16) = acc[2][f];
#pragma unroll
  for (int f = 0; f < 7; f++) acc[0][f] += *(const f32x4*)(in1 + f * 1024 + l * 16);
  bar();
  char* const in2 = smem + (w ^ 3) * 16384;
#pragma unroll
  for (int i = 0; i < 1; i++) wait_vm<K2_PF>();
#pragma unroll
  for (int i = 0; i < 4; i++) pin(ep[i]);
  asm volatile("" : "+v"(bw));
  const float bs = (rho & 1) ? bfhi(bw) : bflo(bw);
  STAMP(5);
  const int odd = fp & 1;
  u32x2 hv[8];
#pragma unroll
  for (int f = 0; f < 7; f++) {
    f32x4 v = acc[0][f] + *(const f32x4*)(in2 + (7 + f) * 1024 + l * 16);
    v = v + bs;
    const u32x4 ze = ep[f >> 1], zo = ep[(f + 1) >> 1];
    const u32 za = odd ? ((f & 1) ? zo.x : zo.z) : ((f & 1) ? ze.z : ze.x);
    const u32 zb = odd ? ((f & 1) ? zo.y : zo.w) : ((f & 1) ? ze.w : ze.y);
    hv[f] = (u32x2){pk_bf16(v.x * bflo(za), v.y * bfhi(za)), pk_bf16(v.z * bflo(zb), v.w * bfhi(zb))};
  }
  const u32 hb = (u32)(WS_HF + (tt * 28 + ((fp * 7) >> 1)) * 1024 + l * 16);
  if (odd) {
    gstore8(ws, hb + 8, hv[0]);
#pragma unroll
    for (int i = 0; i < 3; i++) gstore16(ws, hb + (i + 1) * 1024, (u32x4){hv[1 + 2 * i].x, hv[1 + 2 * i].y, hv[2 + 2 * i].x, hv[2 + 2 * i].y});
  } else {
#pragma unroll
    for (int i = 0; i < 3; i++) gstore16(ws, hb + i * 1024, (u32x4){hv[2 * i].x, hv[2 * i].y, hv[1 + 2 * i].x, hv[1 + 2 * i].y});
    gstore8(ws, hb + 3 * 1024, hv[6]);
  }
  STAMP(6); STAMP_FLUSH(1);
}

constexpr int KG_ROW = 272, KG_WAVE = 53 * KG_ROW;
constexpr int CV_CS = 4160, CV_T = 8 * CV_CS, CV_U = 2 * CV_T, CV_CMP = CV_U + 2 * 4608;
DEV f32x2 cexp_pow2(float gl, float fh, float fl, float p) {
  float m = __builtin_amdgcn_exp2f(gl * p);
  float r = __builtin_amdgcn_fractf(fh * p) + fl * p;
  return (f32x2){m * __builtin_amdgcn_cosf(r), m * __builtin_amdgcn_sinf(r)};
}
DEV float exp_acc(float x) {
  const float h = x * 1.44269504089f;
  const float lo = fmaf(x, 1.44269504089f, -h) + x * 1.925963e-8f;
  return __builtin_amdgcn_exp2f(h) * fmaf(lo, 0.69314718056f, 1.f);
}
struct KGen { f32x2 e1, e2, e4, e8, e16, e32, e64, e128, e256, e512, e1024, e2048, c0; };
DEV KGen kg_gens(float la, float ai, float cr, float ci, float dt, float sc) {
  const float zr = -rbf(exp_acc(la)) * dt, zi = ai * dt;
  const float gl = zr * 1.44269504089f;
  const float fh = zi * 0.15915494309f;
  const float fl = fmaf(zi, 0.15915494309f, -fh) + zi * 6.4206382e-9f;
  KGen k;
  k.c0 = (f32x2){cr * sc, ci * sc};
  k.e1 = cexp_pow2(gl, fh, fl, 1.f); k.e2 = csqr(k.e1); k.e4 = csqr(k.e2); k.e8 = csqr(k.e4);
  k.e16 = cexp_pow2(gl, fh, fl, 16.f); k.e32 = csqr(k.e16); k.e64 = csqr(k.e32); k.e128 = csqr(k.e64);
  k.e256 = cexp_pow2(gl, fh, fl, 256.f); k.e512 = csqr(k.e256); k.e1024 = csqr(k.e512); k.e2048 = csqr(k.e1024);
  return k;
}
DEV void kg_tables(const KGen& k, u32* tw) {
#define TW(row, v) tw[(row) * (KG_ROW / 4)] = (v)
  {
    f32x2 tb[16];
    tb[0] = (f32x2){128.f, 0.f};
    tb[1] = k.e8 * 128.f;
    tb[2] = k.e16 * 128.f; tb[3] = cmul(tb[1], k.e16);
    tb[4] = k.e32 * 128.f;
#pragma unroll
    for (int i = 1; i < 4; i++) tb[4 + i] = cmul(tb[i], k.e32);
    tb[8] = k.e64 * 128.f;
#pragma unroll
    for (int i = 1; i < 8; i++) tb[8 + i] = cmul(tb[i], k.e64);
    TW(0, pk_f16(128.f, 0.f));
    TW(16, pk_f16(k.e128.x * 128.f, k.e128.y * 128.f));
#pragma unroll
    for (int i = 1; i < 16; i++) { TW(i, pk_f16(tb[i].x, tb[i].y)); f32x2 u = cmul(tb[i], k.e128); TW(16 + i, pk_f16(u.x, u.y)); }
  }
  {
    f32x2 a[21];
    a[0] = k.c0; a[1] = cmul(k.c0, k.e1); a[2] = cmul(k.c0, k.e2); a[3] = cmul(a[1], k.e2);
#pragma unroll
    for (int i = 0; i < 4; i++) a[4 + i] = cmul(a[i], k.e4);
#pragma unroll
    for (int i = 0; i < 4; i++) a[8 + i] = cmul(a[2 * i], k.e256);
    a[12] = cmul(a[0], k.e512); a[13] = cmul(a[4], k.e512); a[14] = cmul(a[8], k.e512); a[15] = cmul(a[10], k.e512);
    a[16] = cmul(a[0], k.e1024); a[17] = cmul(a[8], k.e1024); a[18] = cmul(a[12], k.e1024); a[19] = cmul(a[14], k.e1024);
    a[20] = cmul(a[0], k.e2048);
#pragma unroll
    for (int i = 0; i < 21; i++) TW(32 + i, pk_f16(a[i].x, -a[i].y));
  }
#undef TW
}
template <int CTRL> DEV u32 dpp(u32 old, u32 src) { return __builtin_amdgcn_update_dpp(old, src, CTRL, 0xf, 0xf, false); }
template <int CTRL> DEV float dpp_add(float v) { return v + __uint_as_float(__builtin_amdgcn_update_dpp(0u, __float_as_uint(v), CTRL, 0xf, 0xf, true)); }
DEV void cv_frags(u32x4 (&A)[8], u32x4 (&B)[8], const char* pa, const char* pb) {
#pragma unroll
  for (int i = 0; i < 8; i++) {
    A[i] = *(const u32x4*)(pa - 512 * i);
    B[i] = *(const u32x4*)(pb + 512 * i);
  }
}
DEV void cv_mma(const u32x4 (&A)[8], const u32x4 (&B)[8], f32x4 (&acc)[8]) {
#pragma unroll
  for (int a = 0; a < 8; a++)
#pragma unroll
    for (int s = 7; s >= a; s--) acc[s] = mfma_h(A[a], B[s - a], acc[s]);
}
#define SSM_PRIO 0
DEV void k2_ssm(char* smem, int cb, const u16* lar, const u16* aim, const u16* cre, const u16* cim, const u16* ldt, const u16* dsk, char* ws) {
  STAMP_DECL; STAMP(0);
  __builtin_amdgcn_s_setprio(3);
  const int w = __builtin_amdgcn_readfirstlane(threadIdx.x >> 6), l = threadIdx.x & 63;
  const int ch = w >> 1, d = cb * 2 + ch, hw = w & 1, t = hw * 64 + l;
  const int rho = l & 15, q = l >> 4;
  const size_t o = (size_t)d * NMODE;
  const u32 vla = *(const u32*)(lar + o + hw * 128 + 2 * l), vai = *(const u32*)(aim + o + hw * 128 + 2 * l);
  const u32 vcr = *(const u32*)(cre + o + hw * 128 + 2 * l), vci = *(const u32*)(cim + o + hw * 128 + 2 * l);
  const u32 ocr = *(const u32*)(cre + o + (hw ^ 1) * 128 + 2 * l), oci = *(const u32*)(cim + o + (hw ^ 1) * 128 + 2 * l);
  const u32 vdt = ldt[d], vdk = dsk[d];
  char* const T = smem + ch * CV_T;
  char* const U = smem + CV_U + ch * 4608;
  u16* const cmp = (u16*)(smem + CV_CMP + ch * 1408);
#pragma unroll
  for (int i = 0; i < 2; i++) dma16(ws + WS_UT + (size_t)d * (SEQ * 2) + (hw * 2 + i) * 1024, l * 16, lds_addr(U) + 512 + (hw * 2 + i) * 1024);
  __builtin_amdgcn_s_setprio(SSM_PRIO);
  if (t < 32) *(u32x4*)(U + t * 16) = (u32x4){0u, 0u, 0u, 0u};
  u32 gi[2][8];
#pragma unroll
  for (int c = 0; c < 2; c++)
#pragma unroll
    for (int j = 0; j < 4; j++) {
      const int l0 = 2047 - (hw * 128 + c * 64 + l) * 8 - 2 * j, l1 = l0 - 1;
      const u32 bits = pk_bf16((float)l0, (float)l1);
      int i0 = (int)(bits & 0xffffu) - 0x4280, i1 = (int)(bits >> 16) - 0x4280;
      if (c == 1) { i0 = l0 < 128 ? l0 : i0; i1 = l1 < 128 ? l1 : i1; }
      gi[c][2 * j] = i0; gi[c][2 * j + 1] = i1;
    }
  const float dt = rbf(exp_acc(bf2f(vdt)));
  const float dk = bf2f(vdk);
  float S = fabsf(bflo(vcr)) + fabsf(bfhi(vcr)) + fabsf(bflo(vci)) + fabsf(bfhi(vci));
  float S2 = fabsf(bflo(ocr)) + fabsf(bfhi(ocr)) + fabsf(bflo(oci)) + fabsf(bfhi(oci));
  S = hw ? S2 + S : S + S2;
  S = dpp_add<0xb1>(S); S = dpp_add<0x4e>(S); S = dpp_add<0x141>(S); S = dpp_add<0x140>(S);
  S = xsum32(xsum16(S));
  int e = (int)((__float_as_uint(S) >> 23) & 255u) - 125;
  e = e < -100 ? -100 : (e > 100 ? 100 : e);
  e = __builtin_amdgcn_readfirstlane(e);
  const float sc = __uint_as_float((u32)(127 + 8 - e) << 23);
  const float ks = dt * __uint_as_float((u32)(127 + e - 14) << 23);
  STAMP(1);
  char* tab = smem + w * KG_WAVE;
  f32x16 kacc = {};
  {
    const int r = l & 31, h = l >> 5;
    const char* pa = tab + (32 + (r < 21 ? r : 20)) * KG_ROW + h * 16;
    const char* pb = tab + r * KG_ROW + h * 16;
    const KGen k0 = kg_gens(bflo(vla), bflo(vai), bflo(vcr), bflo(vci), dt, sc);
    STAMP(8);
    kg_tables(k0, (u32*)(tab + l * 4));
    SB();
    STAMP(9);
    u32x4 fa[8], fb[8];
#pragma unroll
    for (int kk = 0; kk < 8; kk++) { fa[kk] = *(const u32x4*)(pa + kk * 32); fb[kk] = *(const u32x4*)(pb + kk * 32); }
    SB();
    const KGen k1 = kg_gens(bfhi(vla), bfhi(vai), bfhi(vcr), bfhi(vci), dt, sc);
#pragma unroll
    for (int kk = 0; kk < 8; kk++) kacc = mfma32_h(fa[kk], fb[kk], kacc);
    STAMP(10);
    kg_tables(k1, (u32*)(tab + l * 4));
    STAMP(11);
#pragma unroll
    for (int kk = 0; kk < 8; kk++) kacc = mfma32_h(*(const u32x4*)(pa + kk * 32), *(const u32x4*)(pb + kk * 32), kacc);
    STAMP(12);
    float* give = (float*)tab + l;
    float keep[6];
#pragma unroll
    for (int i = 0; i < 6; i++) { give[i * 64] = hw ? kacc[i] : kacc[6 + i]; keep[i] = hw ? kacc[6 + i] : kacc[i]; }
    const int b1 = h ? 384 + 2 * r : 256 + 4 * r, b2 = h ? (r == 0 ? 640 : 672) : 512 + r;
    const int x0 = hw ? b1 + (h ? 64 : 2) : 4 * h + 8 * r;
    const int x1 = hw ? b2 : x0 + 2, d1 = hw ? (h ? (r == 0 ? 32 : 0) : 32) : 1;
    const int x2 = hw ? (h ? 672 : b2 + 64) : b1, d2 = hw ? (h ? 0 : 32) : 1;
    bar();
    const float* take = (const float*)(smem + (w ^ 1) * KG_WAVE) + l;
    const int xi[6] = {x0, x0 + 1, x1, x1 + d1, x2, x2 + d2};
#pragma unroll
    for (int i = 0; i < 6; i++) {
      const float o = take[i * 64];
      cmp[xi[i]] = __builtin_bit_cast(u16, (_Float16)(hw ? o + keep[i] : keep[i] + o));
    }
  }
  STAMP(2);
  bar();
  {
    u32 ra[3][4];
#pragma unroll
    for (int c = 0; c < 2; c++)
#pragma unroll
      for (int j = 0; j < 4; j++) ra[c][j] = (u32)cmp[gi[c][2 * j]] | ((u32)cmp[gi[c][2 * j + 1]] << 16);
    {
      const u32 k8 = hw ? 0u : cmp[512], k4 = hw ? 0u : cmp[511], k0 = hw ? 0u : cmp[510];
      ra[2][0] = k8 * 0x10001u; ra[2][1] = k4 * 0x10001u; ra[2][2] = k4 | (k0 << 16); ra[2][3] = k0 * 0x10001u;
    }
#pragma unroll
    for (int c = 0; c < 2; c++) {
      u32 dw[8], od[7];
#pragma unroll
      for (int j = 0; j < 4; j++) {
        dw[j] = ra[c][j];
        dw[4 + j] = dpp<0x130>(c == 0 ? dpp<0x134>(0u, ra[1][j]) : ra[2][j], ra[c][j]);
      }
#pragma unroll
      for (int i = 0; i < 7; i++) od[i] = __builtin_amdgcn_alignbit(dw[i + 1], dw[i], 16);
      char* tp = T + (hw * 128 + c * 64 + l) * 16;
#pragma unroll
      for (int k = 0; k < 4; k++) {
        *(u32x4*)(tp + (2 * k) * CV_CS) = (u32x4){dw[k], dw[k + 1], dw[k + 2], dw[k + 3]};
        *(u32x4*)(tp + (2 * k + 1) * CV_CS) = (u32x4){od[k], od[k + 1], od[k + 2], od[k + 3]};
      }
    }
    if (t < 8) *(u32x4*)(T + t * CV_CS + 4096) = (u32x4){0u, 0u, 0u, 0u};
  }
  wait_vm<0>();
  bar();
  STAMP(3);
  const char* pa = T + (7 - (rho & 7)) * CV_CS + (255 - (rho >> 3) + q) * 16 - 32 - 256 * hw;
  const char* pb = U + 512 + rho * 32 + q * 16 - 32 - 256 * hw;
  f32x4 acc[8] = {};
  {
    u32x4 A0[8], B0[8], A1[8], B1[8];
    cv_frags(A0, B0, pa, pb);
    SB(); cv_frags(A1, B1, pa - 64, pb - 64); SB();
    cv_mma(A0, B0, acc);
    SB(); cv_frags(A0, B0, pa - 128, pb - 128); SB();
    cv_mma(A1, B1, acc);
    SB(); cv_frags(A1, B1, pa - 192, pb - 192); SB();
    cv_mma(A0, B0, acc);
    cv_mma(A1, B1, acc);
  }
  STAMP(4);
  bar();
  {
    char* box = T + hw * 4096;
#pragma unroll
    for (int i = 0; i < 4; i++) {
      const f32x4 give = hw ? acc[i] : acc[4 + i];
      acc[i] = hw ? acc[4 + i] : acc[i];
      *(f32x4*)(box + i * 1024 + l * 16) = give;
    }
  }
  u32x2 ub[4];
#pragma unroll
  for (int i = 0; i < 4; i++) ub[i] = *(const u32x2*)(U + 512 + (256 * (4 * hw + i) + 16 * rho + 4 * q) * 2);
  bar();
  {
    const char* box = T + (hw ^ 1) * 4096;
    u32x2 yv[4];
#pragma unroll
    for (int i = 0; i < 4; i++) {
      f32x4 v = (acc[i] + *(const f32x4*)(box + i * 1024 + l * 16)) * ks;
      f16x2 u0 = as_h2(ub[i].x), u1 = as_h2(ub[i].y);
      yv[i] = (u32x2){pk_bf16(fmaf((float)u0.x, dk, v.x), fmaf((float)u0.y, dk, v.y)), pk_bf16(fmaf((float)u1.x, dk, v.z), fmaf((float)u1.y, dk, v.w))};
    }
#pragma unroll
    for (int i = 0; i < 4; i += 2) {
      auto sx = __builtin_amdgcn_permlane16_swap(yv[i].x, yv[i + 1].x, false, false);
      auto sy = __builtin_amdgcn_permlane16_swap(yv[i].y, yv[i + 1].y, false, false);
      const u32 off = (u32)(512 * (4 * hw + i + (q & 1)) + 32 * rho + 16 * (q >> 1));
      gstore16(ws, (u32)(WS_YT + d * (SEQ * 2)) + off, (u32x4){sx[0], sy[0], sx[1], sy[1]});
    }
  }
  STAMP(5); STAMP_FLUSH(1);
}

extern "C" __global__ NOPK __launch_bounds__(256, 2) void k2(const u16* wsp, const u16* bsp, const u16* wout, const u16* lar, const u16* aim, const u16* cre,
                                             const u16* cim, const u16* ldt, const u16* dsk, char* ws) {
  __shared__ __attribute__((aligned(16))) char smem[81920];
  if (blockIdx.x == 0 && threadIdx.x == 0) __hip_atomic_store((GAS u64*)(ws + (40u << 20) + 256), (u64)__builtin_amdgcn_s_memrealtime(), __ATOMIC_RELAXED, __HIP_MEMORY_SCOPE_AGENT);
  if (blockIdx.x < 256) k2_gemm(smem, blockIdx.x, wsp, bsp, wout, ws);
  else k2_ssm(smem, blockIdx.x - 256, lar, aim, cre, cim, ldt, dsk, ws);
}

#define K3_LEAD 3
struct K3G { const char* wo; const char* hf; u32 woff; u32 hoff[4]; u32 wdst; const char* yt; u32 yoff; u32 ydst; const char* xb; u32 xo; };
template <int I> DEV void k3_issue(const K3G& g, u32x4 (&hf)[7][4], u32x4 (&xv)[2]) {
  if constexpr (I < 7) {
#pragma unroll
    for (int i = 0; i < 4; i++) dma16(g.wo + (size_t)(I * 32 + i * 8) * (DM * 2), g.woff, g.wdst + (I * 4 + i) * 1024);
#pragma unroll
    for (int t = 0; t < 4; t++) gload16<0>(hf[I][t], g.hf + I * 1024, g.hoff[t]);
  } else if constexpr (I == 7) {
    dma16(g.yt, g.yoff, g.ydst);
    dma16(g.yt + 32 * (SEQ * 2), g.yoff, g.ydst + 1024);
    gload16<0>(xv[0], g.xb, g.xo);
    gload16<0>(xv[1], g.xb + 8 * (DM * 2), g.xo);
  }
}
constexpr int k3_cnt(int units) { return units <= 7 ? 8 * units : 60; }
template <int I> DEV void k3_step(const K3G& g, u32x4 (&hf)[7][4], u32x4 (&xv)[2], const char* const (&wp)[4], f32x4 (&acc)[4][4]) {
  wait_vm<k3_cnt(I + K3_LEAD) - 8 * (I + 1)>();
#pragma unroll
  for (int t = 0; t < 4; t++) pin(hf[I][t]);
  u32x4 wf[4];
#pragma unroll
  for (int p = 0; p < 4; p++) wf[p] = tr_frag(wp[p] + I * 4096, wp[p] + I * 4096 + 2048);
  k3_issue<I + K3_LEAD>(g, hf, xv);
#pragma unroll
  for (int p = 0; p < 4; p++)
#pragma unroll
    for (int t = 0; t < 4; t++) acc[t][p] = mfma_bf(wf[p], hf[I][t], acc[t][p]);
  if constexpr (I < 6) k3_step<I + 1>(g, hf, xv, wp, acc);
}
template <int A, int B> DEV void k3_pro(const K3G& g, u32x4 (&hf)[7][4], u32x4 (&xv)[2]) {
  if constexpr (A < B) { k3_issue<A>(g, hf, xv); k3_pro<A + 1, B>(g, hf, xv); }
}
extern "C" __global__ __launch_bounds__(256, 1) void k3(const u16* x, const u16* wout, u16* out, char* ws) {
  __shared__ __attribute__((aligned(16))) char smem[4 * 28672 + 8192];
  STAMP_DECL; STAMP(0);
  const u64 t2_ = __builtin_amdgcn_s_memrealtime();
  const int b = blockIdx.x, xcd = b & 7, idx = b >> 3;
  const int tp = xcd * 4 + idx / 7, dp = idx % 7;
  const int w = __builtin_amdgcn_readfirstlane(threadIdx.x >> 6), l = threadIdx.x & 63;
  const int rho = l & 15, q = l >> 4, qq = (l >> 2) & 3, pp = l & 3;
  char* const reg = smem + w * 28672;
  char* const Y = smem + 4 * 28672;
  K3G g;
  g.wo = (const char*)wout + (size_t)(w * 224) * (DM * 2) + dp * 128;
  g.hf = ws + WS_HF + ((size_t)tp * 4 * 28 + w * 7) * 1024;
  g.woff = (u32)((l >> 3) * (DM * 2) + (((l & 7) ^ ((l >> 4) << 1)) * 16));
#pragma unroll
  for (int t = 0; t < 4; t++) g.hoff[t] = (u32)((t ^ w) * 28 * 1024 + l * 16);
  g.wdst = lds_addr(reg);
  g.yt = ws + WS_YT + (size_t)(dp * 64) * (SEQ * 2) + tp * 128 + w * 32;
  g.yoff = (u32)((l >> 1) * (SEQ * 2) + (l & 1) * 16);
  g.ydst = lds_addr(Y) + w * 2048;
  g.xo = (u32)((l >> 3) * (DM * 2) + (l & 7) * 16);
  const size_t xb = (size_t)(tp * 64 + w * 16) * (DM * 2) + dp * 128;
  g.xb = (const char*)x + xb;
  u32x4 hf[7][4], xv[2];
  k3_pro<0, K3_LEAD>(g, hf, xv);
  const char* wp[4];
  const int sg = ((q & 1) << 1) | (qq >> 1);
#pragma unroll
  for (int p = 0; p < 4; p++) wp[p] = reg + (4 * q + qq) * 128 + ((p ^ sg) * 32) + 8 * pp;
  f32x4 acc[4][4] = {};
  STAMP(1);
  k3_step<0>(g, hf, xv, wp, acc);
  STAMP(2);
#pragma unroll
  for (int t = 1; t < 4; t++)
#pragma unroll
    for (int p = 0; p < 4; p++) *(f32x4*)(reg + t * 4352 + rho * 272 + p * 64 + q * 16) = acc[t][p];
  wait_vm<0>();
  pin(xv[0]); pin(xv[1]);
#pragma unroll
  for (int p = 0; p < 4; p++) {
    u32x2 yv = tr_read(Y + w * 2048 + (16 * p + 4 * q + qq) * 32 + 8 * pp);
    *(f32x4*)(reg + rho * 272 + p * 64 + q * 16) = acc[0][p] + (f32x4){bflo(yv.x), bfhi(yv.x), bflo(yv.y), bfhi(yv.y)};
  }
  bar();
  STAMP(3);
#pragma unroll
  for (int i = 0; i < 2; i++) {
    f32x4 a = {bflo(xv[i].x), bfhi(xv[i].x), bflo(xv[i].y), bfhi(xv[i].y)}, c = {bflo(xv[i].z), bfhi(xv[i].z), bflo(xv[i].w), bfhi(xv[i].w)};
#pragma unroll
    for (int t = 0; t < 4; t++) {
      const char* rp = smem + (w ^ t) * 28672 + t * 4352 + (8 * i + (l >> 3)) * 272 + (l & 7) * 32;
      a += *(const f32x4*)(rp); c += *(const f32x4*)(rp + 16);
    }
    u32x4 o = {pk_bf16(a.x, a.y), pk_bf16(a.z, a.w), pk_bf16(c.x, c.y), pk_bf16(c.z, c.w)};
    gstore16(out, (u32)xb + i * 8 * (DM * 2) + g.xo, o);
  }
  STAMP(4); STAMP_FLUSH(2);
  if (blockIdx.x == 0) {
    const u64 t0_ = *(volatile u64*)(ws + (40u << 20)), t1_ = *(volatile u64*)(ws + (40u << 20) + 256);
    const u64 t3_ = __builtin_amdgcn_s_memrealtime();
    u64 d = DIAG == 1 ? t1_ - t0_ : (DIAG == 2 ? t2_ - t1_ : (DIAG == 3 ? t3_ - t2_ : 0));
    d = d > 5000 ? 5000 : d;
    while (__builtin_amdgcn_s_memrealtime() < t3_ + 4 * d) __builtin_amdgcn_s_sleep(1);
  }
  asm volatile("k3_code_end:");
}

#define K1_GRID 256
#define K2_GRID 480
extern "C" void kernel_launch(void* const* d_in, const int* in_sizes, int n_in, void* d_out, int out_size, void* d_ws, size_t ws_size,
                              hipStream_t stream) {
  const u16* x = (const u16*)d_in[0];
  char* ws = (char*)d_ws;
  Extra ex = {{(const char*)d_out, (const char*)d_in[1], (const char*)d_in[2], (const char*)d_in[3], (const char*)d_in[4], (const char*)d_in[5], (const char*)d_in[6], (const char*)d_in[10]}};
  k1<<<K1_GRID, 512, 0, stream>>>(x, (const u16*)d_in[8], (const u16*)d_in[9], (const u16*)d_in[11], ws, ex);
  k2<<<K2_GRID, 256, 0, stream>>>((const u16*)d_in[9], (const u16*)d_in[10], (const u16*)d_in[11], (const u16*)d_in[1], (const u16*)d_in[2],
                              (const u16*)d_in[3], (const u16*)d_in[4], (const u16*)d_in[5], (const u16*)d_in[6], ws);
  k3<<<224, 256, 0, stream>>>(x, (const u16*)d_in[11], (u16*)d_out, ws);
}
```

```cpp
#define DIAG 8
#include <hip/hip_runtime.h>
#include <stdint.h>

typedef unsigned short u16;
typedef unsigned int u32;
typedef unsigned long long u64;
typedef __attribute__((ext_vector_type(4))) u32 u32x4;
typedef __attribute__((ext_vector_type(2))) u32 u32x2;
typedef __attribute__((ext_vector_type(4))) float f32x4;
typedef __attribute__((ext_vector_type(2))) float f32x2;
typedef __attribute__((ext_vector_type(16))) float f32x16;
typedef __attribute__((ext_vector_type(8))) __bf16 bf16x8;
typedef __attribute__((ext_vector_type(2))) __bf16 bf16x2;
typedef __attribute__((ext_vector_type(8))) _Float16 f16x8;
typedef __attribute__((ext_vector_type(2))) _Float16 f16x2;
typedef __attribute__((ext_vector_type(4))) short s16x4;
#define LDSAS __attribute__((address_space(3)))
#define GAS __attribute__((address_space(1)))
#define DEV __device__ __forceinline__
#if defined(__HIP_DEVICE_COMPILE__)
#define NOPK __attribute__((target("no-packed-fp32-ops")))
#else
#define NOPK
#endif
#define SB() __builtin_amdgcn_sched_barrier(0)

constexpr int SEQ = 2048, DM = 448, NMODE = 256, FF = 1792, FH = 896;

constexpr size_t WS_Z2F = 0;
constexpr size_t WS_Z1F = 4u << 20;
constexpr size_t WS_HF  = 8u << 20;
constexpr size_t WS_UT  = 12u << 20;
constexpr size_t WS_YT  = 14u << 20;
constexpr size_t WS_GR  = 16u << 20;
constexpr size_t WS_EP  = 17u << 20;

#define STAMP(i)
#define STAMP_DECL
#define STAMP_ARG
#define STAMP_PASS
#define STAMP_FLUSH(kern)
DEV float bf2f(u32 v) { return __uint_as_float(v << 16); }
DEV float bflo(u32 v) { return __uint_as_float(v << 16); }
DEV float bfhi(u32 v) { return __uint_as_float(v & 0xffff0000u); }
DEV u32 pk_bf16(float a, float b) { bf16x2 v = {(__bf16)a, (__bf16)b}; return __builtin_bit_cast(u32, v); }
DEV u32 pk_f16(float a, float b) { f16x2 v = {(_Float16)a, (_Float16)b}; return __builtin_bit_cast(u32, v); }
DEV f16x2 as_h2(u32 v) { return __builtin_bit_cast(f16x2, v); }
DEV float rbf(float a) { return __uint_as_float(pk_bf16(a, a) << 16); }
DEV f32x4 mfma_bf(u32x4 a, u32x4 b, f32x4 c) {
  return __builtin_amdgcn_mfma_f32_16x16x32_bf16(__builtin_bit_cast(bf16x8, a), __builtin_bit_cast(bf16x8, b), c, 0, 0, 0);
}
DEV f32x4 mfma_h(u32x4 a, u32x4 b, f32x4 c) {
  return __builtin_amdgcn_mfma_f32_16x16x32_f16(__builtin_bit_cast(f16x8, a), __builtin_bit_cast(f16x8, b), c, 0, 0, 0);
}
DEV f32x16 mfma32_h(u32x4 a, u32x4 b, f32x16 c) {
  return __builtin_amdgcn_mfma_f32_32x32x16_f16(__builtin_bit_cast(f16x8, a), __builtin_bit_cast(f16x8, b), c, 0, 0, 0);
}
DEV u32x2 tr_read(const char* p) {
  s16x4 v = __builtin_amdgcn_ds_read_tr16_b64_v4i16((LDSAS s16x4*)p);
  return __builtin_bit_cast(u32x2, v);
}
DEV u32x4 tr_frag(const char* p0, const char* p1) {
  u32x2 a = tr_read(p0), b = tr_read(p1);
  return (u32x4){a.x, a.y, b.x, b.y};
}
#define LDMOD ""
#define DMAMOD ""
DEV u32 lds_addr(const void* p) { return (u32)(uintptr_t)(LDSAS const char*)p; }
DEV void dma16(const void* sbase, u32 voff, u32 ldst) {
  asm volatile("s_mov_b32 m0, %2\n\ts_nop 0\n\tglobal_load_lds_dwordx4 %0, %1 " DMAMOD ::"v"(voff), "s"(sbase), "s"(ldst) : "memory");
}
template <int OFF> DEV void gload16(u32x4& d, const void* sbase, u32 voff) {
  asm volatile("global_load_dwordx4 %0, %1, %2 offset:%3 " LDMOD : "=v"(d) : "v"(voff), "s"(sbase), "i"(OFF) : "memory");
}
DEV void gstore16(void* sbase, u32 voff, u32x4 d) {
  asm volatile("global_store_dwordx4 %0, %1, %2 sc1\n\ts_nop 1" ::"v"(voff), "v"(d), "s"(sbase) : "memory");
}
DEV void gstore8(void* sbase, u32 voff, u32x2 d) {
  asm volatile("global_store_dwordx2 %0, %1, %2 sc1" ::"v"(voff), "v"(d), "s"(sbase) : "memory");
}
DEV void gstore16p(void* sbase, u32 voff, u32x4 d) {
  asm volatile("global_store_dwordx4 %0, %1, %2\n\ts_nop 1" ::"v"(voff), "v"(d), "s"(sbase) : "memory");
}
#define Z2STORE gstore16
DEV void gload4(u32& d, const void* sbase, u32 voff) {
  asm volatile("global_load_dword %0, %1, %2" : "=v"(d) : "v"(voff), "s"(sbase) : "memory");
}
template <int N> DEV void wait_vm() { asm volatile("s_waitcnt vmcnt(%0)" ::"i"(N) : "memory"); }
DEV void touch(u32& d, const void* p) { asm volatile("global_load_dword %0, %1, off" : "=v"(d) : "v"(p) : "memory"); }
DEV void keep(u32& a) { asm volatile("" : "+v"(a)); }
DEV void pin(u32x4& a) { asm volatile("" : "+v"(a)); }
DEV void bar() { asm volatile("s_waitcnt lgkmcnt(0)\n\ts_barrier" ::: "memory"); }
DEV float xsum16(float v) { auto r = __builtin_amdgcn_permlane16_swap(__float_as_uint(v), __float_as_uint(v), false, false); return __uint_as_float(r[0]) + __uint_as_float(r[1]); }
DEV float xsum32(float v) { auto r = __builtin_amdgcn_permlane32_swap(__float_as_uint(v), __float_as_uint(v), false, false); return __uint_as_float(r[0]) + __uint_as_float(r[1]); }
DEV f32x2 ln_scalars(float sum, float sq, float inv_n) {
  float m = sum * inv_n;
  float v = fmaxf(sq * inv_n - m * m, 0.f);
  float sd = rbf(__builtin_amdgcn_sqrtf(rbf(rbf(v) + 1e-5f)));
  return (f32x2){rbf(m), __builtin_amdgcn_rcpf(sd)};
}
DEV f32x2 gelu2(f32x2 x) {
  const float c1 = -2.3022081983f, c2 = -0.1029432396f;
  f32x2 t = x * x;
  t = t * c2 + c1;
  t = t * x;
  f32x2 e = {__builtin_amdgcn_exp2f(t.x), __builtin_amdgcn_exp2f(t.y)};
  e = e + 1.0f;
  f32x2 r = {__builtin_amdgcn_rcpf(e.x), __builtin_amdgcn_rcpf(e.y)};
  return x * r;
}
DEV f32x2 cmul(f32x2 a, f32x2 b) { return (f32x2){fmaf(a.x, b.x, -(a.y * b.y)), fmaf(a.x, b.y, a.y * b.x)}; }
DEV f32x2 csqr(f32x2 a) { return (f32x2){a.x * a.x - a.y * a.y, 2.f * a.x * a.y}; }

#define K1_PKACT
#define K1_PUBDELAY 8
#define TSTRIDE 65536u
#define K1_PF_STOP 11000u
#define K1_PRIO 2
#define K1_PF 4
constexpr int k1_slot(int u) { return u < 9 ? u : u - 9; }
constexpr int K1_X = 73728, K1_UT = K1_X + 65536, K1_CS = K1_UT + 32 * 272, K1_LN = K1_CS + 512, K1_LN1 = K1_LN + 1024, K1_CNT = K1_LN1 + 1024, K1_DUMP = K1_CNT + 64, K1_LDS = K1_DUMP + 4096;

typedef LDSAS char* lptr;
DEV lptr pinned(lptr p) { asm volatile("" : "+v"(p)); return p; }
DEV u32x4 lread16(lptr p) { return *(LDSAS u32x4*)p; }
DEV u32x4 ltr_frag(lptr p0, lptr p1) {
  s16x4 a = __builtin_amdgcn_ds_read_tr16_b64_v4i16((LDSAS s16x4*)p0), b = __builtin_amdgcn_ds_read_tr16_b64_v4i16((LDSAS s16x4*)p1);
  u32x2 x = __builtin_bit_cast(u32x2, a), y = __builtin_bit_cast(u32x2, b);
  return (u32x4){x.x, x.y, y.x, y.y};
}
DEV void mma(f32x4& c, const u32x4& a, const u32x4& b) { asm volatile("v_mfma_f32_16x16x32_bf16 %0, %1, %2, %0" : "+v"(c) : "v"(a), "v"(b)); }
DEV void mma0(f32x4& c, const u32x4& a, const u32x4& b) { asm volatile("v_mfma_f32_16x16x32_bf16 %0, %1, %2, 0" : "=&v"(c) : "v"(a), "v"(b)); }
template <bool FIRST> DEV void mmaf(f32x4& c, const u32x4& a, const u32x4& b) { if constexpr (FIRST) mma0(c, a, b); else mma(c, a, b); }
DEV void mma_drain() { SB(); asm volatile("s_nop 7\n\ts_nop 1" ::: "memory"); SB(); }
DEV void sbar() { asm volatile("s_barrier" ::: "memory"); }

struct K1L { const char* win; const char* xb; u32 xoff[2]; u32 woff[2]; u32 wdst; u32 xdst; int m0; };
template <int U> DEV void k1_unit(const K1L& g) {
  int line = U % 7 + g.m0; line = line >= 7 ? line - 7 : line;
  if constexpr (U < 7) {
#pragma unroll
    for (int i = 0; i < 4; i++) dma16(g.xb + line * 128 + (i >> 1) * 16 * (DM * 2), g.xoff[i & 1], g.xdst + (U & 3) * 16384 + i * 1024);
  }
#pragma unroll
  for (int i = 0; i < 2; i++) dma16(g.win + (size_t)line * 64 * (FF * 2) + (U < 7 ? FH * 2 : 0), g.woff[i], g.wdst + k1_slot(U) * 8192 + i * 1024);
}
struct K1C { lptr str[4]; lptr wp[4]; };
DEV void k1_xr(const K1C& g, u32x4 (&xf)[4], int M) {
#pragma unroll
  for (int i = 0; i < 4; i++) xf[i] = lread16(g.str[i] + (M & 3) * 16384);
}
template <int U> DEV u32x4 k1_wf(const K1C& g, int f) {
  if constexpr (k1_slot(U) < 8) return ltr_frag(g.wp[f & 3] + k1_slot(U) * 8192 + (f >> 2) * 4096, g.wp[f & 3] + k1_slot(U) * 8192 + (f >> 2) * 4096 + 512);
  else { lptr p = pinned(g.wp[f & 3] + 65536); return ltr_frag(p + (f >> 2) * 4096, p + (f >> 2) * 4096 + 512); }
}
template <int U> DEV void k1_main(const K1C& g, const u32x4 (&xf)[4], u32x4 (&wf)[8], f32x4 (&acc)[2][4], f32x4& cs, const u32x4& ones) {
#pragma unroll
  for (int f = 0; f < 8; f++) {
    if (U % 7 == 0 && f < 4) {
      if (f == 0) mma0(cs, wf[f], ones);
      mma0(acc[0][f & 3], wf[f], xf[0]);
      mma0(acc[1][f & 3], wf[f], xf[1]);
    } else {
      if ((f & 3) == 0) mma(cs, wf[f], ones);
      mma(acc[0][f & 3], wf[f], xf[(f >> 2) * 2]);
      mma(acc[1][f & 3], wf[f], xf[(f >> 2) * 2 + 1]);
    }
    SB(); if constexpr (U % 7 < 6) wf[f] = k1_wf<U + 1>(g, f); SB();
  }
}
template <int M>
DEV void k1_stage(const K1C& g, u32x4 (&xf)[7][4], u32x4 (&wf)[8], f32x4 (&acc)[2][4], f32x4& cs, f32x4 (&rsum)[2], f32x4 (&gram)[2], const u32x4& ones) {
  asm volatile("s_waitcnt lgkmcnt(15)\n\ts_barrier" ::: "memory");
  if constexpr (M < 6) k1_xr(g, xf[M + 1], M + 1);
  SB();
#pragma unroll
  for (int i = 0; i < 4; i++) {
    if (M == 0 && i < 2) { mma0(rsum[i & 1], ones, xf[M][i]); mma0(gram[i & 1], xf[M][i], xf[M][i]); }
    else { mma(rsum[i & 1], ones, xf[M][i]); mma(gram[i & 1], xf[M][i], xf[M][i]); }
  }
  k1_main<M>(g, xf[M], wf, acc, cs, ones);
  if constexpr (M < 6) k1_stage<M + 1>(g, xf, wf, acc, cs, rsum, gram, ones);
}
template <int M>
DEV void k1_z1loop(const K1C& g, u32x4 (&xf)[7][4], u32x4 (&wf)[8], f32x4 (&acc)[2][4], f32x4& cs, const u32x4& ones, const char* pf, u32 l16, u32 dump) {
  if constexpr (M + 1 < K1_PF) dma16(pf + (M + 1) * 1024, l16, dump);
  k1_main<M + 7>(g, xf[M], wf, acc, cs, ones);
  if constexpr (M < 6) k1_z1loop<M + 1>(g, xf, wf, acc, cs, ones, pf, l16, dump);
}
DEV void k1_act(f32x4 (&acc)[2][4], const char* csb, int w, const f32x2 (&ln1)[2]) {
#pragma unroll
  for (int p = 0; p < 4; p++) {
    const f32x4 c = *(const f32x4*)(csb + (p ^ w) * 64);
#pragma unroll
    for (int t = 0; t < 2; t++) {
      f32x2 a = (acc[t][p].xy - c.xy * ln1[t].x) * ln1[t].y;
      f32x2 b = (acc[t][p].zw - c.zw * ln1[t].x) * ln1[t].y;
      a = gelu2(a); b = gelu2(b);
      acc[t][p] = (f32x4){a.x, a.y, b.x, b.y};
    }
  }
}
DEV void act4_stats(f32x4& x, const f32x4& c, float m, float rs, float c1, float& s, float& s2) {
  float x0 = x.x, x1 = x.y, x2 = x.z, x3 = x.w, t0, t1, t2, t3;
  asm("v_fma_f32 %0, -%10, %14, %0\n\tv_fma_f32 %1, -%11, %14, %1\n\tv_fma_f32 %2, -%12, %14, %2\n\tv_fma_f32 %3, -%13, %14, %3\n\t"
      "v_mul_f32 %0, %0, %15\n\tv_mul_f32 %1, %1, %15\n\tv_mul_f32 %2, %2, %15\n\tv_mul_f32 %3, %3, %15\n\t"
      "v_mul_f32 %4, %0, %0\n\tv_mul_f32 %5, %1, %1\n\tv_mul_f32 %6, %2, %2\n\tv_mul_f32 %7, %3, %3\n\t"
      "v_fmamk_f32 %4, %4, 0xbdd2d3e8, %16\n\tv_fmamk_f32 %5, %5, 0xbdd2d3e8, %16\n\tv_fmamk_f32 %6, %6, 0xbdd2d3e8, %16\n\tv_fmamk_f32 %7, %7, 0xbdd2d3e8, %16\n\t"
      "v_mul_f32 %4, %4, %0\n\tv_mul_f32 %5, %5, %1\n\tv_mul_f32 %6, %6, %2\n\tv_mul_f32 %7, %7, %3\n\t"
      "v_exp_f32 %4, %4\n\tv_exp_f32 %5, %5\n\tv_exp_f32 %6, %6\n\tv_exp_f32 %7, %7\n\t"
      "v_add_f32 %4, 1.0, %4\n\tv_add_f32 %5, 1.0, %5\n\tv_add_f32 %6, 1.0, %6\n\tv_add_f32 %7, 1.0, %7\n\t"
      "v_rcp_f32 %4, %4\n\tv_rcp_f32 %5, %5\n\tv_rcp_f32 %6, %6\n\tv_rcp_f32 %7, %7\n\t"
      "v_mul_f32 %0, %0, %4\n\tv_mul_f32 %1, %1, %5\n\tv_mul_f32 %2, %2, %6\n\tv_mul_f32 %3, %3, %7\n\t"
      "v_add_f32 %4, %0, %1\n\tv_fmac_f32 %9, %0, %0\n\tv_add_f32 %5, %2, %3\n\tv_fmac_f32 %9, %1, %1\n\t"
      "v_add_f32 %8, %8, %4\n\tv_fmac_f32 %9, %2, %2\n\tv_add_f32 %8, %8, %5\n\tv_fmac_f32 %9, %3, %3"
      : "+v"(x0), "+v"(x1), "+v"(x2), "+v"(x3), "=&v"(t0), "=&v"(t1), "=&v"(t2), "=&v"(t3), "+v"(s), "+v"(s2)
      : "v"(c.x), "v"(c.y), "v"(c.z), "v"(c.w), "v"(m), "v"(rs), "v"(c1));
  x = (f32x4){x0, x1, x2, x3};
}
DEV int lane_id() { int l = __builtin_amdgcn_mbcnt_hi(~0u, __builtin_amdgcn_mbcnt_lo(~0u, 0u)); asm volatile("" : "+v"(l)); return l; }
DEV void quad_sync(lptr cnt, int l) {
  asm volatile("s_waitcnt lgkmcnt(0)" ::: "memory");
  if (l == 0) __hip_atomic_fetch_add((LDSAS u32*)cnt, 1u, __ATOMIC_RELAXED, __HIP_MEMORY_SCOPE_WORKGROUP);
  while (__builtin_amdgcn_readfirstlane(*(volatile LDSAS u32*)cnt) < 4u) __builtin_amdgcn_s_sleep(0);
  asm volatile("" ::: "memory");
}
DEV void lbar0() { asm volatile("s_waitcnt vmcnt(6) lgkmcnt(0)\n\ts_barrier" ::: "memory"); }

struct Extra { const char* p[8]; };
DEV void k1_body(char* smem, const u16* x, const u16* win, const u16* wsp, const u16* wout, char* ws, const Extra& ex) {
  STAMP_DECL; STAMP(0);
  const int b = blockIdx.x, xcd = b & 7, idx = b >> 3;
  const int w8 = __builtin_amdgcn_readfirstlane(threadIdx.x >> 6), l = threadIdx.x & 63, w = w8 & 3;
  if (b >= 224) {
    const int j = (idx - 28) * 8 + w8;
    const u32 t_start = (u32)__builtin_amdgcn_s_memtime();
    const char* p = (const char*)wsp + ((size_t)xcd * 1024 + 112 * K1_PF) * 1024;
    constexpr int REST = 1024 - 112 * K1_PF;
    u32 dc;
    {
      u64 c0, c1;
      asm volatile("s_getpc_b64 vcc\n\ts_add_u32 vcc_lo, vcc_lo, k1@rel32@lo+4\n\ts_addc_u32 vcc_hi, vcc_hi, k1@rel32@hi+12\n\ts_mov_b64 %0, vcc" : "=s"(c0) :: "scc", "vcc");
      asm volatile("s_getpc_b64 vcc\n\ts_add_u32 vcc_lo, vcc_lo, k3_code_end@rel32@lo+4\n\ts_addc_u32 vcc_hi, vcc_hi, k3_code_end@rel32@hi+12\n\ts_mov_b64 %0, vcc" : "=s"(c1) :: "scc", "vcc");
      const u32 len = (u32)(c1 - c0) & ~3u, o = (u32)(j * 64 + l) * 128u;
      gload4(dc, (const void*)c0, j < 8 ? (o < len ? o : len - 4u) : 0u);
    }
    {
      u32 d0, d1, d2, d3;
      const int n = j * 64 + l;
      const char* p = n < 448 ? ex.p[0] + n * 4096 : (n < 672 ? ex.p[1 + (n - 448) / 56] + ((n - 448) % 56) * 4096 : ex.p[5 + (n % 3)]);
      asm volatile("" : "+v"(p));
      gload4(d0, ws, (u32)((n * TSTRIDE) % (18u << 20)));
      gload4(d1, ws, (u32)(((n + 2048) * TSTRIDE) % (18u << 20)));
      gload4(d2, ws, (u32)(((n + 4096) * TSTRIDE) % (18u << 20)));
      touch(d3, p);
      wait_vm<0>();
      keep(d0); keep(d1); keep(d2); keep(d3);
      keep(dc);
    }
    const char* po = (const char*)wout + (size_t)(xcd * 98) * 1024;
#pragma unroll
    for (int k = 0; k < 4; k++) { int c = j + 32 * k; c = c < 98 ? c : 97; dma16(po + c * 1024, l * 16, lds_addr(smem) + w8 * 1024); }
#pragma unroll 1
    for (int i = j; i < REST; i += 32) {
      dma16(p + (size_t)i * 1024, l * 16, lds_addr(smem) + w8 * 1024);
      if ((u32)__builtin_amdgcn_s_memtime() - t_start > K1_PF_STOP) break;
    }
    STAMP(1); STAMP_FLUSH(0);
    return;
  }
  const int i7 = (idx * 37) >> 8;
  const int panel = (xcd >> 1) * 4 + i7, sib = (xcd & 1) * 7 + idx - 7 * i7;
  const lptr lds = (lptr)smem;
  K1L g;
  if (w8 >= 4) {
    g.win = (const char*)win + (size_t)(16 * w) * (FF * 2) + sib * 128;
    g.xb = (const char*)x + (size_t)(panel * 128 + w * 32) * (DM * 2);
    g.xoff[0] = (u32)((l >> 3) * (DM * 2) + (((l & 7) ^ (l >> 4)) * 16));
    g.xoff[1] = (u32)((8 + (l >> 3)) * (DM * 2) + (((l & 7) ^ ((l >> 4) | 4)) * 16));
    g.woff[0] = (u32)((l >> 3) * (FF * 2) + (((l & 7) ^ (((l >> 4) & 1) << 1)) * 16));
    g.woff[1] = (u32)((8 + (l >> 3)) * (FF * 2) + (((l & 7) ^ (((l >> 4) & 1) << 1) ^ 4) * 16));
    g.xdst = lds_addr(smem) + K1_X + w * 4096;
    g.wdst = lds_addr(smem) + w * 2048;
    g.m0 = sib >> 1;
    k1_unit<0>(g); k1_unit<1>(g);
  }
  K1C c;
  {
    const int rho = l & 15, q = l >> 4, qq = (l >> 2) & 3, pp = l & 3;
#pragma unroll
    for (int i = 0; i < 4; i++) c.str[i] = pinned(lds + K1_X + w * 4096 + (i & 1) * 2048 + rho * 128 + (((4 * (i >> 1) + q) ^ (rho >> 1)) * 16));
    const int sg = (qq >> 1) | ((q & 1) << 1);
#pragma unroll
    for (int f = 0; f < 4; f++) c.wp[f] = lds + (8 * q + qq) * 128 + (((f ^ w) ^ sg) * 32) + 8 * pp;
  }
  u32x4 ones = {0x3f803f80u, 0x3f803f80u, 0x3f803f80u, 0x3f803f80u};
  pin(ones);
  u32x4 xf[7][4], wf[8];
  f32x4 acc[2][4], cs;
  if (w8 >= 4) {
    lbar0();
    k1_unit<2>(g); k1_xr(c, xf[0], 0); lbar0();
    k1_unit<3>(g); k1_xr(c, xf[1], 1); lbar0();
    k1_unit<4>(g); k1_xr(c, xf[2], 2); lbar0();
    k1_unit<5>(g); k1_xr(c, xf[3], 3); lbar0();
    k1_unit<6>(g); k1_xr(c, xf[4], 4); lbar0();
    k1_unit<7>(g); k1_unit<8>(g); k1_unit<9>(g); k1_xr(c, xf[5], 5); lbar0();
    k1_unit<10>(g); k1_unit<11>(g); k1_unit<12>(g); k1_xr(c, xf[6], 6);
    asm volatile("s_waitcnt lgkmcnt(0)\n\ts_barrier" ::: "memory");
    k1_unit<13>(g);
    STAMP(1);
    const char* pf = (const char*)wsp + ((size_t)xcd * 1024 + (idx * 4 + w) * K1_PF) * 1024;
    const u32 l16 = l * 16, dump = lds_addr(smem) + K1_DUMP + w * 1024;
    if constexpr (K1_PF > 0) dma16(pf, l16, dump);
    wait_vm<(K1_PF > 0 ? 1 : 0)>(); sbar();
    STAMP(2);
    f32x2 ln1[2];
    {
    const int rho = l & 15, q = l >> 4;
#pragma unroll
    for (int t = 0; t < 2; t++) ln1[t] = *(const f32x2*)(smem + K1_LN1 + w * 256 + (t * 16 + rho) * 8);
#pragma unroll
    for (int f = 0; f < 8; f++) wf[f] = k1_wf<7>(c, f);
#pragma unroll
    for (int t = 0; t < 2; t++) {
      const u32x4 va = xf[0][t], vb = xf[0][2 + t];
      const u32 msk = (sib & 1) ? 0xffffffffu : 0u;
      const u32x4 v = (va & ~msk) | (vb & msk);
      u16* ub = (u16*)(smem + K1_UT + (8 * q) * 272 + (w * 32 + t * 16 + rho) * 2);
#pragma unroll
      for (int j = 0; j < 4; j++) {
        float a = rbf(rbf(bflo(v[j]) - ln1[t].x) * ln1[t].y), bb = rbf(rbf(bfhi(v[j]) - ln1[t].x) * ln1[t].y);
        u32 o = pk_f16(a, bb);
        ub[(2 * j) * 136] = (u16)o;
        ub[(2 * j + 1) * 136] = (u16)(o >> 16);
      }
    }
}
    SB();
    k1_z1loop<0>(c, xf, wf, acc, cs, ones, pf, l16, dump);
    mma_drain();
    const int l = lane_id(), rho = l & 15, q = l >> 4;
    if (rho == 0) *(f32x4*)(smem + K1_CS + 256 + w * 64 + q * 16) = cs;
    STAMP(3);
    quad_sync(lds + K1_CNT, l);
    STAMP(4);
#pragma unroll
    for (int i = 0; i < 2; i++) {
      const u32x4 uo = *(const u32x4*)(smem + K1_UT + (w * 8 + i * 4 + q) * 272 + rho * 16);
      gstore16(ws, (u32)(WS_UT + (sib * 32 + w * 8 + i * 4 + q) * (SEQ * 2) + panel * 256 + rho * 16), uo);
    }
    k1_act(acc, smem + K1_CS + 256 + q * 16, w, ln1);
#pragma unroll
    for (int t = 0; t < 2; t++)
#pragma unroll
      for (int cc = 0; cc < 2; cc++) {
        u32x2 a = {pk_bf16(acc[t][2 * cc].x, acc[t][2 * cc].y), pk_bf16(acc[t][2 * cc].z, acc[t][2 * cc].w)};
        u32x2 bb = {pk_bf16(acc[t][2 * cc + 1].x, acc[t][2 * cc + 1].y), pk_bf16(acc[t][2 * cc + 1].z, acc[t][2 * cc + 1].w)};
        const u32x4 o = (w & 1) ? (u32x4){bb.x, bb.y, a.x, a.y} : (u32x4){a.x, a.y, bb.x, bb.y};
        gstore16(ws, (u32)(WS_Z1F + ((panel * 8 + w * 2 + t) * 28 + sib * 2 + (cc ^ (w >> 1))) * 1024 + l * 16), o);
      }
    STAMP(5); STAMP_FLUSH(0);
    return;
  }
  const int grp = panel * 4 + w;
  u32 tch;
  {
    const int h = l >> 5, n = (w & 1) * 32 + (l & 31);
    (void)h;
    const int wl = panel * 56 + (n < 56 ? n : 55);
    const u32 xo = (u32)(panel * 128 * (DM * 2) + (sib * 64 + n) * 128);
    const u32 wo = (u32)((wl >> 1) * (FF * 2) + ((wl & 1) ? 0 : FH * 2) + sib * 128);
    gload4(tch, w < 2 ? (const void*)x : (const void*)win, w < 2 ? xo : wo);
  }
  u32 tag;
  asm volatile("s_load_dword %0, %1, 0x0\n\ts_waitcnt lgkmcnt(0)" : "=s"(tag) : "s"(ws + WS_EP + grp * 256) : "memory");
  tag += 1u;
  f32x4 rsum[2], gram[2];
  if (threadIdx.x < 2) *(u32*)(smem + K1_CNT + threadIdx.x * 4) = 0u;
  bar();
  STAMP(2);
  k1_xr(c, xf[0], 0);
#pragma unroll
  for (int f = 0; f < 8; f++) wf[f] = k1_wf<0>(c, f);
  k1_stage<0>(c, xf, wf, acc, cs, rsum, gram, ones);
  mma_drain();
  wait_vm<0>(); keep(tch);
  const int rho = l & 15, q = l >> 4, qq = (l >> 2) & 3, pp = l & 3;
  if (rho == 0) *(f32x4*)(smem + K1_CS + w * 64 + q * 16) = cs;
  STAMP(3);
  f32x2 ln1[2];
#pragma unroll
  for (int t = 0; t < 2; t++) {
    float d = (rho & 2) ? ((rho & 1) ? gram[t].w : gram[t].z) : ((rho & 1) ? gram[t].y : gram[t].x);
    d = ((rho >> 2) == q) ? d : 0.f;
    d = xsum32(xsum16(d));
    ln1[t] = ln_scalars(rsum[t].x, d, 1.f / DM);
    if (q == 0) *(f32x2*)(smem + K1_LN1 + w * 256 + (t * 16 + rho) * 8) = ln1[t];
  }
  bar();
  __builtin_amdgcn_s_setprio(K1_PRIO);
  STAMP(4);
  GAS u64* gr = (GAS u64*)(ws + WS_GR) + (size_t)grp * 14 * 64;
  {
    float st[2][2] = {{0.f, 0.f}, {0.f, 0.f}};
    k1_act(acc, smem + K1_CS + q * 16, w, ln1);
#pragma unroll
    for (int t = 0; t < 2; t++) {
      f32x2 s = {0.f, 0.f}, s2 = {0.f, 0.f};
#pragma unroll
      for (int p = 0; p < 4; p++) {
        s += acc[t][p].xy; s += acc[t][p].zw;
        s2 += acc[t][p].xy * acc[t][p].xy; s2 += acc[t][p].zw * acc[t][p].zw;
      }
      st[t][0] = s.x + s.y; st[t][1] = s2.x + s2.y;
    }
#pragma unroll
    for (int t = 0; t < 2; t++) { st[t][0] = xsum32(xsum16(st[t][0])); st[t][1] = xsum32(xsum16(st[t][1])); }
    const float v = (q & 2) ? ((q & 1) ? st[1][1] : st[0][1]) : ((q & 1) ? st[1][0] : st[0][0]);
    __hip_atomic_store(gr + sib * 64 + l, ((u64)tag << 32) | __float_as_uint(v), __ATOMIC_RELAXED, __HIP_MEMORY_SCOPE_AGENT);
  }
  STAMP(5);
  __builtin_amdgcn_s_sleep(K1_PUBDELAY);
  STAMP(6);
  u64 rv[14];
#pragma unroll
  for (int i = 0; i < 14; i++) rv[i] = __hip_atomic_load(gr + i * 64 + l, __ATOMIC_RELAXED, __HIP_MEMORY_SCOPE_AGENT);
  STAMP(7);
  float tot;
  for (u32 spins = 0;; spins++) {
    bool ok = true; tot = 0.f;
#pragma unroll
    for (int i = 0; i < 14; i++) { ok &= (u32)(rv[i] >> 32) == tag; tot += __uint_as_float((u32)rv[i]); }
    if (__all(ok) || spins > (1u << 20)) break;
#pragma unroll
    for (int i = 0; i < 14; i++) rv[i] = __hip_atomic_load(gr + i * 64 + l, __ATOMIC_RELAXED, __HIP_MEMORY_SCOPE_AGENT);
  }
  STAMP(8);
  if (sib == 0 && l == 0) __hip_atomic_store((GAS u32*)(ws + WS_EP + grp * 256), tag, __ATOMIC_RELAXED, __HIP_MEMORY_SCOPE_AGENT);
  {
    auto bb = __builtin_amdgcn_permlane32_swap(__float_as_uint(tot), __float_as_uint(tot), false, false);
    const f32x2 lr = ln_scalars(__uint_as_float(bb[0]), __uint_as_float(bb[1]), 1.f / FH);
    auto lm = __builtin_amdgcn_permlane16_swap(__float_as_uint(lr.x), __float_as_uint(lr.x), false, false);
    auto ls = __builtin_amdgcn_permlane16_swap(__float_as_uint(lr.y), __float_as_uint(lr.y), false, false);
    char* const stg = smem + K1_X + w * 8192;
#pragma unroll
    for (int t = 0; t < 2; t++) {
      const float m2 = __uint_as_float(lm[t]), r2 = __uint_as_float(ls[t]);
#pragma unroll
      for (int p = 0; p < 4; p++) {
        f32x2 a = (acc[t][p].xy - m2) * r2, b2 = (acc[t][p].zw - m2) * r2;
        *(u32x2*)(stg + (16 * t + rho) * 136 + p * 32 + q * 8) = (u32x2){pk_bf16(a.x, a.y), pk_bf16(b2.x, b2.y)};
      }
    }
#pragma unroll
    for (int p = 0; p < 4; p++) {
      const char* rp = stg + (8 * q + qq) * 136 + p * 32 + pp * 8;
      u32x4 o = tr_frag(rp, rp + 4 * 136);
      const int ft = sib * 4 + (p ^ w);
      Z2STORE(ws, (u32)(WS_Z2F + (((ft / 7) * 64 + grp) * 7 + ft % 7) * 1024 + l * 16), o);
    }
  }
  STAMP(9); STAMP_FLUSH(0);
}
#define K1_ATTR
extern "C" __global__ K1_ATTR __launch_bounds__(512, 2) void k1(const u16* x, const u16* win, const u16* wsp, const u16* wout, char* ws, Extra ex) {
  __shared__ __attribute__((aligned(16))) char smem[K1_LDS];
  if (blockIdx.x == 0 && threadIdx.x == 0) __hip_atomic_store((GAS u64*)(ws + (40u << 20)), (u64)__builtin_amdgcn_s_memrealtime(), __ATOMIC_RELAXED, __HIP_MEMORY_SCOPE_AGENT);
  if (threadIdx.x == 0) __hip_atomic_store((GAS u64*)(ws + (41u << 20)) + blockIdx.x, (u64)__builtin_amdgcn_s_memrealtime(), __ATOMIC_RELAXED, __HIP_MEMORY_SCOPE_AGENT);
  k1_body(smem, x, win, wsp, wout, ws, ex);
  if ((threadIdx.x & 63) == 0) __hip_atomic_store((GAS u64*)(ws + (42u << 20)) + blockIdx.x * 8 + (threadIdx.x >> 6), (u64)__builtin_amdgcn_s_memrealtime(), __ATOMIC_RELAXED, __HIP_MEMORY_SCOPE_AGENT);
}

#define K2_R 18
#define K2_PF 7
#define K2_TD 0
constexpr bool k2_touch_at(int tau) { return K2_TD > 0 && tau % 14 == 0 && tau / 7 + K2_TD <= 14; }
constexpr int K2_EPI = 5, K2_EPI_AT = 112 - K2_R + 2, K2_PRO = 16 + K2_R + (K2_TD > 0 ? 2 : 0);
constexpr int k2_apiece(int tau) {
  int s = tau / 7, f = tau % 7;
  int a = (s + 3) / 2 + ((s & 1) ? 0 : 1) - ((s & 1) ? 0 : 1);
  if (s & 1) { a = (s + 3) / 2; return (a <= 7 && (f & 1) == 0) ? 8 * a + f / 2 : -1; }
  a = (s + 2) / 2;
  return (a >= 2 && a <= 7 && f < 4) ? 8 * a + 4 + f : -1;
}
constexpr int k2_ops_after(int tau) { return (tau + K2_R < 112 ? 1 : 0) + (k2_apiece(tau) >= 0 ? 1 : 0) + (tau == K2_EPI_AT ? K2_EPI + K2_PF : 0) + (k2_touch_at(tau) ? 1 : 0); }
constexpr int k2_issued(int tau) { int n = K2_PRO; for (int i = 0; i < tau; i++) n += k2_ops_after(i); return n; }
constexpr int k2_pos_B(int n) { return n < 14 ? 8 + n + 1 : (n < K2_R ? 16 + n + 1 : k2_issued(n - K2_R) + 1); }
constexpr int k2_pos_A(int a) { return a == 0 ? 8 : (a == 1 ? 30 : k2_issued(7 * (2 * a - 2) + 4)); }
constexpr int k2_wait(int tau) {
  int need = k2_pos_B(tau);
  int s = tau / 7, f = tau % 7;
  if ((s & 1) && f == 2 && (s + 1) / 2 <= 7) { int a = k2_pos_A((s + 1) / 2); need = a > need ? a : need; }
  return k2_issued(tau) - need;
}
#define K2_MFMA mfma_bf
struct K2G { const char* wsp0; const char* wsp; const char* z2; const char* z1; const char* bsp; const char* pf; u32 aoff[2]; u32 boff; u32 adst; u32 dump; u32 tb; int arot; int w; };
template <int P> DEV void k2_a1(const K2G& g) {
  constexpr int SS = P / 8;
  const int i = (P % 8) ^ g.arot;
  dma16(g.wsp + SS * 128 + (size_t)i * 8 * 4096, g.aoff[P & 1], g.adst + (SS & 1) * 8192 + ((i ^ (2 * g.w)) * 1024));
}
template <int A, int B> DEV void k2_as(const K2G& g) { if constexpr (A < B) { k2_a1<A>(g); k2_as<A + 1, B>(g); } }
template <int N> DEV void k2_b1(const K2G& g, u32x4 (&bf)[K2_R]) {
 gload16<(N % 8 - 4) * 1024>(bf[N % K2_R], g.z2 + (N / 8) * 8192, g.boff); }
template <int A, int B> DEV void k2_bs(const K2G& g, u32x4 (&bf)[K2_R]) { if constexpr (A < B) { k2_b1<A>(g, bf); k2_bs<A + 1, B>(g, bf); } }
template <int I> DEV u32x4 k2_read_a(const char* ap0, const char* ap1, int t) {
  return *(const u32x4*)(((I & 1) ? ap1 : ap0) + ((I >> 1) & 1) * 8192 + t * 2048);
}
template <int TAU>
DEV void k2_group(const K2G& g, u32x4 (&bf)[K2_R], u32x4 (&af)[2][4], const char* ap0, const char* ap1, f32x4 (&acc)[4][7], u32x4 (&ep)[4], u32& bw, u32 (&tch)[2]) {
  constexpr int S = TAU / 7, F = TAU % 7;
  wait_vm<k2_wait(TAU)>();
  pin(bf[TAU % K2_R]);
  if constexpr (S < 15 && F >= 2 && F < 6) af[(S + 1) & 1][F - 2] = k2_read_a<S + 1>(ap0, ap1, F - 2);
  SB();
#pragma unroll
  for (int t = 0; t < 4; t++) acc[t][F] = K2_MFMA(bf[TAU % K2_R], af[S & 1][t], acc[t][F]);
  SB();
  if constexpr (TAU + K2_R < 112) k2_b1<TAU + K2_R>(g, bf);
  if constexpr (k2_apiece(TAU) >= 0) k2_a1<k2_apiece(TAU)>(g);
  if constexpr (k2_touch_at(TAU)) { keep(tch[(TAU / 14) & 1]); gload4(tch[(TAU / 14) & 1], g.z2 + (S + K2_TD) * 7168 - 4096, g.tb); }
  if constexpr (TAU == 111) { keep(tch[0]); keep(tch[1]); }
  if constexpr (TAU == K2_EPI_AT) {
    gload16<0>(ep[0], g.z1, g.boff); gload16<1024>(ep[1], g.z1, g.boff); gload16<2048>(ep[2], g.z1, g.boff); gload16<3072>(ep[3], g.z1, g.boff);
    gload4(bw, g.bsp, (g.boff >> 3) & 28u);
#pragma unroll
    for (int i = 0; i < K2_PF; i++) dma16(g.pf + i * 1024, g.boff, g.dump);
  }
  if constexpr (TAU < 111) k2_group<TAU + 1>(g, bf, af, ap0, ap1, acc, ep, bw, tch);
}

DEV void k2_gemm(char* smem, int b, const u16* wsp, const u16* bsp, const u16* wout, char* ws) {
  STAMP_DECL; STAMP(0);
  const int xcd = b & 7, idx = b >> 3;
  const int tp = (xcd >> 1) * 8 + (idx >> 2), fp = (xcd & 1) * 4 + (idx & 3);
  const int w = __builtin_amdgcn_readfirstlane(threadIdx.x >> 6), l = threadIdx.x & 63;
  const int rho = l & 15, q = l >> 4;
  const int tt = tp * 4 + w;
  char* const reg = smem + w * 16384;
  K2G g;
  g.wsp = (const char*)wsp + (size_t)tp * 64 * 4096 + w * 1024;
  g.wsp0 = (const char*)wsp + (size_t)tp * 64 * 4096 + w * 65536;
  g.z2 = ws + WS_Z2F + ((size_t)(fp * 64 + w * 16) * 7 + 4) * 1024;
  g.z1 = ws + WS_Z1F + ((size_t)tt * 28 + ((fp * 7) >> 1)) * 1024;
  g.bsp = (const char*)bsp + tt * 32;
  {
    int pc = (idx * 4 + w) * 7;
    pc = pc > 784 - 7 ? 784 - 7 : pc;
    g.pf = (const char*)wout + pc * 1024;
  }
  g.aoff[0] = (u32)((l >> 3) * 4096 + (((l & 7) ^ (l >> 4)) * 16));
  g.aoff[1] = (u32)((l >> 3) * 4096 + (((l & 7) ^ ((l >> 4) | 4)) * 16));
  g.boff = l * 16;
  g.adst = lds_addr(reg);
  g.dump = lds_addr(smem) + 65536 + w * 1024;
  g.w = w;
  g.arot = (idx & 3) * 2;
  u32x4 bf[K2_R], af[2][4], ep[4];
  u32 bw, tch[2];
  g.tb = (u32)(((idx >> 2) * 14 + (l < 14 ? l : 13)) * 128);
  tch[0] = tch[1] = 0u;
  k2_as<0, 8>(g); k2_bs<0, 14>(g, bf); k2_as<8, 16>(g); k2_bs<14, K2_R>(g, bf);
  const char* ap0 = reg + rho * 128 + (((0 + q) ^ (rho >> 1)) * 16);
  const char* ap1 = reg + rho * 128 + (((4 + q) ^ (rho >> 1)) * 16);
  f32x4 acc[4][7] = {};
  STAMP(1);
  wait_vm<K2_PRO - 8>();
  STAMP(2);
#pragma unroll
  for (int t = 0; t < 4; t++) af[0][t] = k2_read_a<0>(ap0, ap1, t);
  k2_group<0>(g, bf, af, ap0, ap1, acc, ep, bw, tch);
  STAMP(3);
#pragma unroll
  for (int f = 0; f < 7; f++) {
    *(f32x4*)(reg + f * 1024 + l * 16) = acc[1][f];
    *(f32x4*)(reg + (7 + f) * 1024 + l * 16) = acc[3][f];
  }
  bar();
  STAMP(4);
  char* const in1 = smem + (w ^ 1) * 16384;
#pragma unroll
  for (int f = 0; f < 7; f++) acc[2][f] += *(const f32x4*)(in1 + (7 + f) * 1024 + l * 16);
#pragma unroll
  for (int f = 0; f < 7; f++) *(f32x4*)(in1 + (7 + f) * 1024 + l * 16) = acc[2][f];
#pragma unroll
  for (int f = 0; f < 7; f++) acc[0][f] += *(const f32x4*)(in1 + f * 1024 + l * 16);
  bar();
  char* const in2 = smem + (w ^ 3) * 16384;
#pragma unroll
  for (int i = 0; i < 1; i++) wait_vm<K2_PF>();
#pragma unroll
  for (int i = 0; i < 4; i++) pin(ep[i]);
  asm volatile("" : "+v"(bw));
  const float bs = (rho & 1) ? bfhi(bw) : bflo(bw);
  STAMP(5);
  const int odd = fp & 1;
  u32x2 hv[8];
#pragma unroll
  for (int f = 0; f < 7; f++) {
    f32x4 v = acc[0][f] + *(const f32x4*)(in2 + (7 + f) * 1024 + l * 16);
    v = v + bs;
    const u32x4 ze = ep[f >> 1], zo = ep[(f + 1) >> 1];
    const u32 za = odd ? ((f & 1) ? zo.x : zo.z) : ((f & 1) ? ze.z : ze.x);
    const u32 zb = odd ? ((f & 1) ? zo.y : zo.w) : ((f & 1) ? ze.w : ze.y);
    hv[f] = (u32x2){pk_bf16(v.x * bflo(za), v.y * bfhi(za)), pk_bf16(v.z * bflo(zb), v.w * bfhi(zb))};
  }
  const u32 hb = (u32)(WS_HF + (tt * 28 + ((fp * 7) >> 1)) * 1024 + l * 16);
  if (odd) {
    gstore8(ws, hb + 8, hv[0]);
#pragma unroll
    for (int i = 0; i < 3; i++) gstore16(ws, hb + (i + 1) * 1024, (u32x4){hv[1 + 2 * i].x, hv[1 + 2 * i].y, hv[2 + 2 * i].x, hv[2 + 2 * i].y});
  } else {
#pragma unroll
    for (int i = 0; i < 3; i++) gstore16(ws, hb + i * 1024, (u32x4){hv[2 * i].x, hv[2 * i].y, hv[1 + 2 * i].x, hv[1 + 2 * i].y});
    gstore8(ws, hb + 3 * 1024, hv[6]);
  }
  STAMP(6); STAMP_FLUSH(1);
}

constexpr int KG_ROW = 272, KG_WAVE = 53 * KG_ROW;
constexpr int CV_CS = 4160, CV_T = 8 * CV_CS, CV_U = 2 * CV_T, CV_CMP = CV_U + 2 * 4608;
DEV f32x2 cexp_pow2(float gl, float fh, float fl, float p) {
  float m = __builtin_amdgcn_exp2f(gl * p);
  float r = __builtin_amdgcn_fractf(fh * p) + fl * p;
  return (f32x2){m * __builtin_amdgcn_cosf(r), m * __builtin_amdgcn_sinf(r)};
}
DEV float exp_acc(float x) {
  const float h = x * 1.44269504089f;
  const float lo = fmaf(x, 1.44269504089f, -h) + x * 1.925963e-8f;
  return __builtin_amdgcn_exp2f(h) * fmaf(lo, 0.69314718056f, 1.f);
}
struct KGen { f32x2 e1, e2, e4, e8, e16, e32, e64, e128, e256, e512, e1024, e2048, c0; };
DEV KGen kg_gens(float la, float ai, float cr, float ci, float dt, float sc) {
  const float zr = -rbf(exp_acc(la)) * dt, zi = ai * dt;
  const float gl = zr * 1.44269504089f;
  const float fh = zi * 0.15915494309f;
  const float fl = fmaf(zi, 0.15915494309f, -fh) + zi * 6.4206382e-9f;
  KGen k;
  k.c0 = (f32x2){cr * sc, ci * sc};
  k.e1 = cexp_pow2(gl, fh, fl, 1.f); k.e2 = csqr(k.e1); k.e4 = csqr(k.e2); k.e8 = csqr(k.e4);
  k.e16 = cexp_pow2(gl, fh, fl, 16.f); k.e32 = csqr(k.e16); k.e64 = csqr(k.e32); k.e128 = csqr(k.e64);
  k.e256 = cexp_pow2(gl, fh, fl, 256.f); k.e512 = csqr(k.e256); k.e1024 = csqr(k.e512); k.e2048 = csqr(k.e1024);
  return k;
}
DEV void kg_tables(const KGen& k, u32* tw) {
#define TW(row, v) tw[(row) * (KG_ROW / 4)] = (v)
  {
    f32x2 tb[16];
    tb[0] = (f32x2){128.f, 0.f};
    tb[1] = k.e8 * 128.f;
    tb[2] = k.e16 * 128.f; tb[3] = cmul(tb[1], k.e16);
    tb[4] = k.e32 * 128.f;
#pragma unroll
    for (int i = 1; i < 4; i++) tb[4 + i] = cmul(tb[i], k.e32);
    tb[8] = k.e64 * 128.f;
#pragma unroll
    for (int i = 1; i < 8; i++) tb[8 + i] = cmul(tb[i], k.e64);
    TW(0, pk_f16(128.f, 0.f));
    TW(16, pk_f16(k.e128.x * 128.f, k.e128.y * 128.f));
#pragma unroll
    for (int i = 1; i < 16; i++) { TW(i, pk_f16(tb[i].x, tb[i].y)); f32x2 u = cmul(tb[i], k.e128); TW(16 + i, pk_f16(u.x, u.y)); }
  }
  {
    f32x2 a[21];
    a[0] = k.c0; a[1] = cmul(k.c0, k.e1); a[2] = cmul(k.c0, k.e2); a[3] = cmul(a[1], k.e2);
#pragma unroll
    for (int i = 0; i < 4; i++) a[4 + i] = cmul(a[i], k.e4);
#pragma unroll
    for (int i = 0; i < 4; i++) a[8 + i] = cmul(a[2 * i], k.e256);
    a[12] = cmul(a[0], k.e512); a[13] = cmul(a[4], k.e512); a[14] = cmul(a[8], k.e512); a[15] = cmul(a[10], k.e512);
    a[16] = cmul(a[0], k.e1024); a[17] = cmul(a[8], k.e1024); a[18] = cmul(a[12], k.e1024); a[19] = cmul(a[14], k.e1024);
    a[20] = cmul(a[0], k.e2048);
#pragma unroll
    for (int i = 0; i < 21; i++) TW(32 + i, pk_f16(a[i].x, -a[i].y));
  }
#undef TW
}
template <int CTRL> DEV u32 dpp(u32 old, u32 src) { return __builtin_amdgcn_update_dpp(old, src, CTRL, 0xf, 0xf, false); }
template <int CTRL> DEV float dpp_add(float v) { return v + __uint_as_float(__builtin_amdgcn_update_dpp(0u, __float_as_uint(v), CTRL, 0xf, 0xf, true)); }
DEV void cv_frags(u32x4 (&A)[8], u32x4 (&B)[8], const char* pa, const char* pb) {
#pragma unroll
  for (int i = 0; i < 8; i++) {
    A[i] = *(const u32x4*)(pa - 512 * i);
    B[i] = *(const u32x4*)(pb + 512 * i);
  }
}
DEV void cv_mma(const u32x4 (&A)[8], const u32x4 (&B)[8], f32x4 (&acc)[8]) {
#pragma unroll
  for (int a = 0; a < 8; a++)
#pragma unroll
    for (int s = 7; s >= a; s--) acc[s] = mfma_h(A[a], B[s - a], acc[s]);
}
#define SSM_PRIO 0
DEV void k2_ssm(char* smem, int cb, const u16* lar, const u16* aim, const u16* cre, const u16* cim, const u16* ldt, const u16* dsk, char* ws) {
  STAMP_DECL; STAMP(0);
  __builtin_amdgcn_s_setprio(3);
  const int w = __builtin_amdgcn_readfirstlane(threadIdx.x >> 6), l = threadIdx.x & 63;
  const int ch = w >> 1, d = cb * 2 + ch, hw = w & 1, t = hw * 64 + l;
  const int rho = l & 15, q = l >> 4;
  const size_t o = (size_t)d * NMODE;
  const u32 vla = *(const u32*)(lar + o + hw * 128 + 2 * l), vai = *(const u32*)(aim + o + hw * 128 + 2 * l);
  const u32 vcr = *(const u32*)(cre + o + hw * 128 + 2 * l), vci = *(const u32*)(cim + o + hw * 128 + 2 * l);
  const u32 ocr = *(const u32*)(cre + o + (hw ^ 1) * 128 + 2 * l), oci = *(const u32*)(cim + o + (hw ^ 1) * 128 + 2 * l);
  const u32 vdt = ldt[d], vdk = dsk[d];
  char* const T = smem + ch * CV_T;
  char* const U = smem + CV_U + ch * 4608;
  u16* const cmp = (u16*)(smem + CV_CMP + ch * 1408);
#pragma unroll
  for (int i = 0; i < 2; i++) dma16(ws + WS_UT + (size_t)d * (SEQ * 2) + (hw * 2 + i) * 1024, l * 16, lds_addr(U) + 512 + (hw * 2 + i) * 1024);
  __builtin_amdgcn_s_setprio(SSM_PRIO);
  if (t < 32) *(u32x4*)(U + t * 16) = (u32x4){0u, 0u, 0u, 0u};
  u32 gi[2][8];
#pragma unroll
  for (int c = 0; c < 2; c++)
#pragma unroll
    for (int j = 0; j < 4; j++) {
      const int l0 = 2047 - (hw * 128 + c * 64 + l) * 8 - 2 * j, l1 = l0 - 1;
      const u32 bits = pk_bf16((float)l0, (float)l1);
      int i0 = (int)(bits & 0xffffu) - 0x4280, i1 = (int)(bits >> 16) - 0x4280;
      if (c == 1) { i0 = l0 < 128 ? l0 : i0; i1 = l1 < 128 ? l1 : i1; }
      gi[c][2 * j] = i0; gi[c][2 * j + 1] = i1;
    }
  const float dt = rbf(exp_acc(bf2f(vdt)));
  const float dk = bf2f(vdk);
  float S = fabsf(bflo(vcr)) + fabsf(bfhi(vcr)) + fabsf(bflo(vci)) + fabsf(bfhi(vci));
  float S2 = fabsf(bflo(ocr)) + fabsf(bfhi(ocr)) + fabsf(bflo(oci)) + fabsf(bfhi(oci));
  S = hw ? S2 + S : S + S2;
  S = dpp_add<0xb1>(S); S = dpp_add<0x4e>(S); S = dpp_add<0x141>(S); S = dpp_add<0x140>(S);
  S = xsum32(xsum16(S));
  int e = (int)((__float_as_uint(S) >> 23) & 255u) - 125;
  e = e < -100 ? -100 : (e > 100 ? 100 : e);
  e = __builtin_amdgcn_readfirstlane(e);
  const float sc = __uint_as_float((u32)(127 + 8 - e) << 23);
  const float ks = dt * __uint_as_float((u32)(127 + e - 14) << 23);
  STAMP(1);
  char* tab = smem + w * KG_WAVE;
  f32x16 kacc = {};
  {
    const int r = l & 31, h = l >> 5;
    const char* pa = tab + (32 + (r < 21 ? r : 20)) * KG_ROW + h * 16;
    const char* pb = tab + r * KG_ROW + h * 16;
    const KGen k0 = kg_gens(bflo(vla), bflo(vai), bflo(vcr), bflo(vci), dt, sc);
    STAMP(8);
    kg_tables(k0, (u32*)(tab + l * 4));
    SB();
    STAMP(9);
    u32x4 fa[8], fb[8];
#pragma unroll
    for (int kk = 0; kk < 8; kk++) { fa[kk] = *(const u32x4*)(pa + kk * 32); fb[kk] = *(const u32x4*)(pb + kk * 32); }
    SB();
    const KGen k1 = kg_gens(bfhi(vla), bfhi(vai), bfhi(vcr), bfhi(vci), dt, sc);
#pragma unroll
    for (int kk = 0; kk < 8; kk++) kacc = mfma32_h(fa[kk], fb[kk], kacc);
    STAMP(10);
    kg_tables(k1, (u32*)(tab + l * 4));
    STAMP(11);
#pragma unroll
    for (int kk = 0; kk < 8; kk++) kacc = mfma32_h(*(const u32x4*)(pa + kk * 32), *(const u32x4*)(pb + kk * 32), kacc);
    STAMP(12);
    float* give = (float*)tab + l;
    float keep[6];
#pragma unroll
    for (int i = 0; i < 6; i++) { give[i * 64] = hw ? kacc[i] : kacc[6 + i]; keep[i] = hw ? kacc[6 + i] : kacc[i]; }
    const int b1 = h ? 384 + 2 * r : 256 + 4 * r, b2 = h ? (r == 0 ? 640 : 672) : 512 + r;
    const int x0 = hw ? b1 + (h ? 64 : 2) : 4 * h + 8 * r;
    const int x1 = hw ? b2 : x0 + 2, d1 = hw ? (h ? (r == 0 ? 32 : 0) : 32) : 1;
    const int x2 = hw ? (h ? 672 : b2 + 64) : b1, d2 = hw ? (h ? 0 : 32) : 1;
    bar();
    const float* take = (const float*)(smem + (w ^ 1) * KG_WAVE) + l;
    const int xi[6] = {x0, x0 + 1, x1, x1 + d1, x2, x2 + d2};
#pragma unroll
    for (int i = 0; i < 6; i++) {
      const float o = take[i * 64];
      cmp[xi[i]] = __builtin_bit_cast(u16, (_Float16)(hw ? o + keep[i] : keep[i] + o));
    }
  }
  STAMP(2);
  bar();
  {
    u32 ra[3][4];
#pragma unroll
    for (int c = 0; c < 2; c++)
#pragma unroll
      for (int j = 0; j < 4; j++) ra[c][j] = (u32)cmp[gi[c][2 * j]] | ((u32)cmp[gi[c][2 * j + 1]] << 16);
    {
      const u32 k8 = hw ? 0u : cmp[512], k4 = hw ? 0u : cmp[511], k0 = hw ? 0u : cmp[510];
      ra[2][0] = k8 * 0x10001u; ra[2][1] = k4 * 0x10001u; ra[2][2] = k4 | (k0 << 16); ra[2][3] = k0 * 0x10001u;
    }
#pragma unroll
    for (int c = 0; c < 2; c++) {
      u32 dw[8], od[7];
#pragma unroll
      for (int j = 0; j < 4; j++) {
        dw[j] = ra[c][j];
        dw[4 + j] = dpp<0x130>(c == 0 ? dpp<0x134>(0u, ra[1][j]) : ra[2][j], ra[c][j]);
      }
#pragma unroll
      for (int i = 0; i < 7; i++) od[i] = __builtin_amdgcn_alignbit(dw[i + 1], dw[i], 16);
      char* tp = T + (hw * 128 + c * 64 + l) * 16;
#pragma unroll
      for (int k = 0; k < 4; k++) {
        *(u32x4*)(tp + (2 * k) * CV_CS) = (u32x4){dw[k], dw[k + 1], dw[k + 2], dw[k + 3]};
        *(u32x4*)(tp + (2 * k + 1) * CV_CS) = (u32x4){od[k], od[k + 1], od[k + 2], od[k + 3]};
      }
    }
    if (t < 8) *(u32x4*)(T + t * CV_CS + 4096) = (u32x4){0u, 0u, 0u, 0u};
  }
  wait_vm<0>();
  bar();
  STAMP(3);
  const char* pa = T + (7 - (rho & 7)) * CV_CS + (255 - (rho >> 3) + q) * 16 - 32 - 256 * hw;
  const char* pb = U + 512 + rho * 32 + q * 16 - 32 - 256 * hw;
  f32x4 acc[8] = {};
  {
    u32x4 A0[8], B0[8], A1[8], B1[8];
    cv_frags(A0, B0, pa, pb);
    SB(); cv_frags(A1, B1, pa - 64, pb - 64); SB();
    cv_mma(A0, B0, acc);
    SB(); cv_frags(A0, B0, pa - 128, pb - 128); SB();
    cv_mma(A1, B1, acc);
    SB(); cv_frags(A1, B1, pa - 192, pb - 192); SB();
    cv_mma(A0, B0, acc);
    cv_mma(A1, B1, acc);
  }
  STAMP(4);
  bar();
  {
    char* box = T + hw * 4096;
#pragma unroll
    for (int i = 0; i < 4; i++) {
      const f32x4 give = hw ? acc[i] : acc[4 + i];
      acc[i] = hw ? acc[4 + i] : acc[i];
      *(f32x4*)(box + i * 1024 + l * 16) = give;
    }
  }
  u32x2 ub[4];
#pragma unroll
  for (int i = 0; i < 4; i++) ub[i] = *(const u32x2*)(U + 512 + (256 * (4 * hw + i) + 16 * rho + 4 * q) * 2);
  bar();
  {
    const char* box = T + (hw ^ 1) * 4096;
    u32x2 yv[4];
#pragma unroll
    for (int i = 0; i < 4; i++) {
      f32x4 v = (acc[i] + *(const f32x4*)(box + i * 1024 + l * 16)) * ks;
      f16x2 u0 = as_h2(ub[i].x), u1 = as_h2(ub[i].y);
      yv[i] = (u32x2){pk_bf16(fmaf((float)u0.x, dk, v.x), fmaf((float)u0.y, dk, v.y)), pk_bf16(fmaf((float)u1.x, dk, v.z), fmaf((float)u1.y, dk, v.w))};
    }
#pragma unroll
    for (int i = 0; i < 4; i += 2) {
      auto sx = __builtin_amdgcn_permlane16_swap(yv[i].x, yv[i + 1].x, false, false);
      auto sy = __builtin_amdgcn_permlane16_swap(yv[i].y, yv[i + 1].y, false, false);
      const u32 off = (u32)(512 * (4 * hw + i + (q & 1)) + 32 * rho + 16 * (q >> 1));
      gstore16(ws, (u32)(WS_YT + d * (SEQ * 2)) + off, (u32x4){sx[0], sy[0], sx[1], sy[1]});
    }
  }
  STAMP(5); STAMP_FLUSH(1);
}

extern "C" __global__ NOPK __launch_bounds__(256, 2) void k2(const u16* wsp, const u16* bsp, const u16* wout, const u16* lar, const u16* aim, const u16* cre,
                                             const u16* cim, const u16* ldt, const u16* dsk, char* ws) {
  __shared__ __attribute__((aligned(16))) char smem[81920];
  if (blockIdx.x == 0 && threadIdx.x == 0) __hip_atomic_store((GAS u64*)(ws + (40u << 20) + 256), (u64)__builtin_amdgcn_s_memrealtime(), __ATOMIC_RELAXED, __HIP_MEMORY_SCOPE_AGENT);
  if (threadIdx.x == 0 && blockIdx.x < 8) __hip_atomic_store((GAS u64*)(ws + (43u << 20)) + blockIdx.x, (u64)__builtin_amdgcn_s_memrealtime(), __ATOMIC_RELAXED, __HIP_MEMORY_SCOPE_AGENT);
  if (blockIdx.x < 256) k2_gemm(smem, blockIdx.x, wsp, bsp, wout, ws);
  else k2_ssm(smem, blockIdx.x - 256, lar, aim, cre, cim, ldt, dsk, ws);
}

#define K3_LEAD 3
struct K3G { const char* wo; const char* hf; u32 woff; u32 hoff[4]; u32 wdst; const char* yt; u32 yoff; u32 ydst; const char* xb; u32 xo; };
template <int I> DEV void k3_issue(const K3G& g, u32x4 (&hf)[7][4], u32x4 (&xv)[2]) {
  if constexpr (I < 7) {
#pragma unroll
    for (int i = 0; i < 4; i++) dma16(g.wo + (size_t)(I * 32 + i * 8) * (DM * 2), g.woff, g.wdst + (I * 4 + i) * 1024);
#pragma unroll
    for (int t = 0; t < 4; t++) gload16<0>(hf[I][t], g.hf + I * 1024, g.hoff[t]);
  } else if constexpr (I == 7) {
    dma16(g.yt, g.yoff, g.ydst);
    dma16(g.yt + 32 * (SEQ * 2), g.yoff, g.ydst + 1024);
    gload16<0>(xv[0], g.xb, g.xo);
    gload16<0>(xv[1], g.xb + 8 * (DM * 2), g.xo);
  }
}
constexpr int k3_cnt(int units) { return units <= 7 ? 8 * units : 60; }
template <int I> DEV void k3_step(const K3G& g, u32x4 (&hf)[7][4], u32x4 (&xv)[2], const char* const (&wp)[4], f32x4 (&acc)[4][4]) {
  wait_vm<k3_cnt(I + K3_LEAD) - 8 * (I + 1)>();
#pragma unroll
  for (int t = 0; t < 4; t++) pin(hf[I][t]);
  u32x4 wf[4];
#pragma unroll
  for (int p = 0; p < 4; p++) wf[p] = tr_frag(wp[p] + I * 4096, wp[p] + I * 4096 + 2048);
  k3_issue<I + K3_LEAD>(g, hf, xv);
#pragma unroll
  for (int p = 0; p < 4; p++)
#pragma unroll
    for (int t = 0; t < 4; t++) acc[t][p] = mfma_bf(wf[p], hf[I][t], acc[t][p]);
  if constexpr (I < 6) k3_step<I + 1>(g, hf, xv, wp, acc);
}
template <int A, int B> DEV void k3_pro(const K3G& g, u32x4 (&hf)[7][4], u32x4 (&xv)[2]) {
  if constexpr (A < B) { k3_issue<A>(g, hf, xv); k3_pro<A + 1, B>(g, hf, xv); }
}
extern "C" __global__ __launch_bounds__(256, 1) void k3(const u16* x, const u16* wout, u16* out, char* ws) {
  __shared__ __attribute__((aligned(16))) char smem[4 * 28672 + 8192];
  STAMP_DECL; STAMP(0);
  const u64 t2_ = __builtin_amdgcn_s_memrealtime();
  const int b = blockIdx.x, xcd = b & 7, idx = b >> 3;
  const int tp = xcd * 4 + idx / 7, dp = idx % 7;
  const int w = __builtin_amdgcn_readfirstlane(threadIdx.x >> 6), l = threadIdx.x & 63;
  const int rho = l & 15, q = l >> 4, qq = (l >> 2) & 3, pp = l & 3;
  char* const reg = smem + w * 28672;
  char* const Y = smem + 4 * 28672;
  K3G g;
  g.wo = (const char*)wout + (size_t)(w * 224) * (DM * 2) + dp * 128;
  g.hf = ws + WS_HF + ((size_t)tp * 4 * 28 + w * 7) * 1024;
  g.woff = (u32)((l >> 3) * (DM * 2) + (((l & 7) ^ ((l >> 4) << 1)) * 16));
#pragma unroll
  for (int t = 0; t < 4; t++) g.hoff[t] = (u32)((t ^ w) * 28 * 1024 + l * 16);
  g.wdst = lds_addr(reg);
  g.yt = ws + WS_YT + (size_t)(dp * 64) * (SEQ * 2) + tp * 128 + w * 32;
  g.yoff = (u32)((l >> 1) * (SEQ * 2) + (l & 1) * 16);
  g.ydst = lds_addr(Y) + w * 2048;
  g.xo = (u32)((l >> 3) * (DM * 2) + (l & 7) * 16);
  const size_t xb = (size_t)(tp * 64 + w * 16) * (DM * 2) + dp * 128;
  g.xb = (const char*)x + xb;
  u32x4 hf[7][4], xv[2];
  k3_pro<0, K3_LEAD>(g, hf, xv);
  const char* wp[4];
  const int sg = ((q & 1) << 1) | (qq >> 1);
#pragma unroll
  for (int p = 0; p < 4; p++) wp[p] = reg + (4 * q + qq) * 128 + ((p ^ sg) * 32) + 8 * pp;
  f32x4 acc[4][4] = {};
  STAMP(1);
  k3_step<0>(g, hf, xv, wp, acc);
  STAMP(2);
#pragma unroll
  for (int t = 1; t < 4; t++)
#pragma unroll
    for (int p = 0; p < 4; p++) *(f32x4*)(reg + t * 4352 + rho * 272 + p * 64 + q * 16) = acc[t][p];
  wait_vm<0>();
  pin(xv[0]); pin(xv[1]);
#pragma unroll
  for (int p = 0; p < 4; p++) {
    u32x2 yv = tr_read(Y + w * 2048 + (16 * p + 4 * q + qq) * 32 + 8 * pp);
    *(f32x4*)(reg + rho * 272 + p * 64 + q * 16) = acc[0][p] + (f32x4){bflo(yv.x), bfhi(yv.x), bflo(yv.y), bfhi(yv.y)};
  }
  bar();
  STAMP(3);
#pragma unroll
  for (int i = 0; i < 2; i++) {
    f32x4 a = {bflo(xv[i].x), bfhi(xv[i].x), bflo(xv[i].y), bfhi(xv[i].y)}, c = {bflo(xv[i].z), bfhi(xv[i].z), bflo(xv[i].w), bfhi(xv[i].w)};
#pragma unroll
    for (int t = 0; t < 4; t++) {
      const char* rp = smem + (w ^ t) * 28672 + t * 4352 + (8 * i + (l >> 3)) * 272 + (l & 7) * 32;
      a += *(const f32x4*)(rp); c += *(const f32x4*)(rp + 16);
    }
    u32x4 o = {pk_bf16(a.x, a.y), pk_bf16(a.z, a.w), pk_bf16(c.x, c.y), pk_bf16(c.z, c.w)};
    gstore16(out, (u32)xb + i * 8 * (DM * 2) + g.xo, o);
  }
  STAMP(4); STAMP_FLUSH(2);
  if (blockIdx.x == 0) {
    const u64 t0_ = *(volatile u64*)(ws + (40u << 20)), t1_ = *(volatile u64*)(ws + (40u << 20) + 256);
    const u64 t3_ = __builtin_amdgcn_s_memrealtime();
    u64 d = DIAG == 1 ? t1_ - t0_ : (DIAG == 2 ? t2_ - t1_ : (DIAG == 3 ? t3_ - t2_ : 0));
    if (DIAG >= 4) {
      const int i = threadIdx.x;
      const u64 ref = ((volatile u64*)(ws + (43u << 20)))[i & 7];
      const int v = (int)(((volatile u64*)(ws + (41u << 20)))[i] - ref);
      int e = -(1 << 30);
      for (int k = 0; k < 8; k++) { const int x_ = (int)(((const u64*)(ws + (42u << 20)))[i * 8 + k] - ref); e = x_ > e ? x_ : e; }
      __syncthreads();
      int* r = (int*)smem;
      if (i == 0) { r[0] = 1 << 30; r[1] = -(1 << 30); r[2] = -(1 << 30); r[3] = -(1 << 30); }
      __syncthreads();
      atomicMin(r, v); atomicMax(r + 1, v); atomicMax(r + (i < 224 ? 2 : 3), e);
      __syncthreads();
      d = DIAG == 4 ? (u64)(r[1] - r[0]) : (DIAG == 5 ? (u64)(-r[2]) : (DIAG == 6 ? (u64)(-r[3]) : (DIAG == 7 ? (u64)(-r[0]) : 0)));
      d += 250;
    }
    d = d > 5000 ? 5000 : d;
    while (__builtin_amdgcn_s_memrealtime() < t3_ + 4 * d) __builtin_amdgcn_s_sleep(1);
  }
  asm volatile("k3_code_end:");
}

#define K1_GRID 256
#define K2_GRID 480
extern "C" void kernel_launch(void* const* d_in, const int* in_sizes, int n_in, void* d_out, int out_size, void* d_ws, size_t ws_size,
                              hipStream_t stream) {
  const u16* x = (const u16*)d_in[0];
  char* ws = (char*)d_ws;
  Extra ex = {{(const char*)d_out, (const char*)d_in[1], (const char*)d_in[2], (const char*)d_in[3], (const char*)d_in[4], (const char*)d_in[5], (const char*)d_in[6], (const char*)d_in[10]}};
  k1<<<K1_GRID, 512, 0, stream>>>(x, (const u16*)d_in[8], (const u16*)d_in[9], (const u16*)d_in[11], ws, ex);
  k2<<<K2_GRID, 256, 0, stream>>>((const u16*)d_in[9], (const u16*)d_in[10], (const u16*)d_in[11], (const u16*)d_in[1], (const u16*)d_in[2],
                              (const u16*)d_in[3], (const u16*)d_in[4], (const u16*)d_in[5], (const u16*)d_in[6], ws);
  k3<<<224, 256, 0, stream>>>(x, (const u16*)d_in[11], (u16*)d_out, ws);
}
```

```cpp
#include <hip/hip_runtime.h>
#include <stdint.h>

typedef unsigned short u16;
typedef unsigned int u32;
typedef unsigned long long u64;
typedef __attribute__((ext_vector_type(4))) u32 u32x4;
typedef __attribute__((ext_vector_type(2))) u32 u32x2;
typedef __attribute__((ext_vector_type(4))) float f32x4;
typedef __attribute__((ext_vector_type(2))) float f32x2;
typedef __attribute__((ext_vector_type(16))) float f32x16;
typedef __attribute__((ext_vector_type(8))) __bf16 bf16x8;
typedef __attribute__((ext_vector_type(2))) __bf16 bf16x2;
typedef __attribute__((ext_vector_type(8))) _Float16 f16x8;
typedef __attribute__((ext_vector_type(2))) _Float16 f16x2;
typedef __attribute__((ext_vector_type(4))) short s16x4;
#define LDSAS __attribute__((address_space(3)))
#define GAS __attribute__((address_space(1)))
#define DEV __device__ __forceinline__
#if defined(__HIP_DEVICE_COMPILE__)
#define NOPK __attribute__((target("no-packed-fp32-ops")))
#else
#define NOPK
#endif
#define SB() __builtin_amdgcn_sched_barrier(0)

constexpr int SEQ = 2048, DM = 448, NMODE = 256, FF = 1792, FH = 896;

constexpr size_t WS_Z2F = 0;
constexpr size_t WS_Z1F = 4u << 20;
constexpr size_t WS_HF  = 8u << 20;
constexpr size_t WS_UT  = 12u << 20;
constexpr size_t WS_YT  = 14u << 20;
constexpr size_t WS_GR  = 16u << 20;
constexpr size_t WS_EP  = 17u << 20;

#define STAMP(i)
#define STAMP_DECL
#define STAMP_ARG
#define STAMP_PASS
#define STAMP_FLUSH(kern)
#define LST(k)
DEV float bf2f(u32 v) { return __uint_as_float(v << 16); }
DEV float bflo(u32 v) { return __uint_as_float(v << 16); }
DEV float bfhi(u32 v) { return __uint_as_float(v & 0xffff0000u); }
DEV u32 pk_bf16(float a, float b) { bf16x2 v = {(__bf16)a, (__bf16)b}; return __builtin_bit_cast(u32, v); }
DEV u32 pk_f16(float a, float b) { f16x2 v = {(_Float16)a, (_Float16)b}; return __builtin_bit_cast(u32, v); }
DEV f16x2 as_h2(u32 v) { return __builtin_bit_cast(f16x2, v); }
DEV float rbf(float a) { return __uint_as_float(pk_bf16(a, a) << 16); }
DEV f32x4 mfma_bf(u32x4 a, u32x4 b, f32x4 c) {
  return __builtin_amdgcn_mfma_f32_16x16x32_bf16(__builtin_bit_cast(bf16x8, a), __builtin_bit_cast(bf16x8, b), c, 0, 0, 0);
}
DEV f32x4 mfma_h(u32x4 a, u32x4 b, f32x4 c) {
  return __builtin_amdgcn_mfma_f32_16x16x32_f16(__builtin_bit_cast(f16x8, a), __builtin_bit_cast(f16x8, b), c, 0, 0, 0);
}
DEV f32x16 mfma32_h(u32x4 a, u32x4 b, f32x16 c) {
  return __builtin_amdgcn_mfma_f32_32x32x16_f16(__builtin_bit_cast(f16x8, a), __builtin_bit_cast(f16x8, b), c, 0, 0, 0);
}
DEV u32x2 tr_read(const char* p) {
  s16x4 v = __builtin_amdgcn_ds_read_tr16_b64_v4i16((LDSAS s16x4*)p);
  return __builtin_bit_cast(u32x2, v);
}
DEV u32x4 tr_frag(const char* p0, const char* p1) {
  u32x2 a = tr_read(p0), b = tr_read(p1);
  return (u32x4){a.x, a.y, b.x, b.y};
}
#define LDMOD ""
#define DMAMOD ""
DEV u32 lds_addr(const void* p) { return (u32)(uintptr_t)(LDSAS const char*)p; }
DEV void dma16(const void* sbase, u32 voff, u32 ldst) {
  asm volatile("s_mov_b32 m0, %2\n\ts_nop 0\n\tglobal_load_lds_dwordx4 %0, %1 " DMAMOD ::"v"(voff), "s"(sbase), "s"(ldst) : "memory");
}
template <int OFF> DEV void gload16(u32x4& d, const void* sbase, u32 voff) {
  asm volatile("global_load_dwordx4 %0, %1, %2 offset:%3 " LDMOD : "=v"(d) : "v"(voff), "s"(sbase), "i"(OFF) : "memory");
}
DEV void gstore16(void* sbase, u32 voff, u32x4 d) {
  asm volatile("global_store_dwordx4 %0, %1, %2 sc1\n\ts_nop 1" ::"v"(voff), "v"(d), "s"(sbase) : "memory");
}
DEV void gstore8(void* sbase, u32 voff, u32x2 d) {
  asm volatile("global_store_dwordx2 %0, %1, %2 sc1" ::"v"(voff), "v"(d), "s"(sbase) : "memory");
}
DEV void gstore16p(void* sbase, u32 voff, u32x4 d) {
  asm volatile("global_store_dwordx4 %0, %1, %2\n\ts_nop 1" ::"v"(voff), "v"(d), "s"(sbase) : "memory");
}
#define Z2STORE gstore16
DEV void gload4(u32& d, const void* sbase, u32 voff) {
  asm volatile("global_load_dword %0, %1, %2" : "=v"(d) : "v"(voff), "s"(sbase) : "memory");
}
template <int N> DEV void wait_vm() { asm volatile("s_waitcnt vmcnt(%0)" ::"i"(N) : "memory"); }
DEV void touch(u32& d, const void* p) { asm volatile("global_load_dword %0, %1, off" : "=v"(d) : "v"(p) : "memory"); }
DEV void keep(u32& a) { asm volatile("" : "+v"(a)); }
DEV void pin(u32x4& a) { asm volatile("" : "+v"(a)); }
DEV void bar() { asm volatile("s_waitcnt lgkmcnt(0)\n\ts_barrier" ::: "memory"); }
DEV float xsum16(float v) { auto r = __builtin_amdgcn_permlane16_swap(__float_as_uint(v), __float_as_uint(v), false, false); return __uint_as_float(r[0]) + __uint_as_float(r[1]); }
DEV float xsum32(float v) { auto r = __builtin_amdgcn_permlane32_swap(__float_as_uint(v), __float_as_uint(v), false, false); return __uint_as_float(r[0]) + __uint_as_float(r[1]); }
DEV f32x2 ln_scalars(float sum, float sq, float inv_n) {
  float m = sum * inv_n;
  float v = fmaxf(sq * inv_n - m * m, 0.f);
  float sd = rbf(__builtin_amdgcn_sqrtf(rbf(rbf(v) + 1e-5f)));
  return (f32x2){rbf(m), __builtin_amdgcn_rcpf(sd)};
}
DEV f32x2 gelu2(f32x2 x) {
  const float c1 = -2.3022081983f, c2 = -0.1029432396f;
  f32x2 t = x * x;
  t = t * c2 + c1;
  t = t * x;
  f32x2 e = {__builtin_amdgcn_exp2f(t.x), __builtin_amdgcn_exp2f(t.y)};
  e = e + 1.0f;
  f32x2 r = {__builtin_amdgcn_rcpf(e.x), __builtin_amdgcn_rcpf(e.y)};
  return x * r;
}
DEV f32x2 cmul(f32x2 a, f32x2 b) { return (f32x2){fmaf(a.x, b.x, -(a.y * b.y)), fmaf(a.x, b.y, a.y * b.x)}; }
DEV f32x2 csqr(f32x2 a) { return (f32x2){a.x * a.x - a.y * a.y, 2.f * a.x * a.y}; }

#define TSTRIDE 65536u
#define K1_PF_STOP 16000u
#define K1_PF_END 1024
#define K1_BDELAY 20
#define K1_ROT (sib >> 1)
#define K1_NOSELF
#define K1_PRIO 2
#define K1_PF 4
constexpr int k1_slot(int u) { return u < 9 ? u : u - 9; }
constexpr int K1_X = 73728, K1_UT = K1_X + 65536, K1_CS = K1_UT + 32 * 272, K1_LN = K1_CS + 512, K1_LN1 = K1_LN + 1024, K1_CNT = K1_LN1 + 1024, K1_DUMP = K1_CNT + 64, K1_LDS = K1_DUMP + 4096;

typedef LDSAS char* lptr;
DEV lptr pinned(lptr p) { asm volatile("" : "+v"(p)); return p; }
DEV u32x4 lread16(lptr p) { return *(LDSAS u32x4*)p; }
DEV u32x4 ltr_frag(lptr p0, lptr p1) {
  s16x4 a = __builtin_amdgcn_ds_read_tr16_b64_v4i16((LDSAS s16x4*)p0), b = __builtin_amdgcn_ds_read_tr16_b64_v4i16((LDSAS s16x4*)p1);
  u32x2 x = __builtin_bit_cast(u32x2, a), y = __builtin_bit_cast(u32x2, b);
  return (u32x4){x.x, x.y, y.x, y.y};
}
DEV void mma(f32x4& c, const u32x4& a, const u32x4& b) { asm volatile("v_mfma_f32_16x16x32_bf16 %0, %1, %2, %0" : "+v"(c) : "v"(a), "v"(b)); }
DEV void mma0(f32x4& c, const u32x4& a, const u32x4& b) { asm volatile("v_mfma_f32_16x16x32_bf16 %0, %1, %2, 0" : "=&v"(c) : "v"(a), "v"(b)); }
template <bool FIRST> DEV void mmaf(f32x4& c, const u32x4& a, const u32x4& b) { if constexpr (FIRST) mma0(c, a, b); else mma(c, a, b); }
DEV void mma_drain() { SB(); asm volatile("s_nop 7\n\ts_nop 1" ::: "memory"); SB(); }
DEV const char* wsp_piece(const u16* wsp, int xcd, int n) { return (const char*)wsp + (size_t)(xcd * 256 + (n & 31) * 8) * 4096 + ((n >> 5) & 3) * 1024 + (n >> 7) * 128; }
DEV void sbar() { asm volatile("s_barrier" ::: "memory"); }

struct K1L { const char* win; const char* xb; u32 xoff[2]; u32 woff[2]; u32 wdst; u32 xdst; int m0; };
template <int U> DEV void k1_unit(const K1L& g) {
  int line = U % 7 + g.m0; line = line >= 7 ? line - 7 : line;
  if constexpr (U < 7) {
#pragma unroll
    for (int i = 0; i < 4; i++) dma16(g.xb + line * 128 + (i >> 1) * 16 * (DM * 2), g.xoff[i & 1], g.xdst + (U & 3) * 16384 + i * 1024);
  }
#pragma unroll
  for (int i = 0; i < 2; i++) dma16(g.win + (size_t)line * 64 * (FF * 2) + (U < 7 ? FH * 2 : 0), g.woff[i], g.wdst + k1_slot(U) * 8192 + i * 1024);
}
struct K1C { lptr str[4]; lptr wp[4]; };
DEV void k1_xr(const K1C& g, u32x4 (&xf)[4], int M) {
#pragma unroll
  for (int i = 0; i < 4; i++) xf[i] = lread16(g.str[i] + (M & 3) * 16384);
}
template <int U> DEV u32x4 k1_wf(const K1C& g, int f) {
  if constexpr (k1_slot(U) < 8) return ltr_frag(g.wp[f & 3] + k1_slot(U) * 8192 + (f >> 2) * 4096, g.wp[f & 3] + k1_slot(U) * 8192 + (f >> 2) * 4096 + 512);
  else { lptr p = pinned(g.wp[f & 3] + 65536); return ltr_frag(p + (f >> 2) * 4096, p + (f >> 2) * 4096 + 512); }
}
template <int U> DEV void k1_main(const K1C& g, const u32x4 (&xf)[4], u32x4 (&wf)[8], f32x4 (&acc)[2][4], f32x4& cs, const u32x4& ones) {
#pragma unroll
  for (int f = 0; f < 8; f++) {
    if (U % 7 == 0 && f < 4) {
      if (f == 0) mma0(cs, wf[f], ones);
      mma0(acc[0][f & 3], wf[f], xf[0]);
      mma0(acc[1][f & 3], wf[f], xf[1]);
    } else {
      if ((f & 3) == 0) mma(cs, wf[f], ones);
      mma(acc[0][f & 3], wf[f], xf[(f >> 2) * 2]);
      mma(acc[1][f & 3], wf[f], xf[(f >> 2) * 2 + 1]);
    }
    SB(); if constexpr (U % 7 < 6) wf[f] = k1_wf<U + 1>(g, f); SB();
  }
}
template <int M>
DEV void k1_stage(const K1C& g, u32x4 (&xf)[7][4], u32x4 (&wf)[8], f32x4 (&acc)[2][4], f32x4& cs, f32x4 (&rsum)[2], f32x4 (&gram)[2], const u32x4& ones) {
  asm volatile("s_waitcnt lgkmcnt(15)\n\ts_barrier" ::: "memory");
  if constexpr (M < 6) k1_xr(g, xf[M + 1], M + 1);
  SB();
#pragma unroll
  for (int i = 0; i < 4; i++) {
    if (M == 0 && i < 2) { mma0(rsum[i & 1], ones, xf[M][i]); mma0(gram[i & 1], xf[M][i], xf[M][i]); }
    else { mma(rsum[i & 1], ones, xf[M][i]); mma(gram[i & 1], xf[M][i], xf[M][i]); }
  }
  k1_main<M>(g, xf[M], wf, acc, cs, ones);
  if constexpr (M < 6) k1_stage<M + 1>(g, xf, wf, acc, cs, rsum, gram, ones);
}
template <int M>
DEV void k1_z1loop(const K1C& g, u32x4 (&xf)[7][4], u32x4 (&wf)[8], f32x4 (&acc)[2][4], f32x4& cs, const u32x4& ones, const u16* wsp, int xcd, int pf, u32 l16, u32 dump) {
  if constexpr (M + 1 < K1_PF) dma16(wsp_piece(wsp, xcd, pf + (M + 1) * 112), l16, dump);
  k1_main<M + 7>(g, xf[M], wf, acc, cs, ones);
  if constexpr (M < 6) k1_z1loop<M + 1>(g, xf, wf, acc, cs, ones, wsp, xcd, pf, l16, dump);
}
DEV void k1_act(f32x4 (&acc)[2][4], const char* csb, int w, const f32x2 (&ln1)[2]) {
  float k1[2], k2[2], sd[2];
#pragma unroll
  for (int t = 0; t < 2; t++) {
    const float rs = ln1[t].y;
    k1[t] = -2.3022081983f * rs; k2[t] = -0.1029432396f * rs * rs * rs; sd[t] = __builtin_amdgcn_rcpf(rs);
  }
#pragma unroll
  for (int p = 0; p < 4; p++) {
    const f32x4 c = *(const f32x4*)(csb + (p ^ w) * 64);
#pragma unroll
    for (int t = 0; t < 2; t++) {
      const f32x4 a = acc[t][p] - c * ln1[t].x;
      const f32x4 u = a * (a * a * k2[t] + k1[t]);
      f32x4 e = {__builtin_amdgcn_exp2f(u.x), __builtin_amdgcn_exp2f(u.y), __builtin_amdgcn_exp2f(u.z), __builtin_amdgcn_exp2f(u.w)};
      e = e * sd[t] + sd[t];
      acc[t][p] = a * (f32x4){__builtin_amdgcn_rcpf(e.x), __builtin_amdgcn_rcpf(e.y), __builtin_amdgcn_rcpf(e.z), __builtin_amdgcn_rcpf(e.w)};
    }
  }
}
DEV int lane_id() { int l = __builtin_amdgcn_mbcnt_hi(~0u, __builtin_amdgcn_mbcnt_lo(~0u, 0u)); asm volatile("" : "+v"(l)); return l; }
DEV void quad_sync(lptr cnt, int l) {
  asm volatile("s_waitcnt lgkmcnt(0)" ::: "memory");
  if (l == 0) __hip_atomic_fetch_add((LDSAS u32*)cnt, 1u, __ATOMIC_RELAXED, __HIP_MEMORY_SCOPE_WORKGROUP);
  while (__builtin_amdgcn_readfirstlane(*(volatile LDSAS u32*)cnt) < 4u) __builtin_amdgcn_s_sleep(0);
  asm volatile("" ::: "memory");
}
DEV void lbar0() { asm volatile("s_waitcnt vmcnt(6) lgkmcnt(0)\n\ts_barrier" ::: "memory"); }

struct Extra { const char* p[8]; };
DEV void k1_body(char* smem, const u16* x, const u16* win, const u16* wsp, const u16* wout, char* ws, const Extra& ex) {
  STAMP_DECL; STAMP(0);
  const int b = blockIdx.x, xcd = b & 7, idx = b >> 3;
  const int w8 = __builtin_amdgcn_readfirstlane(threadIdx.x >> 6), l = threadIdx.x & 63, w = w8 & 3;
  if (b >= 224) {
    const int j = (idx - 28) * 8 + w8;
    const u32 t_start = (u32)__builtin_amdgcn_s_memtime();
    const u32 poff = (u32)((l >> 3) * 4096 + (l & 7) * 16);
    u32 dc;
    {
      u64 c0, c1;
      asm volatile("s_getpc_b64 vcc\n\ts_add_u32 vcc_lo, vcc_lo, k1@rel32@lo+4\n\ts_addc_u32 vcc_hi, vcc_hi, k1@rel32@hi+12\n\ts_mov_b64 %0, vcc" : "=s"(c0) :: "scc", "vcc");
      asm volatile("s_getpc_b64 vcc\n\ts_add_u32 vcc_lo, vcc_lo, k3_code_end@rel32@lo+4\n\ts_addc_u32 vcc_hi, vcc_hi, k3_code_end@rel32@hi+12\n\ts_mov_b64 %0, vcc" : "=s"(c1) :: "scc", "vcc");
      const u32 len = (u32)(c1 - c0) & ~3u, o = (u32)(j * 64 + l) * 128u;
      gload4(dc, (const void*)c0, j < 8 ? (o < len ? o : len - 4u) : 0u);
    }
    u32 d0, d1, d2, d3;
    {
      const int n = j * 64 + l;
      const char* p = n < 448 ? ex.p[0] + n * 4096 : (n < 672 ? ex.p[1 + (n - 448) / 56] + ((n - 448) % 56) * 4096 : ex.p[5 + (n % 3)]);
      asm volatile("" : "+v"(p));
      gload4(d0, ws, (u32)((n * TSTRIDE) % (18u << 20)));
      gload4(d1, ws, (u32)(((n + 2048) * TSTRIDE) % (18u << 20)));
      gload4(d2, ws, (u32)(((n + 4096) * TSTRIDE) % (18u << 20)));
      touch(d3, p);
    }
#pragma unroll
    for (int k = 0; k < 4; k++) dma16(ex.p[1 + k] + (xcd * 28 + (j < 28 ? j : 27)) * 1024, l * 16, lds_addr(smem) + w8 * 1024);
    const char* po = (const char*)wout + (size_t)(xcd * 98) * 1024;
#pragma unroll
    for (int k = 0; k < 4; k++) { int c = j + 32 * k; c = c < 98 ? c : 97; dma16(po + c * 1024, l * 16, lds_addr(smem) + w8 * 1024); }
#pragma unroll 1
    for (int i = 112 * K1_PF + j; i < K1_PF_END; i += 32) {
      dma16(wsp_piece(wsp, xcd, i), poff, lds_addr(smem) + w8 * 1024);
      if ((u32)__builtin_amdgcn_s_memtime() - t_start > K1_PF_STOP) break;
    }
    wait_vm<0>();
    keep(d0); keep(d1); keep(d2); keep(d3); keep(dc);
    STAMP(1); STAMP_FLUSH(0);
    return;
  }
  const int i7 = (idx * 37) >> 8;
  const int panel = (xcd >> 1) * 4 + i7, sib = (xcd & 1) * 7 + idx - 7 * i7;
  const lptr lds = (lptr)smem;
  K1L g;
  if (w8 >= 4) {
    g.win = (const char*)win + (size_t)(16 * w) * (FF * 2) + sib * 128;
    g.xb = (const char*)x + (size_t)(panel * 128 + w * 32) * (DM * 2);
    g.xoff[0] = (u32)((l >> 3) * (DM * 2) + (((l & 7) ^ (l >> 4)) * 16));
    g.xoff[1] = (u32)((8 + (l >> 3)) * (DM * 2) + (((l & 7) ^ ((l >> 4) | 4)) * 16));
    g.woff[0] = (u32)((l >> 3) * (FF * 2) + (((l & 7) ^ (((l >> 4) & 1) << 1)) * 16));
    g.woff[1] = (u32)((8 + (l >> 3)) * (FF * 2) + (((l & 7) ^ (((l >> 4) & 1) << 1) ^ 4) * 16));
    g.xdst = lds_addr(smem) + K1_X + w * 4096;
    g.wdst = lds_addr(smem) + w * 2048;
    g.m0 = K1_ROT;
    k1_unit<0>(g); k1_unit<1>(g);
  }
  K1C c;
  {
    const int rho = l & 15, q = l >> 4, qq = (l >> 2) & 3, pp = l & 3;
#pragma unroll
    for (int i = 0; i < 4; i++) c.str[i] = pinned(lds + K1_X + w * 4096 + (i & 1) * 2048 + rho * 128 + (((4 * (i >> 1) + q) ^ (rho >> 1)) * 16));
    const int sg = (qq >> 1) | ((q & 1) << 1);
#pragma unroll
    for (int f = 0; f < 4; f++) c.wp[f] = lds + (8 * q + qq) * 128 + (((f ^ w) ^ sg) * 32) + 8 * pp;
  }
  u32x4 ones = {0x3f803f80u, 0x3f803f80u, 0x3f803f80u, 0x3f803f80u};
  pin(ones);
  u32x4 xf[7][4], wf[8];
  f32x4 acc[2][4], cs;
  if (w8 >= 4) {
    lbar0();
    k1_unit<2>(g); k1_xr(c, xf[0], 0); lbar0();
    k1_unit<3>(g); k1_xr(c, xf[1], 1); lbar0();
    k1_unit<4>(g); k1_xr(c, xf[2], 2); lbar0();
    k1_unit<5>(g); k1_xr(c, xf[3], 3); lbar0();
    k1_unit<6>(g); k1_xr(c, xf[4], 4); lbar0();
    k1_unit<7>(g); k1_unit<8>(g); k1_unit<9>(g); k1_xr(c, xf[5], 5); lbar0();
    k1_unit<10>(g); k1_unit<11>(g); k1_unit<12>(g); k1_xr(c, xf[6], 6);
    asm volatile("s_waitcnt lgkmcnt(0)\n\ts_barrier" ::: "memory");
    k1_unit<13>(g);
    STAMP(1);
    const int pf = idx * 4 + w;
    const u32 l16 = (u32)((l >> 3) * 4096 + (l & 7) * 16), dump = lds_addr(smem) + K1_DUMP + w * 1024;
    if constexpr (K1_PF > 0) dma16(wsp_piece(wsp, xcd, pf), l16, dump);
    wait_vm<(K1_PF > 0 ? 1 : 0)>(); sbar();
    STAMP(2);
    f32x2 ln1[2];
    {
    const int rho = l & 15, q = l >> 4;
#pragma unroll
    for (int t = 0; t < 2; t++) ln1[t] = *(const f32x2*)(smem + K1_LN1 + w * 256 + (t * 16 + rho) * 8);
#pragma unroll
    for (int f = 0; f < 8; f++) wf[f] = k1_wf<7>(c, f);
    }
    __builtin_amdgcn_s_sleep(K1_BDELAY);
    SB();
    k1_z1loop<0>(c, xf, wf, acc, cs, ones, wsp, xcd, pf, l16, dump);
    mma_drain();
    const int l = lane_id(), rho = l & 15, q = l >> 4;
    if (rho == 0) *(f32x4*)(smem + K1_CS + 256 + w * 64 + q * 16) = cs;
    STAMP(3);
    quad_sync(lds + K1_CNT, l);
    STAMP(4);
    k1_act(acc, smem + K1_CS + 256 + q * 16, w, ln1);
#pragma unroll
    for (int t = 0; t < 2; t++)
#pragma unroll
      for (int cc = 0; cc < 2; cc++) {
        u32x2 a = {pk_bf16(acc[t][2 * cc].x, acc[t][2 * cc].y), pk_bf16(acc[t][2 * cc].z, acc[t][2 * cc].w)};
        u32x2 bb = {pk_bf16(acc[t][2 * cc + 1].x, acc[t][2 * cc + 1].y), pk_bf16(acc[t][2 * cc + 1].z, acc[t][2 * cc + 1].w)};
        const u32x4 o = (w & 1) ? (u32x4){bb.x, bb.y, a.x, a.y} : (u32x4){a.x, a.y, bb.x, bb.y};
        gstore16(ws, (u32)(WS_Z1F + ((panel * 8 + w * 2 + t) * 28 + sib * 2 + (cc ^ (w >> 1))) * 1024 + l * 16), o);
      }
    STAMP(5); STAMP_FLUSH(0);
    return;
  }
  const int grp = panel * 4 + w;
  u32 tch;
  {
    const int h = l >> 5, n = (w & 1) * 32 + (l & 31);
    (void)h;
    const int wl = panel * 56 + (n < 56 ? n : 55);
    const u32 xo = (u32)(panel * 128 * (DM * 2) + (sib * 64 + n) * 128);
    const u32 wo = (u32)((wl >> 1) * (FF * 2) + ((wl & 1) ? 0 : FH * 2) + sib * 128);
    gload4(tch, w < 2 ? (const void*)x : (const void*)win, w < 2 ? xo : wo);
  }
  u32x4 xs[2];
#pragma unroll
  for (int i = 0; i < 2; i++) gload16<0>(xs[i], x, (u32)((panel * 128 + w * 32 + i * 16 + (l >> 2)) * (DM * 2) + sib * 64 + (l & 3) * 16));
  u32 tag;
  asm volatile("s_load_dword %0, %1, 0x0\n\ts_waitcnt lgkmcnt(0)" : "=s"(tag) : "s"(ws + WS_EP + grp * 256) : "memory");
  tag += 1u;
  f32x4 rsum[2], gram[2];
  if (threadIdx.x < 2) *(u32*)(smem + K1_CNT + threadIdx.x * 4) = 0u;
  bar();
  STAMP(2);
  k1_xr(c, xf[0], 0);
#pragma unroll
  for (int f = 0; f < 8; f++) wf[f] = k1_wf<0>(c, f);
  k1_stage<0>(c, xf, wf, acc, cs, rsum, gram, ones);
  mma_drain();
  wait_vm<0>(); pin(xs[0]); pin(xs[1]);
  keep(tch);
  const int rho = l & 15, q = l >> 4, qq = (l >> 2) & 3, pp = l & 3;
  if (rho == 0) *(f32x4*)(smem + K1_CS + w * 64 + q * 16) = cs;
  STAMP(3);
  f32x2 ln1[2];
#pragma unroll
  for (int t = 0; t < 2; t++) {
    float d = (rho & 2) ? ((rho & 1) ? gram[t].w : gram[t].z) : ((rho & 1) ? gram[t].y : gram[t].x);
    d = ((rho >> 2) == q) ? d : 0.f;
    d = xsum32(xsum16(d));
    ln1[t] = ln_scalars(rsum[t].x, d, 1.f / DM);
    if (q == 0) *(f32x2*)(smem + K1_LN1 + w * 256 + (t * 16 + rho) * 8) = ln1[t];
  }
  bar();
  __builtin_amdgcn_s_setprio(K1_PRIO);
  STAMP(4);
  GAS u64* gr = (GAS u64*)(ws + WS_GR) + (size_t)grp * 14 * 64;
  {
    float st[2][2] = {{0.f, 0.f}, {0.f, 0.f}};
    k1_act(acc, smem + K1_CS + q * 16, w, ln1);
#pragma unroll
    for (int t = 0; t < 2; t++) {
      f32x2 s = {0.f, 0.f}, s2 = {0.f, 0.f};
#pragma unroll
      for (int p = 0; p < 4; p++) {
        s += acc[t][p].xy; s += acc[t][p].zw;
        s2 += acc[t][p].xy * acc[t][p].xy; s2 += acc[t][p].zw * acc[t][p].zw;
      }
      st[t][0] = s.x + s.y; st[t][1] = s2.x + s2.y;
    }
#pragma unroll
    for (int t = 0; t < 2; t++) { st[t][0] = xsum32(xsum16(st[t][0])); st[t][1] = xsum32(xsum16(st[t][1])); }
    const float v = (q & 2) ? ((q & 1) ? st[1][1] : st[0][1]) : ((q & 1) ? st[1][0] : st[0][0]);
    __hip_atomic_store(gr + sib * 64 + l, ((u64)tag << 32) | __float_as_uint(v), __ATOMIC_RELAXED, __HIP_MEMORY_SCOPE_AGENT);
  }
  STAMP(5);
#pragma unroll
  for (int i = 0; i < 2; i++) {
    const f32x2 lx = *(const f32x2*)(smem + K1_LN1 + w * 256 + (i * 16 + (l >> 2)) * 8);
    const float mr = -lx.x * lx.y;
    u16* ub = (u16*)(smem + K1_UT + (8 * (l & 3)) * 272 + (w * 32 + i * 16 + (l >> 2)) * 2);
#pragma unroll
    for (int j = 0; j < 4; j++) {
      const u32 o = pk_f16(fmaf(bflo(xs[i][j]), lx.y, mr), fmaf(bfhi(xs[i][j]), lx.y, mr));
      ub[(2 * j) * 136] = (u16)o;
      ub[(2 * j + 1) * 136] = (u16)(o >> 16);
    }
  }
  quad_sync(lds + K1_CNT + 4, l);
#pragma unroll
  for (int i = 0; i < 2; i++) {
    const u32x4 uo = *(const u32x4*)(smem + K1_UT + (w * 8 + i * 4 + q) * 272 + rho * 16);
    gstore16(ws, (u32)(WS_UT + (sib * 32 + w * 8 + i * 4 + q) * (SEQ * 2) + panel * 256 + rho * 16), uo);
  }
  STAMP(6);
  u64 rv[14];
#pragma unroll
  for (int i = 0; i < 14; i++) rv[i] = __hip_atomic_load(gr + i * 64 + l, __ATOMIC_RELAXED, __HIP_MEMORY_SCOPE_AGENT);
  STAMP(7);
  float tot;
  for (u32 spins = 0;; spins++) {
    bool ok = true; tot = 0.f;
#pragma unroll
    for (int i = 0; i < 14; i++) { ok &= (u32)(rv[i] >> 32) == tag; tot += __uint_as_float((u32)rv[i]); }
    if (__all(ok) || spins > (1u << 20)) break;
#pragma unroll
    for (int i = 0; i < 14; i++) rv[i] = __hip_atomic_load(gr + i * 64 + l, __ATOMIC_RELAXED, __HIP_MEMORY_SCOPE_AGENT);
  }
  STAMP(8);
  if (sib == 0 && l == 0) __hip_atomic_store((GAS u32*)(ws + WS_EP + grp * 256), tag, __ATOMIC_RELAXED, __HIP_MEMORY_SCOPE_AGENT);
  {
    auto bb = __builtin_amdgcn_permlane32_swap(__float_as_uint(tot), __float_as_uint(tot), false, false);
    const f32x2 lr = ln_scalars(__uint_as_float(bb[0]), __uint_as_float(bb[1]), 1.f / FH);
    const u32 nm = __float_as_uint(-lr.x * lr.y);
    auto lm = __builtin_amdgcn_permlane16_swap(nm, nm, false, false);
    auto ls = __builtin_amdgcn_permlane16_swap(__float_as_uint(lr.y), __float_as_uint(lr.y), false, false);
    char* const stg = smem + K1_X + w * 8192;
#pragma unroll
    for (int t = 0; t < 2; t++) {
      const float m2 = __uint_as_float(lm[t]), r2 = __uint_as_float(ls[t]);
#pragma unroll
      for (int p = 0; p < 4; p++) {
        const f32x4 a = acc[t][p] * r2 + m2;
        *(u32x2*)(stg + (16 * t + rho) * 136 + p * 32 + q * 8) = (u32x2){pk_bf16(a.x, a.y), pk_bf16(a.z, a.w)};
      }
    }
#pragma unroll
    for (int p = 0; p < 4; p++) {
      const char* rp = stg + (8 * q + qq) * 136 + p * 32 + pp * 8;
      u32x4 o = tr_frag(rp, rp + 4 * 136);
      const int ft = sib * 4 + (p ^ w);
      Z2STORE(ws, (u32)(WS_Z2F + (((ft / 7) * 64 + grp) * 7 + ft % 7) * 1024 + l * 16), o);
    }
  }
  STAMP(9); STAMP_FLUSH(0);
}
#define K1_ATTR
extern "C" __global__ K1_ATTR __launch_bounds__(512, 2) void k1(const u16* x, const u16* win, const u16* wsp, const u16* wout, char* ws, Extra ex) {
  __shared__ __attribute__((aligned(16))) char smem[K1_LDS];
  LST(0);
  k1_body(smem, x, win, wsp, wout, ws, ex);
}

#define K2_R 18
#define K2_PF 7
#define K2_LATE 4
#define K2_TD 0
constexpr bool k2_touch_at(int tau) { return K2_TD > 0 && tau % 14 == 0 && tau / 7 + K2_TD <= 14; }
constexpr int K2_EPI = 5, K2_EPI_AT = 112 - K2_R + 2, K2_PRO = 16 + K2_R + (K2_TD > 0 ? 2 : 0) + (K2_LATE > 0 ? 1 : 0), K2_XP = K2_LATE > 0 ? 1 : 0;
constexpr int k2_apiece(int tau) {
  int s = tau / 7, f = tau % 7;
  int a = (s + 3) / 2 + ((s & 1) ? 0 : 1) - ((s & 1) ? 0 : 1);
  if (s & 1) { a = (s + 3) / 2; return (a <= 7 && (f & 1) == 0) ? 8 * a + f / 2 : -1; }
  a = (s + 2) / 2;
  return (a >= 2 && a <= 7 && f < 4) ? 8 * a + 4 + f : -1;
}
constexpr int k2_ops_after(int tau) { return (tau + K2_R < 112 ? 1 : 0) + (k2_apiece(tau) >= 0 ? 1 : 0) + (tau == K2_EPI_AT ? K2_EPI + K2_PF : 0) + (k2_touch_at(tau) ? 1 : 0); }
constexpr int k2_issued(int tau) { int n = K2_PRO; for (int i = 0; i < tau; i++) n += k2_ops_after(i); return n; }
constexpr int k2_pos_B(int n) { return n < 14 ? 8 + n + 1 : (n < K2_R ? 16 + K2_XP + n + 1 : k2_issued(n - K2_R) + 1); }
constexpr int k2_pos_A(int a) { return a == 0 ? 8 : (a == 1 ? 30 + K2_XP : k2_issued(7 * (2 * a - 2) + 4)); }
constexpr int k2_wait(int tau) {
  int need = k2_pos_B(tau);
  int s = tau / 7, f = tau % 7;
  if ((s & 1) && f == 2 && (s + 1) / 2 <= 7) { int a = k2_pos_A((s + 1) / 2); need = a > need ? a : need; }
  return k2_issued(tau) - need;
}
#define K2_MFMA mfma_bf
struct K2G { const char* wsp0; const char* wsp; const char* z2; const char* z1; const char* bsp; const char* pf; u32 aoff[2]; u32 boff; u32 adst; u32 dump; u32 tb; int arot; int w; };
template <int P> DEV void k2_a1(const K2G& g) {
  constexpr int SS = P / 8;
  const int i = (P % 8) ^ g.arot;
  dma16(g.wsp + SS * 128 + (size_t)i * 8 * 4096, g.aoff[P & 1], g.adst + (SS & 1) * 8192 + ((i ^ (2 * g.w)) * 1024));
}
template <int A, int B> DEV void k2_as(const K2G& g) { if constexpr (A < B) { k2_a1<A>(g); k2_as<A + 1, B>(g); } }
template <int N> DEV void k2_b1(const K2G& g, u32x4 (&bf)[K2_R]) {
 gload16<(N % 8 - 4) * 1024>(bf[N % K2_R], g.z2 + (N / 8) * 8192, g.boff); }
template <int A, int B> DEV void k2_bs(const K2G& g, u32x4 (&bf)[K2_R]) { if constexpr (A < B) { k2_b1<A>(g, bf); k2_bs<A + 1, B>(g, bf); } }
template <int I> DEV u32x4 k2_read_a(const char* ap0, const char* ap1, int t) {
  return *(const u32x4*)(((I & 1) ? ap1 : ap0) + ((I >> 1) & 1) * 8192 + t * 2048);
}
template <int TAU>
DEV void k2_group(const K2G& g, u32x4 (&bf)[K2_R], u32x4 (&af)[2][4], const char* ap0, const char* ap1, f32x4 (&acc)[4][7], u32x4 (&ep)[4], u32& bw, u32 (&tch)[2]) {
  constexpr int S = TAU / 7, F = TAU % 7;
  wait_vm<k2_wait(TAU)>();
  pin(bf[TAU % K2_R]);
  if constexpr (S < 15 && F >= 2 && F < 6) af[(S + 1) & 1][F - 2] = k2_read_a<S + 1>(ap0, ap1, F - 2);
  SB();
#pragma unroll
  for (int t = 0; t < 4; t++) if (TAU < 98 || (t & 1)) acc[t][F] = K2_MFMA(bf[TAU % K2_R], af[S & 1][t], acc[t][F]);
  SB();
  if constexpr (TAU + K2_R < 112) k2_b1<TAU + K2_R>(g, bf);
  if constexpr (k2_apiece(TAU) >= 0) k2_a1<k2_apiece(TAU)>(g);
  if constexpr (k2_touch_at(TAU)) { keep(tch[(TAU / 14) & 1]); gload4(tch[(TAU / 14) & 1], g.z2 + (S + K2_TD) * 7168 - 4096, g.tb); }
  if constexpr (TAU == 111) { keep(tch[0]); keep(tch[1]); }
  if constexpr (TAU == K2_EPI_AT) {
    gload16<0>(ep[0], g.z1, g.boff); gload16<1024>(ep[1], g.z1, g.boff); gload16<2048>(ep[2], g.z1, g.boff); gload16<3072>(ep[3], g.z1, g.boff);
    gload4(bw, g.bsp, (g.boff >> 3) & 28u);
#pragma unroll
    for (int i = 0; i < K2_PF; i++) dma16(g.pf + i * 1024, g.boff, g.dump);
  }
  if constexpr (TAU < 111) k2_group<TAU + 1>(g, bf, af, ap0, ap1, acc, ep, bw, tch);
}

DEV void k2_gemm(char* smem, int b, const u16* wsp, const u16* bsp, const u16* wout, char* ws) {
  STAMP_DECL; STAMP(0);
  const int xcd = b & 7, idx = b >> 3;
  const int tp = (xcd >> 1) * 8 + (idx >> 2), fp = (xcd & 1) * 4 + (idx & 3);
  const int w = __builtin_amdgcn_readfirstlane(threadIdx.x >> 6), l = threadIdx.x & 63;
  const int rho = l & 15, q = l >> 4;
  const int tt = tp * 4 + w;
  char* const reg = smem + w * 16384;
  K2G g;
  g.wsp = (const char*)wsp + (size_t)tp * 64 * 4096 + w * 1024;
  g.wsp0 = (const char*)wsp + (size_t)tp * 64 * 4096 + w * 65536;
  g.z2 = ws + WS_Z2F + ((size_t)(fp * 64 + w * 16) * 7 + 4) * 1024;
  g.z1 = ws + WS_Z1F + ((size_t)tt * 28 + ((fp * 7) >> 1)) * 1024;
  g.bsp = (const char*)bsp + tt * 32;
  {
    int pc = (idx * 4 + w) * 7;
    pc = pc > 784 - 7 ? 784 - 7 : pc;
    g.pf = (const char*)wout + pc * 1024;
  }
  g.aoff[0] = (u32)((l >> 3) * 4096 + (((l & 7) ^ (l >> 4)) * 16));
  g.aoff[1] = (u32)((l >> 3) * 4096 + (((l & 7) ^ ((l >> 4) | 4)) * 16));
  g.boff = l * 16;
  g.adst = lds_addr(reg);
  g.dump = lds_addr(smem) + 65536 + w * 1024;
  g.w = w;
  g.arot = (idx & 3) * 2;
  u32x4 bf[K2_R], af[2][4], ep[4];
  u32 bw, tch[2];
  g.tb = (u32)(((idx >> 2) * 14 + (l < 14 ? l : 13)) * 128);
  tch[0] = tch[1] = 0u;
  k2_as<0, 8>(g); k2_bs<0, 14>(g, bf);
  u32 tl;
  gload4(tl, g.wsp, (u32)((fp * 8 + (l & 7)) * 4096 + (8 - K2_LATE + ((l >> 3) < K2_LATE ? (l >> 3) : K2_LATE - 1)) * 128));
  k2_as<8, 16>(g); k2_bs<14, K2_R>(g, bf);
  const char* ap0 = reg + rho * 128 + (((0 + q) ^ (rho >> 1)) * 16);
  const char* ap1 = reg + rho * 128 + (((4 + q) ^ (rho >> 1)) * 16);
  f32x4 acc[4][7] = {};
  STAMP(1);
  wait_vm<K2_PRO - 8>();
  STAMP(2);
#pragma unroll
  for (int t = 0; t < 4; t++) af[0][t] = k2_read_a<0>(ap0, ap1, t);
  k2_group<0>(g, bf, af, ap0, ap1, acc, ep, bw, tch);
  keep(tl);
#pragma unroll
  for (int i = 0; i < 14; i++) {
    *(f32x4*)(reg + i * 1024 + l * 16) = acc[i < 7 ? 3 : 1][i % 7];
    SB();
    acc[2][i % 7] = K2_MFMA(bf[(98 + i) % K2_R], af[i / 7][2], acc[2][i % 7]);
    acc[0][i % 7] = K2_MFMA(bf[(98 + i) % K2_R], af[i / 7][0], acc[0][i % 7]);
    SB();
  }
  STAMP(3);
  __builtin_amdgcn_s_setprio(2);
  wait_vm<K2_PF>();
#pragma unroll
  for (int i = 0; i < 4; i++) pin(ep[i]);
  keep(bw);
  bar();
  STAMP(4);
  char* const in1 = smem + (w ^ 1) * 16384;
#pragma unroll
  for (int f = 0; f < 7; f++) acc[2][f] += *(const f32x4*)(in1 + f * 1024 + l * 16);
#pragma unroll
  for (int f = 0; f < 7; f++) *(f32x4*)(in1 + f * 1024 + l * 16) = acc[2][f];
  const float bs = (rho & 1) ? bfhi(bw) : bflo(bw);
  const int odd = fp & 1;
  f32x4 zf[7];
#pragma unroll
  for (int f = 0; f < 7; f++) {
    acc[0][f] += *(const f32x4*)(in1 + (7 + f) * 1024 + l * 16) + bs;
    const u32x4 ze = ep[f >> 1], zo = ep[(f + 1) >> 1];
    const u32 za = odd ? ((f & 1) ? zo.x : zo.z) : ((f & 1) ? ze.z : ze.x);
    const u32 zb = odd ? ((f & 1) ? zo.y : zo.w) : ((f & 1) ? ze.w : ze.y);
    zf[f] = (f32x4){bflo(za), bfhi(za), bflo(zb), bfhi(zb)};
    asm volatile("" : "+v"(zf[f]), "+v"(acc[0][f]));
  }
  bar();
  STAMP(5);
  char* const in2 = smem + (w ^ 3) * 16384;
  u32x2 hv[8];
#pragma unroll
  for (int f = 0; f < 7; f++) {
    const f32x4 v = (acc[0][f] + *(const f32x4*)(in2 + f * 1024 + l * 16)) * zf[f];
    hv[f] = (u32x2){pk_bf16(v.x, v.y), pk_bf16(v.z, v.w)};
  }
  const u32 hb = (u32)(WS_HF + (tt * 28 + ((fp * 7) >> 1)) * 1024 + l * 16);
  if (odd) {
    gstore8(ws, hb + 8, hv[0]);
#pragma unroll
    for (int i = 0; i < 3; i++) gstore16(ws, hb + (i + 1) * 1024, (u32x4){hv[1 + 2 * i].x, hv[1 + 2 * i].y, hv[2 + 2 * i].x, hv[2 + 2 * i].y});
  } else {
#pragma unroll
    for (int i = 0; i < 3; i++) gstore16(ws, hb + i * 1024, (u32x4){hv[2 * i].x, hv[2 * i].y, hv[1 + 2 * i].x, hv[1 + 2 * i].y});
    gstore8(ws, hb + 3 * 1024, hv[6]);
  }
  STAMP(6); STAMP_FLUSH(1);
}

constexpr int KG_ROW = 272, KG_WAVE = 53 * KG_ROW;
constexpr int CV_CS = 4160, CV_T = 8 * CV_CS, CV_U = 2 * CV_T, CV_CMP = CV_U + 2 * 4608;
DEV f32x2 cexp_pow2(float gl, float fh, float fl, float p) {
  float m = __builtin_amdgcn_exp2f(gl * p);
  float r = __builtin_amdgcn_fractf(fh * p) + fl * p;
  return (f32x2){m * __builtin_amdgcn_cosf(r), m * __builtin_amdgcn_sinf(r)};
}
DEV float exp_acc(float x) {
  const float h = x * 1.44269504089f;
  const float lo = fmaf(x, 1.44269504089f, -h) + x * 1.925963e-8f;
  return __builtin_amdgcn_exp2f(h) * fmaf(lo, 0.69314718056f, 1.f);
}
struct KGen { f32x2 e1, e2, e4, e8, e16, e32, e64, e128, e256, e512, e1024, e2048, c0; };
DEV KGen kg_gens(float la, float ai, float cr, float ci, float dt, float sc) {
  const float zr = -rbf(exp_acc(la)) * dt, zi = ai * dt;
  const float gl = zr * 1.44269504089f;
  const float fh = zi * 0.15915494309f;
  const float fl = fmaf(zi, 0.15915494309f, -fh) + zi * 6.4206382e-9f;
  KGen k;
  k.c0 = (f32x2){cr * sc, ci * sc};
  k.e1 = cexp_pow2(gl, fh, fl, 1.f); k.e2 = csqr(k.e1); k.e4 = csqr(k.e2); k.e8 = csqr(k.e4);
  k.e16 = cexp_pow2(gl, fh, fl, 16.f); k.e32 = csqr(k.e16); k.e64 = csqr(k.e32); k.e128 = csqr(k.e64);
  k.e256 = cexp_pow2(gl, fh, fl, 256.f); k.e512 = csqr(k.e256); k.e1024 = csqr(k.e512); k.e2048 = csqr(k.e1024);
  return k;
}
DEV void kg_tables(const KGen& k, u32* tw) {
#define TW(row, v) tw[(row) * (KG_ROW / 4)] = (v)
  {
    f32x2 tb[16];
    tb[0] = (f32x2){128.f, 0.f};
    tb[1] = k.e8 * 128.f;
    tb[2] = k.e16 * 128.f; tb[3] = cmul(tb[1], k.e16);
    tb[4] = k.e32 * 128.f;
#pragma unroll
    for (int i = 1; i < 4; i++) tb[4 + i] = cmul(tb[i], k.e32);
    tb[8] = k.e64 * 128.f;
#pragma unroll
    for (int i = 1; i < 8; i++) tb[8 + i] = cmul(tb[i], k.e64);
    TW(0, pk_f16(128.f, 0.f));
    TW(16, pk_f16(k.e128.x * 128.f, k.e128.y * 128.f));
#pragma unroll
    for (int i = 1; i < 16; i++) { TW(i, pk_f16(tb[i].x, tb[i].y)); f32x2 u = cmul(tb[i], k.e128); TW(16 + i, pk_f16(u.x, u.y)); }
  }
  {
    f32x2 a[21];
    a[0] = k.c0; a[1] = cmul(k.c0, k.e1); a[2] = cmul(k.c0, k.e2); a[3] = cmul(a[1], k.e2);
#pragma unroll
    for (int i = 0; i < 4; i++) a[4 + i] = cmul(a[i], k.e4);
#pragma unroll
    for (int i = 0; i < 4; i++) a[8 + i] = cmul(a[2 * i], k.e256);
    a[12] = cmul(a[0], k.e512); a[13] = cmul(a[4], k.e512); a[14] = cmul(a[8], k.e512); a[15] = cmul(a[10], k.e512);
    a[16] = cmul(a[0], k.e1024); a[17] = cmul(a[8], k.e1024); a[18] = cmul(a[12], k.e1024); a[19] = cmul(a[14], k.e1024);
    a[20] = cmul(a[0], k.e2048);
#pragma unroll
    for (int i = 0; i < 21; i++) TW(32 + i, pk_f16(a[i].x, -a[i].y));
  }
#undef TW
}
template <int CTRL> DEV u32 dpp(u32 old, u32 src) { return __builtin_amdgcn_update_dpp(old, src, CTRL, 0xf, 0xf, false); }
template <int CTRL> DEV float dpp_add(float v) { return v + __uint_as_float(__builtin_amdgcn_update_dpp(0u, __float_as_uint(v), CTRL, 0xf, 0xf, true)); }
DEV void cv_frags(u32x4 (&A)[8], u32x4 (&B)[8], const char* pa, const char* pb) {
#pragma unroll
  for (int i = 0; i < 8; i++) {
    A[i] = *(const u32x4*)(pa - 512 * i);
    B[i] = *(const u32x4*)(pb + 512 * i);
  }
}
DEV void cv_mma(const u32x4 (&A)[8], const u32x4 (&B)[8], f32x4 (&acc)[8]) {
#pragma unroll
  for (int a = 0; a < 8; a++)
#pragma unroll
    for (int s = 7; s >= a; s--) acc[s] = mfma_h(A[a], B[s - a], acc[s]);
}
#define SSM_PRIO 0
DEV void k2_ssm(char* smem, int cb, const u16* lar, const u16* aim, const u16* cre, const u16* cim, const u16* ldt, const u16* dsk, char* ws) {
  STAMP_DECL; STAMP(0);
  __builtin_amdgcn_s_setprio(3);
  const int w = __builtin_amdgcn_readfirstlane(threadIdx.x >> 6), l = threadIdx.x & 63;
  const int ch = w >> 1, d = cb * 2 + ch, hw = w & 1, t = hw * 64 + l;
  const int rho = l & 15, q = l >> 4;
  const size_t o = (size_t)d * NMODE;
  const u32 vla = *(const u32*)(lar + o + hw * 128 + 2 * l), vai = *(const u32*)(aim + o + hw * 128 + 2 * l);
  const u32 vcr = *(const u32*)(cre + o + hw * 128 + 2 * l), vci = *(const u32*)(cim + o + hw * 128 + 2 * l);
  const u32 ocr = *(const u32*)(cre + o + (hw ^ 1) * 128 + 2 * l), oci = *(const u32*)(cim + o + (hw ^ 1) * 128 + 2 * l);
  const u32 vdt = ldt[d], vdk = dsk[d];
  char* const T = smem + ch * CV_T;
  char* const U = smem + CV_U + ch * 4608;
  u16* const cmp = (u16*)(smem + CV_CMP + ch * 1408);
#pragma unroll
  for (int i = 0; i < 2; i++) dma16(ws + WS_UT + (size_t)d * (SEQ * 2) + (hw * 2 + i) * 1024, l * 16, lds_addr(U) + 512 + (hw * 2 + i) * 1024);
  __builtin_amdgcn_s_setprio(SSM_PRIO);
  if (t < 32) *(u32x4*)(U + t * 16) = (u32x4){0u, 0u, 0u, 0u};
  u32 gi[2][8];
#pragma unroll
  for (int c = 0; c < 2; c++)
#pragma unroll
    for (int j = 0; j < 4; j++) {
      const int l0 = 2047 - (hw * 128 + c * 64 + l) * 8 - 2 * j, l1 = l0 - 1;
      const u32 bits = pk_bf16((float)l0, (float)l1);
      int i0 = (int)(bits & 0xffffu) - 0x4280, i1 = (int)(bits >> 16) - 0x4280;
      if (c == 1) { i0 = l0 < 128 ? l0 : i0; i1 = l1 < 128 ? l1 : i1; }
      gi[c][2 * j] = i0; gi[c][2 * j + 1] = i1;
    }
  const float dt = rbf(exp_acc(bf2f(vdt)));
  const float dk = bf2f(vdk);
  float S = fabsf(bflo(vcr)) + fabsf(bfhi(vcr)) + fabsf(bflo(vci)) + fabsf(bfhi(vci));
  float S2 = fabsf(bflo(ocr)) + fabsf(bfhi(ocr)) + fabsf(bflo(oci)) + fabsf(bfhi(oci));
  S = hw ? S2 + S : S + S2;
  S = dpp_add<0xb1>(S); S = dpp_add<0x4e>(S); S = dpp_add<0x141>(S); S = dpp_add<0x140>(S);
  S = xsum32(xsum16(S));
  int e = (int)((__float_as_uint(S) >> 23) & 255u) - 125;
  e = e < -100 ? -100 : (e > 100 ? 100 : e);
  e = __builtin_amdgcn_readfirstlane(e);
  const float sc = __uint_as_float((u32)(127 + 8 - e) << 23);
  const float ks = dt * __uint_as_float((u32)(127 + e - 14) << 23);
  STAMP(1);
  char* tab = smem + w * KG_WAVE;
  f32x16 kacc = {};
  {
    const int r = l & 31, h = l >> 5;
    const char* pa = tab + (32 + (r < 21 ? r : 20)) * KG_ROW + h * 16;
    const char* pb = tab + r * KG_ROW + h * 16;
    const KGen k0 = kg_gens(bflo(vla), bflo(vai), bflo(vcr), bflo(vci), dt, sc);
    STAMP(8);
    kg_tables(k0, (u32*)(tab + l * 4));
    SB();
    STAMP(9);
    u32x4 fa[8], fb[8];
#pragma unroll
    for (int kk = 0; kk < 8; kk++) { fa[kk] = *(const u32x4*)(pa + kk * 32); fb[kk] = *(const u32x4*)(pb + kk * 32); }
    SB();
    const KGen k1 = kg_gens(bfhi(vla), bfhi(vai), bfhi(vcr), bfhi(vci), dt, sc);
#pragma unroll
    for (int kk = 0; kk < 8; kk++) kacc = mfma32_h(fa[kk], fb[kk], kacc);
    STAMP(10);
    kg_tables(k1, (u32*)(tab + l * 4));
    STAMP(11);
#pragma unroll
    for (int kk = 0; kk < 8; kk++) kacc = mfma32_h(*(const u32x4*)(pa + kk * 32), *(const u32x4*)(pb + kk * 32), kacc);
    STAMP(12);
    float* give = (float*)tab + l;
    float keep[6];
#pragma unroll
    for (int i = 0; i < 6; i++) { give[i * 64] = hw ? kacc[i] : kacc[6 + i]; keep[i] = hw ? kacc[6 + i] : kacc[i]; }
    const int b1 = h ? 384 + 2 * r : 256 + 4 * r, b2 = h ? (r == 0 ? 640 : 672) : 512 + r;
    const int x0 = hw ? b1 + (h ? 64 : 2) : 4 * h + 8 * r;
    const int x1 = hw ? b2 : x0 + 2, d1 = hw ? (h ? (r == 0 ? 32 : 0) : 32) : 1;
    const int x2 = hw ? (h ? 672 : b2 + 64) : b1, d2 = hw ? (h ? 0 : 32) : 1;
    bar();
    const float* take = (const float*)(smem + (w ^ 1) * KG_WAVE) + l;
    const int xi[6] = {x0, x0 + 1, x1, x1 + d1, x2, x2 + d2};
#pragma unroll
    for (int i = 0; i < 6; i++) {
      const float o = take[i * 64];
      cmp[xi[i]] = __builtin_bit_cast(u16, (_Float16)(hw ? o + keep[i] : keep[i] + o));
    }
  }
  STAMP(2);
  bar();
  {
    u32 ra[3][4];
#pragma unroll
    for (int c = 0; c < 2; c++)
#pragma unroll
      for (int j = 0; j < 4; j++) ra[c][j] = (u32)cmp[gi[c][2 * j]] | ((u32)cmp[gi[c][2 * j + 1]] << 16);
    {
      const u32 k8 = hw ? 0u : cmp[512], k4 = hw ? 0u : cmp[511], k0 = hw ? 0u : cmp[510];
      ra[2][0] = k8 * 0x10001u; ra[2][1] = k4 * 0x10001u; ra[2][2] = k4 | (k0 << 16); ra[2][3] = k0 * 0x10001u;
    }
#pragma unroll
    for (int c = 0; c < 2; c++) {
      u32 dw[8], od[7];
#pragma unroll
      for (int j = 0; j < 4; j++) {
        dw[j] = ra[c][j];
        dw[4 + j] = dpp<0x130>(c == 0 ? dpp<0x134>(0u, ra[1][j]) : ra[2][j], ra[c][j]);
      }
#pragma unroll
      for (int i = 0; i < 7; i++) od[i] = __builtin_amdgcn_alignbit(dw[i + 1], dw[i], 16);
      char* tp = T + (hw * 128 + c * 64 + l) * 16;
#pragma unroll
      for (int k = 0; k < 4; k++) {
        *(u32x4*)(tp + (2 * k) * CV_CS) = (u32x4){dw[k], dw[k + 1], dw[k + 2], dw[k + 3]};
        *(u32x4*)(tp + (2 * k + 1) * CV_CS) = (u32x4){od[k], od[k + 1], od[k + 2], od[k + 3]};
      }
    }
    if (t < 8) *(u32x4*)(T + t * CV_CS + 4096) = (u32x4){0u, 0u, 0u, 0u};
  }
  wait_vm<0>();
  bar();
  STAMP(3);
  const char* pa = T + (7 - (rho & 7)) * CV_CS + (255 - (rho >> 3) + q) * 16 - 32 - 256 * hw;
  const char* pb = U + 512 + rho * 32 + q * 16 - 32 - 256 * hw;
  f32x4 acc[8] = {};
  {
    u32x4 A0[8], B0[8], A1[8], B1[8];
    cv_frags(A0, B0, pa, pb);
    SB(); cv_frags(A1, B1, pa - 64, pb - 64); SB();
    cv_mma(A0, B0, acc);
    SB(); cv_frags(A0, B0, pa - 128, pb - 128); SB();
    cv_mma(A1, B1, acc);
    SB(); cv_frags(A1, B1, pa - 192, pb - 192); SB();
    cv_mma(A0, B0, acc);
    cv_mma(A1, B1, acc);
  }
  STAMP(4);
  bar();
  {
    char* box = T + hw * 4096;
#pragma unroll
    for (int i = 0; i < 4; i++) {
      const f32x4 give = hw ? acc[i] : acc[4 + i];
      acc[i] = hw ? acc[4 + i] : acc[i];
      *(f32x4*)(box + i * 1024 + l * 16) = give;
    }
  }
  u32x2 ub[4];
#pragma unroll
  for (int i = 0; i < 4; i++) ub[i] = *(const u32x2*)(U + 512 + (256 * (4 * hw + i) + 16 * rho + 4 * q) * 2);
  bar();
  {
    const char* box = T + (hw ^ 1) * 4096;
    u32x2 yv[4];
#pragma unroll
    for (int i = 0; i < 4; i++) {
      f32x4 v = (acc[i] + *(const f32x4*)(box + i * 1024 + l * 16)) * ks;
      f16x2 u0 = as_h2(ub[i].x), u1 = as_h2(ub[i].y);
      yv[i] = (u32x2){pk_bf16(fmaf((float)u0.x, dk, v.x), fmaf((float)u0.y, dk, v.y)), pk_bf16(fmaf((float)u1.x, dk, v.z), fmaf((float)u1.y, dk, v.w))};
    }
#pragma unroll
    for (int i = 0; i < 4; i += 2) {
      auto sx = __builtin_amdgcn_permlane16_swap(yv[i].x, yv[i + 1].x, false, false);
      auto sy = __builtin_amdgcn_permlane16_swap(yv[i].y, yv[i + 1].y, false, false);
      const u32 off = (u32)(512 * (4 * hw + i + (q & 1)) + 32 * rho + 16 * (q >> 1));
      gstore16(ws, (u32)(WS_YT + d * (SEQ * 2)) + off, (u32x4){sx[0], sy[0], sx[1], sy[1]});
    }
  }
  STAMP(5); STAMP_FLUSH(1);
}

extern "C" __global__ NOPK __launch_bounds__(256, 2) void k2(const u16* wsp, const u16* bsp, const u16* wout, const u16* lar, const u16* aim, const u16* cre,
                                             const u16* cim, const u16* ldt, const u16* dsk, char* ws) {
  __shared__ __attribute__((aligned(16))) char smem[81920];
  LST(1);
  if (blockIdx.x < 256) k2_gemm(smem, blockIdx.x, wsp, bsp, wout, ws);
  else k2_ssm(smem, blockIdx.x - 256, lar, aim, cre, cim, ldt, dsk, ws);
}

#define K3_LEAD 3
struct K3G { const char* wo; const char* hf; u32 woff; u32 hoff[4]; u32 wdst; const char* yt; u32 yoff; u32 ydst; const char* xb; u32 xo; };
template <int I> DEV void k3_issue(const K3G& g, u32x4 (&hf)[7][4], u32x4 (&xv)[2]) {
  if constexpr (I < 7) {
#pragma unroll
    for (int i = 0; i < 4; i++) dma16(g.wo + (size_t)(I * 32 + i * 8) * (DM * 2), g.woff, g.wdst + (I * 4 + i) * 1024);
#pragma unroll
    for (int t = 0; t < 4; t++) gload16<0>(hf[I][t], g.hf + I * 1024, g.hoff[t]);
  } else if constexpr (I == 7) {
    dma16(g.yt, g.yoff, g.ydst);
    dma16(g.yt + 32 * (SEQ * 2), g.yoff, g.ydst + 1024);
    gload16<0>(xv[0], g.xb, g.xo);
    gload16<0>(xv[1], g.xb + 8 * (DM * 2), g.xo);
  }
}
constexpr int k3_cnt(int units) { return units <= 7 ? 8 * units : 60; }
template <int I> DEV void k3_step(const K3G& g, u32x4 (&hf)[7][4], u32x4 (&xv)[2], const char* const (&wp)[4], f32x4 (&acc)[4][4]) {
  wait_vm<k3_cnt(I + K3_LEAD) - 8 * (I + 1)>();
#pragma unroll
  for (int t = 0; t < 4; t++) pin(hf[I][t]);
  u32x4 wf[4];
#pragma unroll
  for (int p = 0; p < 4; p++) wf[p] = tr_frag(wp[p] + I * 4096, wp[p] + I * 4096 + 2048);
  k3_issue<I + K3_LEAD>(g, hf, xv);
#pragma unroll
  for (int p = 0; p < 4; p++)
#pragma unroll
    for (int t = 0; t < 4; t++) acc[t][p] = mfma_bf(wf[p], hf[I][t], acc[t][p]);
  if constexpr (I < 6) k3_step<I + 1>(g, hf, xv, wp, acc);
}
template <int A, int B> DEV void k3_pro(const K3G& g, u32x4 (&hf)[7][4], u32x4 (&xv)[2]) {
  if constexpr (A < B) { k3_issue<A>(g, hf, xv); k3_pro<A + 1, B>(g, hf, xv); }
}
extern "C" __global__ __launch_bounds__(256, 1) void k3(const u16* x, const u16* wout, u16* out, char* ws) {
  __shared__ __attribute__((aligned(16))) char smem[4 * 28672 + 8192];
  STAMP_DECL; STAMP(0);
  LST(2);
  const int b = blockIdx.x, xcd = b & 7, idx = b >> 3;
  const int tp = xcd * 4 + idx / 7, dp = idx % 7;
  const int w = __builtin_amdgcn_readfirstlane(threadIdx.x >> 6), l = threadIdx.x & 63;
  const int rho = l & 15, q = l >> 4, qq = (l >> 2) & 3, pp = l & 3;
  char* const reg = smem + w * 28672;
  char* const Y = smem + 4 * 28672;
  K3G g;
  g.wo = (const char*)wout + (size_t)(w * 224) * (DM * 2) + dp * 128;
  g.hf = ws + WS_HF + ((size_t)tp * 4 * 28 + w * 7) * 1024;
  g.woff = (u32)((l >> 3) * (DM * 2) + (((l & 7) ^ ((l >> 4) << 1)) * 16));
#pragma unroll
  for (int t = 0; t < 4; t++) g.hoff[t] = (u32)((t ^ w) * 28 * 1024 + l * 16);
  g.wdst = lds_addr(reg);
  g.yt = ws + WS_YT + (size_t)(dp * 64) * (SEQ * 2) + tp * 128 + w * 32;
  g.yoff = (u32)((l >> 1) * (SEQ * 2) + (l & 1) * 16);
  g.ydst = lds_addr(Y) + w * 2048;
  g.xo = (u32)((l >> 3) * (DM * 2) + (l & 7) * 16);
  const size_t xb = (size_t)(tp * 64 + w * 16) * (DM * 2) + dp * 128;
  g.xb = (const char*)x + xb;
  u32x4 hf[7][4], xv[2];
  k3_pro<0, K3_LEAD>(g, hf, xv);
  const char* wp[4];
  const int sg = ((q & 1) << 1) | (qq >> 1);
#pragma unroll
  for (int p = 0; p < 4; p++) wp[p] = reg + (4 * q + qq) * 128 + ((p ^ sg) * 32) + 8 * pp;
  f32x4 acc[4][4] = {};
  STAMP(1);
  k3_step<0>(g, hf, xv, wp, acc);
  STAMP(2);
#pragma unroll
  for (int t = 1; t < 4; t++)
#pragma unroll
    for (int p = 0; p < 4; p++) *(f32x4*)(reg + t * 4352 + rho * 272 + p * 64 + q * 16) = acc[t][p];
  wait_vm<0>();
  pin(xv[0]); pin(xv[1]);
#pragma unroll
  for (int p = 0; p < 4; p++) {
    u32x2 yv = tr_read(Y + w * 2048 + (16 * p + 4 * q + qq) * 32 + 8 * pp);
    *(f32x4*)(reg + rho * 272 + p * 64 + q * 16) = acc[0][p] + (f32x4){bflo(yv.x), bfhi(yv.x), bflo(yv.y), bfhi(yv.y)};
  }
  bar();
  STAMP(3);
#pragma unroll
  for (int i = 0; i < 2; i++) {
    f32x4 a = {bflo(xv[i].x), bfhi(xv[i].x), bflo(xv[i].y), bfhi(xv[i].y)}, c = {bflo(xv[i].z), bfhi(xv[i].z), bflo(xv[i].w), bfhi(xv[i].w)};
#pragma unroll
    for (int t = 0; t < 4; t++) {
      const char* rp = smem + (w ^ t) * 28672 + t * 4352 + (8 * i + (l >> 3)) * 272 + (l & 7) * 32;
      a += *(const f32x4*)(rp); c += *(const f32x4*)(rp + 16);
    }
    u32x4 o = {pk_bf16(a.x, a.y), pk_bf16(a.z, a.w), pk_bf16(c.x, c.y), pk_bf16(c.z, c.w)};
    gstore16(out, (u32)xb + i * 8 * (DM * 2) + g.xo, o);
  }
  STAMP(4); STAMP_FLUSH(2);
  LST(3);
  asm volatile("k3_code_end:");
}

#define K1_GRID 256
#define K2_GRID 480
extern "C" void kernel_launch(void* const* d_in, const int* in_sizes, int n_in, void* d_out, int out_size, void* d_ws, size_t ws_size,
                              hipStream_t stream) {
  const u16* x = (const u16*)d_in[0];
  char* ws = (char*)d_ws;
  Extra ex = {{(const char*)d_out, (const char*)d_in[1], (const char*)d_in[2], (const char*)d_in[3], (const char*)d_in[4], (const char*)d_in[5], (const char*)d_in[6], (const char*)d_in[10]}};
  k1<<<K1_GRID, 512, 0, stream>>>(x, (const u16*)d_in[8], (const u16*)d_in[9], (const u16*)d_in[11], ws, ex);
  k2<<<K2_GRID, 256, 0, stream>>>((const u16*)d_in[9], (const u16*)d_in[10], (const u16*)d_in[11], (const u16*)d_in[1], (const u16*)d_in[2],
                              (const u16*)d_in[3], (const u16*)d_in[4], (const u16*)d_in[5], (const u16*)d_in[6], ws);
  k3<<<224, 256, 0, stream>>>(x, (const u16*)d_in[11], (u16*)d_out, ws);
}
```

```cpp
#include <hip/hip_runtime.h>
#include <stdint.h>

typedef unsigned short u16;
typedef unsigned int u32;
typedef unsigned long long u64;
typedef __attribute__((ext_vector_type(4))) u32 u32x4;
typedef __attribute__((ext_vector_type(2))) u32 u32x2;
typedef __attribute__((ext_vector_type(4))) float f32x4;
typedef __attribute__((ext_vector_type(2))) float f32x2;
typedef __attribute__((ext_vector_type(16))) float f32x16;
typedef __attribute__((ext_vector_type(8))) __bf16 bf16x8;
typedef __attribute__((ext_vector_type(2))) __bf16 bf16x2;
typedef __attribute__((ext_vector_type(8))) _Float16 f16x8;
typedef __attribute__((ext_vector_type(2))) _Float16 f16x2;
typedef __attribute__((ext_vector_type(4))) short s16x4;
#define LDSAS __attribute__((address_space(3)))
#define GAS __attribute__((address_space(1)))
#define DEV __device__ __forceinline__
#if defined(__HIP_DEVICE_COMPILE__)
#define NOPK __attribute__((target("no-packed-fp32-ops")))
#else
#define NOPK
#endif
#define SB() __builtin_amdgcn_sched_barrier(0)

constexpr int SEQ = 2048, DM = 448, NMODE = 256, FF = 1792, FH = 896;

constexpr size_t WS_Z2F = 0;
constexpr size_t WS_Z1F = 4u << 20;
constexpr size_t WS_HF  = 8u << 20;
constexpr size_t WS_UT  = 12u << 20;
constexpr size_t WS_YT  = 14u << 20;
constexpr size_t WS_GR  = 16u << 20;
constexpr size_t WS_EP  = 17u << 20;

#define STAMP(i)
#define STAMP_DECL
#define STAMP_ARG
#define STAMP_PASS
#define STAMP_FLUSH(kern)
#define LST(k)
DEV float bf2f(u32 v) { return __uint_as_float(v << 16); }
DEV float bflo(u32 v) { return __uint_as_float(v << 16); }
DEV float bfhi(u32 v) { return __uint_as_float(v & 0xffff0000u); }
DEV u32 pk_bf16(float a, float b) { bf16x2 v = {(__bf16)a, (__bf16)b}; return __builtin_bit_cast(u32, v); }
DEV u32 pk_f16(float a, float b) { f16x2 v = {(_Float16)a, (_Float16)b}; return __builtin_bit_cast(u32, v); }
DEV f16x2 as_h2(u32 v) { return __builtin_bit_cast(f16x2, v); }
DEV float rbf(float a) { return __uint_as_float(pk_bf16(a, a) << 16); }
DEV f32x4 mfma_bf(u32x4 a, u32x4 b, f32x4 c) {
  return __builtin_amdgcn_mfma_f32_16x16x32_bf16(__builtin_bit_cast(bf16x8, a), __builtin_bit_cast(bf16x8, b), c, 0, 0, 0);
}
DEV f32x4 mfma_h(u32x4 a, u32x4 b, f32x4 c) {
  return __builtin_amdgcn_mfma_f32_16x16x32_f16(__builtin_bit_cast(f16x8, a), __builtin_bit_cast(f16x8, b), c, 0, 0, 0);
}
DEV f32x16 mfma32_h(u32x4 a, u32x4 b, f32x16 c) {
  return __builtin_amdgcn_mfma_f32_32x32x16_f16(__builtin_bit_cast(f16x8, a), __builtin_bit_cast(f16x8, b), c, 0, 0, 0);
}
DEV u32x2 tr_read(const char* p) {
  s16x4 v = __builtin_amdgcn_ds_read_tr16_b64_v4i16((LDSAS s16x4*)p);
  return __builtin_bit_cast(u32x2, v);
}
DEV u32x4 tr_frag(const char* p0, const char* p1) {
  u32x2 a = tr_read(p0), b = tr_read(p1);
  return (u32x4){a.x, a.y, b.x, b.y};
}
#define LDMOD ""
#define DMAMOD ""
DEV u32 lds_addr(const void* p) { return (u32)(uintptr_t)(LDSAS const char*)p; }
DEV void dma16(const void* sbase, u32 voff, u32 ldst) {
  asm volatile("s_mov_b32 m0, %2\n\ts_nop 0\n\tglobal_load_lds_dwordx4 %0, %1 " DMAMOD ::"v"(voff), "s"(sbase), "s"(ldst) : "memory");
}
template <int OFF> DEV void gload16(u32x4& d, const void* sbase, u32 voff) {
  asm volatile("global_load_dwordx4 %0, %1, %2 offset:%3 " LDMOD : "=v"(d) : "v"(voff), "s"(sbase), "i"(OFF) : "memory");
}
DEV void gstore16(void* sbase, u32 voff, u32x4 d) {
  asm volatile("global_store_dwordx4 %0, %1, %2 sc1\n\ts_nop 1" ::"v"(voff), "v"(d), "s"(sbase) : "memory");
}
DEV void gstore8(void* sbase, u32 voff, u32x2 d) {
  asm volatile("global_store_dwordx2 %0, %1, %2 sc1" ::"v"(voff), "v"(d), "s"(sbase) : "memory");
}
DEV void gstore16p(void* sbase, u32 voff, u32x4 d) {
  asm volatile("global_store_dwordx4 %0, %1, %2\n\ts_nop 1" ::"v"(voff), "v"(d), "s"(sbase) : "memory");
}
#define Z2STORE gstore16
DEV void gload4(u32& d, const void* sbase, u32 voff) {
  asm volatile("global_load_dword %0, %1, %2" : "=v"(d) : "v"(voff), "s"(sbase) : "memory");
}
template <int N> DEV void wait_vm() { asm volatile("s_waitcnt vmcnt(%0)" ::"i"(N) : "memory"); }
DEV void touch(u32& d, const void* p) { asm volatile("global_load_dword %0, %1, off" : "=v"(d) : "v"(p) : "memory"); }
DEV void keep(u32& a) { asm volatile("" : "+v"(a)); }
DEV void pin(u32x4& a) { asm volatile("" : "+v"(a)); }
DEV void bar() { asm volatile("s_waitcnt lgkmcnt(0)\n\ts_barrier" ::: "memory"); }
DEV float xsum16(float v) { auto r = __builtin_amdgcn_permlane16_swap(__float_as_uint(v), __float_as_uint(v), false, false); return __uint_as_float(r[0]) + __uint_as_float(r[1]); }
DEV float xsum32(float v) { auto r = __builtin_amdgcn_permlane32_swap(__float_as_uint(v), __float_as_uint(v), false, false); return __uint_as_float(r[0]) + __uint_as_float(r[1]); }
DEV f32x2 ln_scalars(float sum, float sq, float inv_n) {
  float m = sum * inv_n;
  float v = fmaxf(sq * inv_n - m * m, 0.f);
  float sd = rbf(__builtin_amdgcn_sqrtf(rbf(rbf(v) + 1e-5f)));
  return (f32x2){rbf(m), __builtin_amdgcn_rcpf(sd)};
}
DEV f32x2 gelu2(f32x2 x) {
  const float c1 = -2.3022081983f, c2 = -0.1029432396f;
  f32x2 t = x * x;
  t = t * c2 + c1;
  t = t * x;
  f32x2 e = {__builtin_amdgcn_exp2f(t.x), __builtin_amdgcn_exp2f(t.y)};
  e = e + 1.0f;
  f32x2 r = {__builtin_amdgcn_rcpf(e.x), __builtin_amdgcn_rcpf(e.y)};
  return x * r;
}
DEV f32x2 cmul(f32x2 a, f32x2 b) { return (f32x2){fmaf(a.x, b.x, -(a.y * b.y)), fmaf(a.x, b.y, a.y * b.x)}; }
DEV f32x2 csqr(f32x2 a) { return (f32x2){a.x * a.x - a.y * a.y, 2.f * a.x * a.y}; }

#define TSTRIDE 65536u
#define K1_PF_STOP 16000u
#define K1_PF_END 1024
#define K1_BDELAY 20
#define K1_ROT (sib >> 1)
#define K1_PRIO 2
#define K1_PF 4
constexpr int k1_slot(int u) { return u < 9 ? u : u - 9; }
constexpr int K1_X = 73728, K1_UT = K1_X + 65536, K1_CS = K1_UT + 32 * 272, K1_LN = K1_CS + 512, K1_LN1 = K1_LN + 1024, K1_CNT = K1_LN1 + 1024, K1_DUMP = K1_CNT + 64, K1_LDS = K1_DUMP + 4096;

typedef LDSAS char* lptr;
DEV lptr pinned(lptr p) { asm volatile("" : "+v"(p)); return p; }
DEV u32x4 lread16(lptr p) { return *(LDSAS u32x4*)p; }
DEV u32x4 ltr_frag(lptr p0, lptr p1) {
  s16x4 a = __builtin_amdgcn_ds_read_tr16_b64_v4i16((LDSAS s16x4*)p0), b = __builtin_amdgcn_ds_read_tr16_b64_v4i16((LDSAS s16x4*)p1);
  u32x2 x = __builtin_bit_cast(u32x2, a), y = __builtin_bit_cast(u32x2, b);
  return (u32x4){x.x, x.y, y.x, y.y};
}
DEV void mma(f32x4& c, const u32x4& a, const u32x4& b) { asm volatile("v_mfma_f32_16x16x32_bf16 %0, %1, %2, %0" : "+v"(c) : "v"(a), "v"(b)); }
DEV void mma0(f32x4& c, const u32x4& a, const u32x4& b) { asm volatile("v_mfma_f32_16x16x32_bf16 %0, %1, %2, 0" : "=&v"(c) : "v"(a), "v"(b)); }
template <bool FIRST> DEV void mmaf(f32x4& c, const u32x4& a, const u32x4& b) { if constexpr (FIRST) mma0(c, a, b); else mma(c, a, b); }
DEV void mma_drain() { SB(); asm volatile("s_nop 7\n\ts_nop 1" ::: "memory"); SB(); }
DEV const char* wsp_piece(const u16* wsp, int xcd, int n) { return (const char*)wsp + (size_t)(xcd * 256 + (n & 31) * 8) * 4096 + ((n >> 5) & 3) * 1024 + (n >> 7) * 128; }
DEV void sbar() { asm volatile("s_barrier" ::: "memory"); }

struct K1L { const char* win; const char* xb; u32 xoff[2]; u32 woff[2]; u32 wdst; u32 xdst; int m0; };
template <int U> DEV void k1_unit(const K1L& g) {
  int line = U % 7 + g.m0; line = line >= 7 ? line - 7 : line;
  if constexpr (U < 7) {
#pragma unroll
    for (int i = 0; i < 4; i++) dma16(g.xb + line * 128 + (i >> 1) * 16 * (DM * 2), g.xoff[i & 1], g.xdst + (U & 3) * 16384 + i * 1024);
  }
#pragma unroll
  for (int i = 0; i < 2; i++) dma16(g.win + (size_t)line * 64 * (FF * 2) + (U < 7 ? FH * 2 : 0), g.woff[i], g.wdst + k1_slot(U) * 8192 + i * 1024);
}
struct K1C { lptr str[4]; lptr wp[4]; };
DEV void k1_xr(const K1C& g, u32x4 (&xf)[4], int M) {
#pragma unroll
  for (int i = 0; i < 4; i++) xf[i] = lread16(g.str[i] + (M & 3) * 16384);
}
template <int U> DEV u32x4 k1_wf(const K1C& g, int f) {
  if constexpr (k1_slot(U) < 8) return ltr_frag(g.wp[f & 3] + k1_slot(U) * 8192 + (f >> 2) * 4096, g.wp[f & 3] + k1_slot(U) * 8192 + (f >> 2) * 4096 + 512);
  else { lptr p = pinned(g.wp[f & 3] + 65536); return ltr_frag(p + (f >> 2) * 4096, p + (f >> 2) * 4096 + 512); }
}
template <int U> DEV void k1_main(const K1C& g, const u32x4 (&xf)[4], u32x4 (&wf)[8], f32x4 (&acc)[2][4], f32x4& cs, const u32x4& ones) {
#pragma unroll
  for (int f = 0; f < 8; f++) {
    if (U % 7 == 0 && f < 4) {
      if (f == 0) mma0(cs, wf[f], ones);
      mma0(acc[0][f & 3], wf[f], xf[0]);
      mma0(acc[1][f & 3], wf[f], xf[1]);
    } else {
      if ((f & 3) == 0) mma(cs, wf[f], ones);
      mma(acc[0][f & 3], wf[f], xf[(f >> 2) * 2]);
      mma(acc[1][f & 3], wf[f], xf[(f >> 2) * 2 + 1]);
    }
    SB(); if constexpr (U % 7 < 6) wf[f] = k1_wf<U + 1>(g, f); SB();
  }
}
template <int M>
DEV void k1_stage(const K1C& g, u32x4 (&xf)[7][4], u32x4 (&wf)[8], f32x4 (&acc)[2][4], f32x4& cs, f32x4 (&rsum)[2], f32x4 (&gram)[2], const u32x4& ones) {
  asm volatile("s_waitcnt lgkmcnt(15)\n\ts_barrier" ::: "memory");
  if constexpr (M < 6) k1_xr(g, xf[M + 1], M + 1);
  SB();
#pragma unroll
  for (int i = 0; i < 4; i++) {
    if (M == 0 && i < 2) { mma0(rsum[i & 1], ones, xf[M][i]); mma0(gram[i & 1], xf[M][i], xf[M][i]); }
    else { mma(rsum[i & 1], ones, xf[M][i]); mma(gram[i & 1], xf[M][i], xf[M][i]); }
  }
  k1_main<M>(g, xf[M], wf, acc, cs, ones);
  if constexpr (M < 6) k1_stage<M + 1>(g, xf, wf, acc, cs, rsum, gram, ones);
}
template <int M>
DEV void k1_z1loop(const K1C& g, u32x4 (&xf)[7][4], u32x4 (&wf)[8], f32x4 (&acc)[2][4], f32x4& cs, const u32x4& ones, const u16* wsp, int xcd, int pf, u32 l16, u32 dump) {
  if constexpr (M + 1 < K1_PF) dma16(wsp_piece(wsp, xcd, pf + (M + 1) * 112), l16, dump);
  k1_main<M + 7>(g, xf[M], wf, acc, cs, ones);
  if constexpr (M < 6) k1_z1loop<M + 1>(g, xf, wf, acc, cs, ones, wsp, xcd, pf, l16, dump);
}
DEV void k1_act(f32x4 (&acc)[2][4], const char* csb, int w, const f32x2 (&ln1)[2]) {
  float k1[2], k2[2], sd[2];
#pragma unroll
  for (int t = 0; t < 2; t++) {
    const float rs = ln1[t].y;
    k1[t] = -2.3022081983f * rs; k2[t] = -0.1029432396f * rs * rs * rs; sd[t] = __builtin_amdgcn_rcpf(rs);
  }
#pragma unroll
  for (int p = 0; p < 4; p++) {
    const f32x4 c = *(const f32x4*)(csb + (p ^ w) * 64);
#pragma unroll
    for (int t = 0; t < 2; t++) {
      const f32x4 a = acc[t][p] - c * ln1[t].x;
      const f32x4 u = a * (a * a * k2[t] + k1[t]);
      f32x4 e = {__builtin_amdgcn_exp2f(u.x), __builtin_amdgcn_exp2f(u.y), __builtin_amdgcn_exp2f(u.z), __builtin_amdgcn_exp2f(u.w)};
      e = e * sd[t] + sd[t];
      acc[t][p] = a * (f32x4){__builtin_amdgcn_rcpf(e.x), __builtin_amdgcn_rcpf(e.y), __builtin_amdgcn_rcpf(e.z), __builtin_amdgcn_rcpf(e.w)};
    }
  }
}
DEV int lane_id() { int l = __builtin_amdgcn_mbcnt_hi(~0u, __builtin_amdgcn_mbcnt_lo(~0u, 0u)); asm volatile("" : "+v"(l)); return l; }
DEV void quad_sync(lptr cnt, int l) {
  asm volatile("s_waitcnt lgkmcnt(0)" ::: "memory");
  if (l == 0) __hip_atomic_fetch_add((LDSAS u32*)cnt, 1u, __ATOMIC_RELAXED, __HIP_MEMORY_SCOPE_WORKGROUP);
  while (__builtin_amdgcn_readfirstlane(*(volatile LDSAS u32*)cnt) < 4u) __builtin_amdgcn_s_sleep(0);
  asm volatile("" ::: "memory");
}
DEV void lbar0() { asm volatile("s_waitcnt vmcnt(6) lgkmcnt(0)\n\ts_barrier" ::: "memory"); }

struct Extra { const char* p[8]; };
DEV void k1_body(char* smem, const u16* x, const u16* win, const u16* wsp, const u16* wout, char* ws, const Extra& ex) {
  STAMP_DECL; STAMP(0);
  const int b = blockIdx.x, xcd = b & 7, idx = b >> 3;
  const int w8 = __builtin_amdgcn_readfirstlane(threadIdx.x >> 6), l = threadIdx.x & 63, w = w8 & 3;
  if (b >= 224) {
    const int j = (idx - 28) * 8 + w8;
    const u32 t_start = (u32)__builtin_amdgcn_s_memtime();
    const u32 poff = (u32)((l >> 3) * 4096 + (l & 7) * 16);
    u32 dc;
    {
      u64 c0, c1;
      asm volatile("s_getpc_b64 vcc\n\ts_add_u32 vcc_lo, vcc_lo, k1@rel32@lo+4\n\ts_addc_u32 vcc_hi, vcc_hi, k1@rel32@hi+12\n\ts_mov_b64 %0, vcc" : "=s"(c0) :: "scc", "vcc");
      asm volatile("s_getpc_b64 vcc\n\ts_add_u32 vcc_lo, vcc_lo, k3_code_end@rel32@lo+4\n\ts_addc_u32 vcc_hi, vcc_hi, k3_code_end@rel32@hi+12\n\ts_mov_b64 %0, vcc" : "=s"(c1) :: "scc", "vcc");
      const u32 len = (u32)(c1 - c0) & ~3u, o = (u32)(j * 64 + l) * 128u;
      gload4(dc, (const void*)c0, j < 8 ? (o < len ? o : len - 4u) : 0u);
    }
    u32 d0, d1, d2, d3;
    {
      const int n = j * 64 + l;
      const char* p = n < 448 ? ex.p[0] + n * 4096 : (n < 672 ? ex.p[1 + (n - 448) / 56] + ((n - 448) % 56) * 4096 : ex.p[5 + (n % 3)]);
      asm volatile("" : "+v"(p));
      gload4(d0, ws, (u32)((n * TSTRIDE) % (18u << 20)));
      gload4(d1, ws, (u32)(((n + 2048) * TSTRIDE) % (18u << 20)));
      gload4(d2, ws, (u32)(((n + 4096) * TSTRIDE) % (18u << 20)));
      touch(d3, p);
    }
#pragma unroll
    for (int k = 0; k < 4; k++) dma16(ex.p[1 + k] + (xcd * 28 + (j < 28 ? j : 27)) * 1024, l * 16, lds_addr(smem) + w8 * 1024);
    const char* po = (const char*)wout + (size_t)(xcd * 98) * 1024;
#pragma unroll
    for (int k = 0; k < 4; k++) { int c = j + 32 * k; c = c < 98 ? c : 97; dma16(po + c * 1024, l * 16, lds_addr(smem) + w8 * 1024); }
#pragma unroll 1
    for (int i = 112 * K1_PF + j; i < K1_PF_END; i += 32) {
      dma16(wsp_piece(wsp, xcd, i), poff, lds_addr(smem) + w8 * 1024);
      if ((u32)__builtin_amdgcn_s_memtime() - t_start > K1_PF_STOP) break;
    }
    wait_vm<0>();
    keep(d0); keep(d1); keep(d2); keep(d3); keep(dc);
    STAMP(1); STAMP_FLUSH(0);
    return;
  }
  const int i7 = (idx * 37) >> 8;
  const int panel = (xcd >> 1) * 4 + i7, sib = (xcd & 1) * 7 + idx - 7 * i7;
  const lptr lds = (lptr)smem;
  K1L g;
  if (w8 >= 4) {
    g.win = (const char*)win + (size_t)(16 * w) * (FF * 2) + sib * 128;
    g.xb = (const char*)x + (size_t)(panel * 128 + w * 32) * (DM * 2);
    g.xoff[0] = (u32)((l >> 3) * (DM * 2) + (((l & 7) ^ (l >> 4)) * 16));
    g.xoff[1] = (u32)((8 + (l >> 3)) * (DM * 2) + (((l & 7) ^ ((l >> 4) | 4)) * 16));
    g.woff[0] = (u32)((l >> 3) * (FF * 2) + (((l & 7) ^ (((l >> 4) & 1) << 1)) * 16));
    g.woff[1] = (u32)((8 + (l >> 3)) * (FF * 2) + (((l & 7) ^ (((l >> 4) & 1) << 1) ^ 4) * 16));
    g.xdst = lds_addr(smem) + K1_X + w * 4096;
    g.wdst = lds_addr(smem) + w * 2048;
    g.m0 = K1_ROT;
    k1_unit<0>(g); k1_unit<1>(g);
  }
  K1C c;
  {
    const int rho = l & 15, q = l >> 4, qq = (l >> 2) & 3, pp = l & 3;
#pragma unroll
    for (int i = 0; i < 4; i++) c.str[i] = pinned(lds + K1_X + w * 4096 + (i & 1) * 2048 + rho * 128 + (((4 * (i >> 1) + q) ^ (rho >> 1)) * 16));
    const int sg = (qq >> 1) | ((q & 1) << 1);
#pragma unroll
    for (int f = 0; f < 4; f++) c.wp[f] = lds + (8 * q + qq) * 128 + (((f ^ w) ^ sg) * 32) + 8 * pp;
  }
  u32x4 ones = {0x3f803f80u, 0x3f803f80u, 0x3f803f80u, 0x3f803f80u};
  pin(ones);
  u32x4 xf[7][4], wf[8];
  f32x4 acc[2][4], cs;
  if (w8 >= 4) {
    lbar0();
    k1_unit<2>(g); k1_xr(c, xf[0], 0); lbar0();
    k1_unit<3>(g); k1_xr(c, xf[1], 1); lbar0();
    k1_unit<4>(g); k1_xr(c, xf[2], 2); lbar0();
    k1_unit<5>(g); k1_xr(c, xf[3], 3); lbar0();
    k1_unit<6>(g); k1_xr(c, xf[4], 4); lbar0();
    k1_unit<7>(g); k1_unit<8>(g); k1_unit<9>(g); k1_xr(c, xf[5], 5); lbar0();
    k1_unit<10>(g); k1_unit<11>(g); k1_unit<12>(g); k1_xr(c, xf[6], 6);
    asm volatile("s_waitcnt lgkmcnt(0)\n\ts_barrier" ::: "memory");
    k1_unit<13>(g);
    STAMP(1);
    const int pf = idx * 4 + w;
    const u32 l16 = (u32)((l >> 3) * 4096 + (l & 7) * 16), dump = lds_addr(smem) + K1_DUMP + w * 1024;
    if constexpr (K1_PF > 0) dma16(wsp_piece(wsp, xcd, pf), l16, dump);
    wait_vm<(K1_PF > 0 ? 1 : 0)>(); sbar();
    STAMP(2);
    f32x2 ln1[2];
    {
    const int rho = l & 15, q = l >> 4;
#pragma unroll
    for (int t = 0; t < 2; t++) ln1[t] = *(const f32x2*)(smem + K1_LN1 + w * 256 + (t * 16 + rho) * 8);
#pragma unroll
    for (int f = 0; f < 8; f++) wf[f] = k1_wf<7>(c, f);
    }
    __builtin_amdgcn_s_sleep(K1_BDELAY);
    SB();
    k1_z1loop<0>(c, xf, wf, acc, cs, ones, wsp, xcd, pf, l16, dump);
    mma_drain();
    const int l = lane_id(), rho = l & 15, q = l >> 4;
    if (rho == 0) *(f32x4*)(smem + K1_CS + 256 + w * 64 + q * 16) = cs;
    STAMP(3);
    quad_sync(lds + K1_CNT, l);
    STAMP(4);
    k1_act(acc, smem + K1_CS + 256 + q * 16, w, ln1);
#pragma unroll
    for (int t = 0; t < 2; t++)
#pragma unroll
      for (int cc = 0; cc < 2; cc++) {
        u32x2 a = {pk_bf16(acc[t][2 * cc].x, acc[t][2 * cc].y), pk_bf16(acc[t][2 * cc].z, acc[t][2 * cc].w)};
        u32x2 bb = {pk_bf16(acc[t][2 * cc + 1].x, acc[t][2 * cc + 1].y), pk_bf16(acc[t][2 * cc + 1].z, acc[t][2 * cc + 1].w)};
        const u32x4 o = (w & 1) ? (u32x4){bb.x, bb.y, a.x, a.y} : (u32x4){a.x, a.y, bb.x, bb.y};
        gstore16(ws, (u32)(WS_Z1F + ((panel * 8 + w * 2 + t) * 28 + sib * 2 + (cc ^ (w >> 1))) * 1024 + l * 16), o);
      }
    STAMP(5); STAMP_FLUSH(0);
    return;
  }
  const int grp = panel * 4 + w;
  u32 tch;
  {
    const int h = l >> 5, n = (w & 1) * 32 + (l & 31);
    (void)h;
    const int wl = panel * 56 + (n < 56 ? n : 55);
    const u32 xo = (u32)(panel * 128 * (DM * 2) + (sib * 64 + n) * 128);
    const u32 wo = (u32)((wl >> 1) * (FF * 2) + ((wl & 1) ? 0 : FH * 2) + sib * 128);
    gload4(tch, w < 2 ? (const void*)x : (const void*)win, w < 2 ? xo : wo);
  }
  u32x4 xs[2];
#pragma unroll
  for (int i = 0; i < 2; i++) gload16<0>(xs[i], x, (u32)((panel * 128 + w * 32 + i * 16 + (l >> 2)) * (DM * 2) + sib * 64 + (l & 3) * 16));
  u32 tag;
  asm volatile("s_load_dword %0, %1, 0x0\n\ts_waitcnt lgkmcnt(0)" : "=s"(tag) : "s"(ws + WS_EP + grp * 256) : "memory");
  tag += 1u;
  f32x4 rsum[2], gram[2];
  if (threadIdx.x < 2) *(u32*)(smem + K1_CNT + threadIdx.x * 4) = 0u;
  bar();
  STAMP(2);
  k1_xr(c, xf[0], 0);
#pragma unroll
  for (int f = 0; f < 8; f++) wf[f] = k1_wf<0>(c, f);
  k1_stage<0>(c, xf, wf, acc, cs, rsum, gram, ones);
  mma_drain();
  wait_vm<0>(); pin(xs[0]); pin(xs[1]);
  keep(tch);
  const int rho = l & 15, q = l >> 4, qq = (l >> 2) & 3, pp = l & 3;
  if (rho == 0) *(f32x4*)(smem + K1_CS + w * 64 + q * 16) = cs;
  STAMP(3);
  f32x2 ln1[2];
#pragma unroll
  for (int t = 0; t < 2; t++) {
    float d = (rho & 2) ? ((rho & 1) ? gram[t].w : gram[t].z) : ((rho & 1) ? gram[t].y : gram[t].x);
    d = ((rho >> 2) == q) ? d : 0.f;
    d = xsum32(xsum16(d));
    ln1[t] = ln_scalars(rsum[t].x, d, 1.f / DM);
    if (q == 0) *(f32x2*)(smem + K1_LN1 + w * 256 + (t * 16 + rho) * 8) = ln1[t];
  }
  bar();
  __builtin_amdgcn_s_setprio(K1_PRIO);
  STAMP(4);
  GAS u64* gr = (GAS u64*)(ws + WS_GR) + (size_t)grp * 14 * 64;
  {
    float st[2][2] = {{0.f, 0.f}, {0.f, 0.f}};
    k1_act(acc, smem + K1_CS + q * 16, w, ln1);
#pragma unroll
    for (int t = 0; t < 2; t++) {
      f32x2 s = {0.f, 0.f}, s2 = {0.f, 0.f};
#pragma unroll
      for (int p = 0; p < 4; p++) {
        s += acc[t][p].xy; s += acc[t][p].zw;
        s2 += acc[t][p].xy * acc[t][p].xy; s2 += acc[t][p].zw * acc[t][p].zw;
      }
      st[t][0] = s.x + s.y; st[t][1] = s2.x + s2.y;
    }
#pragma unroll
    for (int t = 0; t < 2; t++) { st[t][0] = xsum32(xsum16(st[t][0])); st[t][1] = xsum32(xsum16(st[t][1])); }
    const float v = (q & 2) ? ((q & 1) ? st[1][1] : st[0][1]) : ((q & 1) ? st[1][0] : st[0][0]);
    __hip_atomic_store(gr + sib * 64 + l, ((u64)tag << 32) | __float_as_uint(v), __ATOMIC_RELAXED, __HIP_MEMORY_SCOPE_AGENT);
  }
  STAMP(5);
#pragma unroll
  for (int i = 0; i < 2; i++) {
    const f32x2 lx = *(const f32x2*)(smem + K1_LN1 + w * 256 + (i * 16 + (l >> 2)) * 8);
    const float mr = -lx.x * lx.y;
    u16* ub = (u16*)(smem + K1_UT + (8 * (l & 3)) * 272 + (w * 32 + i * 16 + (l >> 2)) * 2);
#pragma unroll
    for (int j = 0; j < 4; j++) {
      const u32 o = pk_f16(fmaf(bflo(xs[i][j]), lx.y, mr), fmaf(bfhi(xs[i][j]), lx.y, mr));
      ub[(2 * j) * 136] = (u16)o;
      ub[(2 * j + 1) * 136] = (u16)(o >> 16);
    }
  }
  quad_sync(lds + K1_CNT + 4, l);
#pragma unroll
  for (int i = 0; i < 2; i++) {
    const u32x4 uo = *(const u32x4*)(smem + K1_UT + (w * 8 + i * 4 + q) * 272 + rho * 16);
    gstore16(ws, (u32)(WS_UT + (sib * 32 + w * 8 + i * 4 + q) * (SEQ * 2) + panel * 256 + rho * 16), uo);
  }
  STAMP(6);
  u64 rv[14];
#pragma unroll
  for (int i = 0; i < 14; i++) rv[i] = __hip_atomic_load(gr + i * 64 + l, __ATOMIC_RELAXED, __HIP_MEMORY_SCOPE_AGENT);
  STAMP(7);
  float tot;
  for (u32 spins = 0;; spins++) {
    bool ok = true; tot = 0.f;
#pragma unroll
    for (int i = 0; i < 14; i++) { ok &= (u32)(rv[i] >> 32) == tag; tot += __uint_as_float((u32)rv[i]); }
    if (__all(ok) || spins > (1u << 20)) break;
#pragma unroll
    for (int i = 0; i < 14; i++) rv[i] = __hip_atomic_load(gr + i * 64 + l, __ATOMIC_RELAXED, __HIP_MEMORY_SCOPE_AGENT);
  }
  STAMP(8);
  if (sib == 0 && l == 0) __hip_atomic_store((GAS u32*)(ws + WS_EP + grp * 256), tag, __ATOMIC_RELAXED, __HIP_MEMORY_SCOPE_AGENT);
  {
    auto bb = __builtin_amdgcn_permlane32_swap(__float_as_uint(tot), __float_as_uint(tot), false, false);
    const f32x2 lr = ln_scalars(__uint_as_float(bb[0]), __uint_as_float(bb[1]), 1.f / FH);
    const u32 nm = __float_as_uint(-lr.x * lr.y);
    auto lm = __builtin_amdgcn_permlane16_swap(nm, nm, false, false);
    auto ls = __builtin_amdgcn_permlane16_swap(__float_as_uint(lr.y), __float_as_uint(lr.y), false, false);
    char* const stg = smem + K1_X + w * 8192;
#pragma unroll
    for (int t = 0; t < 2; t++) {
      const float m2 = __uint_as_float(lm[t]), r2 = __uint_as_float(ls[t]);
#pragma unroll
      for (int p = 0; p < 4; p++) {
        const f32x4 a = acc[t][p] * r2 + m2;
        *(u32x2*)(stg + (16 * t + rho) * 136 + p * 32 + q * 8) = (u32x2){pk_bf16(a.x, a.y), pk_bf16(a.z, a.w)};
      }
    }
#pragma unroll
    for (int p = 0; p < 4; p++) {
      const char* rp = stg + (8 * q + qq) * 136 + p * 32 + pp * 8;
      u32x4 o = tr_frag(rp, rp + 4 * 136);
      const int ft = sib * 4 + (p ^ w);
      Z2STORE(ws, (u32)(WS_Z2F + (((ft / 7) * 64 + grp) * 7 + ft % 7) * 1024 + l * 16), o);
    }
  }
  STAMP(9); STAMP_FLUSH(0);
}
#define K1_ATTR
extern "C" __global__ K1_ATTR __launch_bounds__(512, 2) void k1(const u16* x, const u16* win, const u16* wsp, const u16* wout, char* ws, Extra ex) {
  __shared__ __attribute__((aligned(16))) char smem[K1_LDS];
  LST(0);
  u32 dself;
  {
    u64 c0;
    asm volatile("s_getpc_b64 %0" : "=s"(c0));
    const u32 t = threadIdx.x, o = ((t >> 6) * 16 + ((t & 63) < 16 ? (t & 63) : 15)) * 128u;
    gload4(dself, (const void*)c0, o);
  }
  k1_body(smem, x, win, wsp, wout, ws, ex);
  wait_vm<0>(); keep(dself);
}

#define K2_R 18
#define K2_PF 7
#define K2_LATE 4
#define K2_TD 0
constexpr bool k2_touch_at(int tau) { return K2_TD > 0 && tau % 14 == 0 && tau / 7 + K2_TD <= 14; }
constexpr int K2_EPI = 5, K2_EPI_AT = 112 - K2_R + 2, K2_PRO = 16 + K2_R + (K2_TD > 0 ? 2 : 0) + (K2_LATE > 0 ? 1 : 0), K2_XP = K2_LATE > 0 ? 1 : 0;
constexpr int k2_apiece(int tau) {
  int s = tau / 7, f = tau % 7;
  int a = (s + 3) / 2 + ((s & 1) ? 0 : 1) - ((s & 1) ? 0 : 1);
  if (s & 1) { a = (s + 3) / 2; return (a <= 7 && (f & 1) == 0) ? 8 * a + f / 2 : -1; }
  a = (s + 2) / 2;
  return (a >= 2 && a <= 7 && f < 4) ? 8 * a + 4 + f : -1;
}
constexpr int k2_ops_after(int tau) { return (tau + K2_R < 112 ? 1 : 0) + (k2_apiece(tau) >= 0 ? 1 : 0) + (tau == K2_EPI_AT ? K2_EPI + K2_PF : 0) + (k2_touch_at(tau) ? 1 : 0); }
constexpr int k2_issued(int tau) { int n = K2_PRO; for (int i = 0; i < tau; i++) n += k2_ops_after(i); return n; }
constexpr int k2_pos_B(int n) { return n < 14 ? 8 + n + 1 : (n < K2_R ? 16 + K2_XP + n + 1 : k2_issued(n - K2_R) + 1); }
constexpr int k2_pos_A(int a) { return a == 0 ? 8 : (a == 1 ? 30 + K2_XP : k2_issued(7 * (2 * a - 2) + 4)); }
constexpr int k2_wait(int tau) {
  int need = k2_pos_B(tau);
  int s = tau / 7, f = tau % 7;
  if ((s & 1) && f == 2 && (s + 1) / 2 <= 7) { int a = k2_pos_A((s + 1) / 2); need = a > need ? a : need; }
  return k2_issued(tau) - need;
}
#define K2_MFMA mfma_bf
struct K2G { const char* wsp0; const char* wsp; const char* z2; const char* z1; const char* bsp; const char* pf; u32 aoff[2]; u32 boff; u32 adst; u32 dump; u32 tb; int arot; int w; };
template <int P> DEV void k2_a1(const K2G& g) {
  constexpr int SS = P / 8;
  const int i = (P % 8) ^ g.arot;
  dma16(g.wsp + SS * 128 + (size_t)i * 8 * 4096, g.aoff[P & 1], g.adst + (SS & 1) * 8192 + ((i ^ (2 * g.w)) * 1024));
}
template <int A, int B> DEV void k2_as(const K2G& g) { if constexpr (A < B) { k2_a1<A>(g); k2_as<A + 1, B>(g); } }
template <int N> DEV void k2_b1(const K2G& g, u32x4 (&bf)[K2_R]) {
 gload16<(N % 8 - 4) * 1024>(bf[N % K2_R], g.z2 + (N / 8) * 8192, g.boff); }
template <int A, int B> DEV void k2_bs(const K2G& g, u32x4 (&bf)[K2_R]) { if constexpr (A < B) { k2_b1<A>(g, bf); k2_bs<A + 1, B>(g, bf); } }
template <int I> DEV u32x4 k2_read_a(const char* ap0, const char* ap1, int t) {
  return *(const u32x4*)(((I & 1) ? ap1 : ap0) + ((I >> 1) & 1) * 8192 + t * 2048);
}
template <int TAU>
DEV void k2_group(const K2G& g, u32x4 (&bf)[K2_R], u32x4 (&af)[2][4], const char* ap0, const char* ap1, f32x4 (&acc)[4][7], u32x4 (&ep)[4], u32& bw, u32 (&tch)[2]) {
  constexpr int S = TAU / 7, F = TAU % 7;
  wait_vm<k2_wait(TAU)>();
  pin(bf[TAU % K2_R]);
  if constexpr (S < 15 && F >= 2 && F < 6) af[(S + 1) & 1][F - 2] = k2_read_a<S + 1>(ap0, ap1, F - 2);
  SB();
#pragma unroll
  for (int t = 0; t < 4; t++) if (TAU < 98 || (t & 1)) acc[t][F] = K2_MFMA(bf[TAU % K2_R], af[S & 1][t], acc[t][F]);
  SB();
  if constexpr (TAU + K2_R < 112) k2_b1<TAU + K2_R>(g, bf);
  if constexpr (k2_apiece(TAU) >= 0) k2_a1<k2_apiece(TAU)>(g);
  if constexpr (k2_touch_at(TAU)) { keep(tch[(TAU / 14) & 1]); gload4(tch[(TAU / 14) & 1], g.z2 + (S + K2_TD) * 7168 - 4096, g.tb); }
  if constexpr (TAU == 111) { keep(tch[0]); keep(tch[1]); }
  if constexpr (TAU == K2_EPI_AT) {
    gload16<0>(ep[0], g.z1, g.boff); gload16<1024>(ep[1], g.z1, g.boff); gload16<2048>(ep[2], g.z1, g.boff); gload16<3072>(ep[3], g.z1, g.boff);
    gload4(bw, g.bsp, (g.boff >> 3) & 28u);
#pragma unroll
    for (int i = 0; i < K2_PF; i++) dma16(g.pf + i * 1024, g.boff, g.dump);
  }
  if constexpr (TAU < 111) k2_group<TAU + 1>(g, bf, af, ap0, ap1, acc, ep, bw, tch);
}

DEV void k2_gemm(char* smem, int b, const u16* wsp, const u16* bsp, const u16* wout, char* ws) {
  STAMP_DECL; STAMP(0);
  const int xcd = b & 7, idx = b >> 3;
  const int tp = (xcd >> 1) * 8 + (idx >> 2), fp = (xcd & 1) * 4 + (idx & 3);
  const int w = __builtin_amdgcn_readfirstlane(threadIdx.x >> 6), l = threadIdx.x & 63;
  const int rho = l & 15, q = l >> 4;
  const int tt = tp * 4 + w;
  char* const reg = smem + w * 16384;
  K2G g;
  g.wsp = (const char*)wsp + (size_t)tp * 64 * 4096 + w * 1024;
  g.wsp0 = (const char*)wsp + (size_t)tp * 64 * 4096 + w * 65536;
  g.z2 = ws + WS_Z2F + ((size_t)(fp * 64 + w * 16) * 7 + 4) * 1024;
  g.z1 = ws + WS_Z1F + ((size_t)tt * 28 + ((fp * 7) >> 1)) * 1024;
  g.bsp = (const char*)bsp + tt * 32;
  {
    int pc = (idx * 4 + w) * 7;
    pc = pc > 784 - 7 ? 784 - 7 : pc;
    g.pf = (const char*)wout + pc * 1024;
  }
  g.aoff[0] = (u32)((l >> 3) * 4096 + (((l & 7) ^ (l >> 4)) * 16));
  g.aoff[1] = (u32)((l >> 3) * 4096 + (((l & 7) ^ ((l >> 4) | 4)) * 16));
  g.boff = l * 16;
  g.adst = lds_addr(reg);
  g.dump = lds_addr(smem) + 65536 + w * 1024;
  g.w = w;
  g.arot = (idx & 3) * 2;
  u32x4 bf[K2_R], af[2][4], ep[4];
  u32 bw, tch[2];
  g.tb = (u32)(((idx >> 2) * 14 + (l < 14 ? l : 13)) * 128);
  tch[0] = tch[1] = 0u;
  k2_as<0, 8>(g); k2_bs<0, 14>(g, bf);
  u32 tl;
  gload4(tl, g.wsp, (u32)((fp * 8 + (l & 7)) * 4096 + (8 - K2_LATE + ((l >> 3) < K2_LATE ? (l >> 3) : K2_LATE - 1)) * 128));
  k2_as<8, 16>(g); k2_bs<14, K2_R>(g, bf);
  const char* ap0 = reg + rho * 128 + (((0 + q) ^ (rho >> 1)) * 16);
  const char* ap1 = reg + rho * 128 + (((4 + q) ^ (rho >> 1)) * 16);
  f32x4 acc[4][7] = {};
  STAMP(1);
  wait_vm<K2_PRO - 8>();
  STAMP(2);
#pragma unroll
  for (int t = 0; t < 4; t++) af[0][t] = k2_read_a<0>(ap0, ap1, t);
  k2_group<0>(g, bf, af, ap0, ap1, acc, ep, bw, tch);
  keep(tl);
#pragma unroll
  for (int i = 0; i < 14; i++) {
    *(f32x4*)(reg + i * 1024 + l * 16) = acc[i < 7 ? 3 : 1][i % 7];
    SB();
    acc[2][i % 7] = K2_MFMA(bf[(98 + i) % K2_R], af[i / 7][2], acc[2][i % 7]);
    acc[0][i % 7] = K2_MFMA(bf[(98 + i) % K2_R], af[i / 7][0], acc[0][i % 7]);
    SB();
  }
  STAMP(3);
  __builtin_amdgcn_s_setprio(2);
  wait_vm<K2_PF>();
#pragma unroll
  for (int i = 0; i < 4; i++) pin(ep[i]);
  keep(bw);
  bar();
  STAMP(4);
  char* const in1 = smem + (w ^ 1) * 16384;
#pragma unroll
  for (int f = 0; f < 7; f++) acc[2][f] += *(const f32x4*)(in1 + f * 1024 + l * 16);
#pragma unroll
  for (int f = 0; f < 7; f++) *(f32x4*)(in1 + f * 1024 + l * 16) = acc[2][f];
  const float bs = (rho & 1) ? bfhi(bw) : bflo(bw);
  const int odd = fp & 1;
  f32x4 zf[7];
#pragma unroll
  for (int f = 0; f < 7; f++) {
    acc[0][f] += *(const f32x4*)(in1 + (7 + f) * 1024 + l * 16) + bs;
    const u32x4 ze = ep[f >> 1], zo = ep[(f + 1) >> 1];
    const u32 za = odd ? ((f & 1) ? zo.x : zo.z) : ((f & 1) ? ze.z : ze.x);
    const u32 zb = odd ? ((f & 1) ? zo.y : zo.w) : ((f & 1) ? ze.w : ze.y);
    zf[f] = (f32x4){bflo(za), bfhi(za), bflo(zb), bfhi(zb)};
    asm volatile("" : "+v"(zf[f]), "+v"(acc[0][f]));
  }
  bar();
  STAMP(5);
  char* const in2 = smem + (w ^ 3) * 16384;
  u32x2 hv[8];
#pragma unroll
  for (int f = 0; f < 7; f++) {
    const f32x4 v = (acc[0][f] + *(const f32x4*)(in2 + f * 1024 + l * 16)) * zf[f];
    hv[f] = (u32x2){pk_bf16(v.x, v.y), pk_bf16(v.z, v.w)};
  }
  const u32 hb = (u32)(WS_HF + (tt * 28 + ((fp * 7) >> 1)) * 1024 + l * 16);
  if (odd) {
    gstore8(ws, hb + 8, hv[0]);
#pragma unroll
    for (int i = 0; i < 3; i++) gstore16(ws, hb + (i + 1) * 1024, (u32x4){hv[1 + 2 * i].x, hv[1 + 2 * i].y, hv[2 + 2 * i].x, hv[2 + 2 * i].y});
  } else {
#pragma unroll
    for (int i = 0; i < 3; i++) gstore16(ws, hb + i * 1024, (u32x4){hv[2 * i].x, hv[2 * i].y, hv[1 + 2 * i].x, hv[1 + 2 * i].y});
    gstore8(ws, hb + 3 * 1024, hv[6]);
  }
  STAMP(6); STAMP_FLUSH(1);
}

constexpr int KG_ROW = 272, KG_WAVE = 53 * KG_ROW;
constexpr int CV_CS = 4160, CV_T = 8 * CV_CS, CV_U = 2 * CV_T, CV_CMP = CV_U + 2 * 4608;
DEV f32x2 cexp_pow2(float gl, float fh, float fl, float p) {
  float m = __builtin_amdgcn_exp2f(gl * p);
  float r = __builtin_amdgcn_fractf(fh * p) + fl * p;
  return (f32x2){m * __builtin_amdgcn_cosf(r), m * __builtin_amdgcn_sinf(r)};
}
DEV float exp_acc(float x) {
  const float h = x * 1.44269504089f;
  const float lo = fmaf(x, 1.44269504089f, -h) + x * 1.925963e-8f;
  return __builtin_amdgcn_exp2f(h) * fmaf(lo, 0.69314718056f, 1.f);
}
struct KGen { f32x2 e1, e2, e4, e8, e16, e32, e64, e128, e256, e512, e1024, e2048, c0; };
DEV KGen kg_gens(float la, float ai, float cr, float ci, float dt, float sc) {
  const float zr = -rbf(exp_acc(la)) * dt, zi = ai * dt;
  const float gl = zr * 1.44269504089f;
  const float fh = zi * 0.15915494309f;
  const float fl = fmaf(zi, 0.15915494309f, -fh) + zi * 6.4206382e-9f;
  KGen k;
  k.c0 = (f32x2){cr * sc, ci * sc};
  k.e1 = cexp_pow2(gl, fh, fl, 1.f); k.e2 = csqr(k.e1); k.e4 = csqr(k.e2); k.e8 = csqr(k.e4);
  k.e16 = cexp_pow2(gl, fh, fl, 16.f); k.e32 = csqr(k.e16); k.e64 = csqr(k.e32); k.e128 = csqr(k.e64);
  k.e256 = cexp_pow2(gl, fh, fl, 256.f); k.e512 = csqr(k.e256); k.e1024 = csqr(k.e512); k.e2048 = csqr(k.e1024);
  return k;
}
DEV void kg_tables(const KGen& k, u32* tw) {
#define TW(row, v) tw[(row) * (KG_ROW / 4)] = (v)
  {
    f32x2 tb[16];
    tb[0] = (f32x2){128.f, 0.f};
    tb[1] = k.e8 * 128.f;
    tb[2] = k.e16 * 128.f; tb[3] = cmul(tb[1], k.e16);
    tb[4] = k.e32 * 128.f;
#pragma unroll
    for (int i = 1; i < 4; i++) tb[4 + i] = cmul(tb[i], k.e32);
    tb[8] = k.e64 * 128.f;
#pragma unroll
    for (int i = 1; i < 8; i++) tb[8 + i] = cmul(tb[i], k.e64);
    TW(0, pk_f16(128.f, 0.f));
    TW(16, pk_f16(k.e128.x * 128.f, k.e128.y * 128.f));
#pragma unroll
    for (int i = 1; i < 16; i++) { TW(i, pk_f16(tb[i].x, tb[i].y)); f32x2 u = cmul(tb[i], k.e128); TW(16 + i, pk_f16(u.x, u.y)); }
  }
  {
    f32x2 a[21];
    a[0] = k.c0; a[1] = cmul(k.c0, k.e1); a[2] = cmul(k.c0, k.e2); a[3] = cmul(a[1], k.e2);
#pragma unroll
    for (int i = 0; i < 4; i++) a[4 + i] = cmul(a[i], k.e4);
#pragma unroll
    for (int i = 0; i < 4; i++) a[8 + i] = cmul(a[2 * i], k.e256);
    a[12] = cmul(a[0], k.e512); a[13] = cmul(a[4], k.e512); a[14] = cmul(a[8], k.e512); a[15] = cmul(a[10], k.e512);
    a[16] = cmul(a[0], k.e1024); a[17] = cmul(a[8], k.e1024); a[18] = cmul(a[12], k.e1024); a[19] = cmul(a[14], k.e1024);
    a[20] = cmul(a[0], k.e2048);
#pragma unroll
    for (int i = 0; i < 21; i++) TW(32 + i, pk_f16(a[i].x, -a[i].y));
  }
#undef TW
}
template <int CTRL> DEV u32 dpp(u32 old, u32 src) { return __builtin_amdgcn_update_dpp(old, src, CTRL, 0xf, 0xf, false); }
template <int CTRL> DEV float dpp_add(float v) { return v + __uint_as_float(__builtin_amdgcn_update_dpp(0u, __float_as_uint(v), CTRL, 0xf, 0xf, true)); }
DEV void cv_frags(u32x4 (&A)[8], u32x4 (&B)[8], const char* pa, const char* pb) {
#pragma unroll
  for (int i = 0; i < 8; i++) {
    A[i] = *(const u32x4*)(pa - 512 * i);
    B[i] = *(const u32x4*)(pb + 512 * i);
  }
}
DEV void cv_mma(const u32x4 (&A)[8], const u32x4 (&B)[8], f32x4 (&acc)[8]) {
#pragma unroll
  for (int a = 0; a < 8; a++)
#pragma unroll
    for (int s = 7; s >= a; s--) acc[s] = mfma_h(A[a], B[s - a], acc[s]);
}
#define SSM_PRIO 0
DEV void k2_ssm(char* smem, int cb, const u16* lar, const u16* aim, const u16* cre, const u16* cim, const u16* ldt, const u16* dsk, char* ws) {
  STAMP_DECL; STAMP(0);
  __builtin_amdgcn_s_setprio(3);
  const int w = __builtin_amdgcn_readfirstlane(threadIdx.x >> 6), l = threadIdx.x & 63;
  const int ch = w >> 1, d = cb * 2 + ch, hw = w & 1, t = hw * 64 + l;
  const int rho = l & 15, q = l >> 4;
  const size_t o = (size_t)d * NMODE;
  const u32 vla = *(const u32*)(lar + o + hw * 128 + 2 * l), vai = *(const u32*)(aim + o + hw * 128 + 2 * l);
  const u32 vcr = *(const u32*)(cre + o + hw * 128 + 2 * l), vci = *(const u32*)(cim + o + hw * 128 + 2 * l);
  const u32 ocr = *(const u32*)(cre + o + (hw ^ 1) * 128 + 2 * l), oci = *(const u32*)(cim + o + (hw ^ 1) * 128 + 2 * l);
  const u32 vdt = ldt[d], vdk = dsk[d];
  char* const T = smem + ch * CV_T;
  char* const U = smem + CV_U + ch * 4608;
  u16* const cmp = (u16*)(smem + CV_CMP + ch * 1408);
#pragma unroll
  for (int i = 0; i < 2; i++) dma16(ws + WS_UT + (size_t)d * (SEQ * 2) + (hw * 2 + i) * 1024, l * 16, lds_addr(U) + 512 + (hw * 2 + i) * 1024);
  __builtin_amdgcn_s_setprio(SSM_PRIO);
  if (t < 32) *(u32x4*)(U + t * 16) = (u32x4){0u, 0u, 0u, 0u};
  u32 gi[2][8];
#pragma unroll
  for (int c = 0; c < 2; c++)
#pragma unroll
    for (int j = 0; j < 4; j++) {
      const int l0 = 2047 - (hw * 128 + c * 64 + l) * 8 - 2 * j, l1 = l0 - 1;
      const u32 bits = pk_bf16((float)l0, (float)l1);
      int i0 = (int)(bits & 0xffffu) - 0x4280, i1 = (int)(bits >> 16) - 0x4280;
      if (c == 1) { i0 = l0 < 128 ? l0 : i0; i1 = l1 < 128 ? l1 : i1; }
      gi[c][2 * j] = i0; gi[c][2 * j + 1] = i1;
    }
  const float dt = rbf(exp_acc(bf2f(vdt)));
  const float dk = bf2f(vdk);
  float S = fabsf(bflo(vcr)) + fabsf(bfhi(vcr)) + fabsf(bflo(vci)) + fabsf(bfhi(vci));
  float S2 = fabsf(bflo(ocr)) + fabsf(bfhi(ocr)) + fabsf(bflo(oci)) + fabsf(bfhi(oci));
  S = hw ? S2 + S : S + S2;
  S = dpp_add<0xb1>(S); S = dpp_add<0x4e>(S); S = dpp_add<0x141>(S); S = dpp_add<0x140>(S);
  S = xsum32(xsum16(S));
  int e = (int)((__float_as_uint(S) >> 23) & 255u) - 125;
  e = e < -100 ? -100 : (e > 100 ? 100 : e);
  e = __builtin_amdgcn_readfirstlane(e);
  const float sc = __uint_as_float((u32)(127 + 8 - e) << 23);
  const float ks = dt * __uint_as_float((u32)(127 + e - 14) << 23);
  STAMP(1);
  char* tab = smem + w * KG_WAVE;
  f32x16 kacc = {};
  {
    const int r = l & 31, h = l >> 5;
    const char* pa = tab + (32 + (r < 21 ? r : 20)) * KG_ROW + h * 16;
    const char* pb = tab + r * KG_ROW + h * 16;
    const KGen k0 = kg_gens(bflo(vla), bflo(vai), bflo(vcr), bflo(vci), dt, sc);
    STAMP(8);
    kg_tables(k0, (u32*)(tab + l * 4));
    SB();
    STAMP(9);
    u32x4 fa[8], fb[8];
#pragma unroll
    for (int kk = 0; kk < 8; kk++) { fa[kk] = *(const u32x4*)(pa + kk * 32); fb[kk] = *(const u32x4*)(pb + kk * 32); }
    SB();
    const KGen k1 = kg_gens(bfhi(vla), bfhi(vai), bfhi(vcr), bfhi(vci), dt, sc);
#pragma unroll
    for (int kk = 0; kk < 8; kk++) kacc = mfma32_h(fa[kk], fb[kk], kacc);
    STAMP(10);
    kg_tables(k1, (u32*)(tab + l * 4));
    STAMP(11);
#pragma unroll
    for (int kk = 0; kk < 8; kk++) kacc = mfma32_h(*(const u32x4*)(pa + kk * 32), *(const u32x4*)(pb + kk * 32), kacc);
    STAMP(12);
    float* give = (float*)tab + l;
    float keep[6];
#pragma unroll
    for (int i = 0; i < 6; i++) { give[i * 64] = hw ? kacc[i] : kacc[6 + i]; keep[i] = hw ? kacc[6 + i] : kacc[i]; }
    const int b1 = h ? 384 + 2 * r : 256 + 4 * r, b2 = h ? (r == 0 ? 640 : 672) : 512 + r;
    const int x0 = hw ? b1 + (h ? 64 : 2) : 4 * h + 8 * r;
    const int x1 = hw ? b2 : x0 + 2, d1 = hw ? (h ? (r == 0 ? 32 : 0) : 32) : 1;
    const int x2 = hw ? (h ? 672 : b2 + 64) : b1, d2 = hw ? (h ? 0 : 32) : 1;
    bar();
    const float* take = (const float*)(smem + (w ^ 1) * KG_WAVE) + l;
    const int xi[6] = {x0, x0 + 1, x1, x1 + d1, x2, x2 + d2};
#pragma unroll
    for (int i = 0; i < 6; i++) {
      const float o = take[i * 64];
      cmp[xi[i]] = __builtin_bit_cast(u16, (_Float16)(hw ? o + keep[i] : keep[i] + o));
    }
  }
  STAMP(2);
  bar();
  {
    u32 ra[3][4];
#pragma unroll
    for (int c = 0; c < 2; c++)
#pragma unroll
      for (int j = 0; j < 4; j++) ra[c][j] = (u32)cmp[gi[c][2 * j]] | ((u32)cmp[gi[c][2 * j + 1]] << 16);
    {
      const u32 k8 = hw ? 0u : cmp[512], k4 = hw ? 0u : cmp[511], k0 = hw ? 0u : cmp[510];
      ra[2][0] = k8 * 0x10001u; ra[2][1] = k4 * 0x10001u; ra[2][2] = k4 | (k0 << 16); ra[2][3] = k0 * 0x10001u;
    }
#pragma unroll
    for (int c = 0; c < 2; c++) {
      u32 dw[8], od[7];
#pragma unroll
      for (int j = 0; j < 4; j++) {
        dw[j] = ra[c][j];
        dw[4 + j] = dpp<0x130>(c == 0 ? dpp<0x134>(0u, ra[1][j]) : ra[2][j], ra[c][j]);
      }
#pragma unroll
      for (int i = 0; i < 7; i++) od[i] = __builtin_amdgcn_alignbit(dw[i + 1], dw[i], 16);
      char* tp = T + (hw * 128 + c * 64 + l) * 16;
#pragma unroll
      for (int k = 0; k < 4; k++) {
        *(u32x4*)(tp + (2 * k) * CV_CS) = (u32x4){dw[k], dw[k + 1], dw[k + 2], dw[k + 3]};
        *(u32x4*)(tp + (2 * k + 1) * CV_CS) = (u32x4){od[k], od[k + 1], od[k + 2], od[k + 3]};
      }
    }
    if (t < 8) *(u32x4*)(T + t * CV_CS + 4096) = (u32x4){0u, 0u, 0u, 0u};
  }
  wait_vm<0>();
  bar();
  STAMP(3);
  const char* pa = T + (7 - (rho & 7)) * CV_CS + (255 - (rho >> 3) + q) * 16 - 32 - 256 * hw;
  const char* pb = U + 512 + rho * 32 + q * 16 - 32 - 256 * hw;
  f32x4 acc[8] = {};
  {
    u32x4 A0[8], B0[8], A1[8], B1[8];
    cv_frags(A0, B0, pa, pb);
    SB(); cv_frags(A1, B1, pa - 64, pb - 64); SB();
    cv_mma(A0, B0, acc);
    SB(); cv_frags(A0, B0, pa - 128, pb - 128); SB();
    cv_mma(A1, B1, acc);
    SB(); cv_frags(A1, B1, pa - 192, pb - 192); SB();
    cv_mma(A0, B0, acc);
    cv_mma(A1, B1, acc);
  }
  STAMP(4);
  bar();
  {
    char* box = T + hw * 4096;
#pragma unroll
    for (int i = 0; i < 4; i++) {
      const f32x4 give = hw ? acc[i] : acc[4 + i];
      acc[i] = hw ? acc[4 + i] : acc[i];
      *(f32x4*)(box + i * 1024 + l * 16) = give;
    }
  }
  u32x2 ub[4];
#pragma unroll
  for (int i = 0; i < 4; i++) ub[i] = *(const u32x2*)(U + 512 + (256 * (4 * hw + i) + 16 * rho + 4 * q) * 2);
  bar();
  {
    const char* box = T + (hw ^ 1) * 4096;
    u32x2 yv[4];
#pragma unroll
    for (int i = 0; i < 4; i++) {
      f32x4 v = (acc[i] + *(const f32x4*)(box + i * 1024 + l * 16)) * ks;
      f16x2 u0 = as_h2(ub[i].x), u1 = as_h2(ub[i].y);
      yv[i] = (u32x2){pk_bf16(fmaf((float)u0.x, dk, v.x), fmaf((float)u0.y, dk, v.y)), pk_bf16(fmaf((float)u1.x, dk, v.z), fmaf((float)u1.y, dk, v.w))};
    }
#pragma unroll
    for (int i = 0; i < 4; i += 2) {
      auto sx = __builtin_amdgcn_permlane16_swap(yv[i].x, yv[i + 1].x, false, false);
      auto sy = __builtin_amdgcn_permlane16_swap(yv[i].y, yv[i + 1].y, false, false);
      const u32 off = (u32)(512 * (4 * hw + i + (q & 1)) + 32 * rho + 16 * (q >> 1));
      gstore16(ws, (u32)(WS_YT + d * (SEQ * 2)) + off, (u32x4){sx[0], sy[0], sx[1], sy[1]});
    }
  }
  STAMP(5); STAMP_FLUSH(1);
}

extern "C" __global__ NOPK __launch_bounds__(256, 2) void k2(const u16* wsp, const u16* bsp, const u16* wout, const u16* lar, const u16* aim, const u16* cre,
                                             const u16* cim, const u16* ldt, const u16* dsk, char* ws) {
  __shared__ __attribute__((aligned(16))) char smem[81920];
  LST(1);
  if (blockIdx.x < 256) k2_gemm(smem, blockIdx.x, wsp, bsp, wout, ws);
  else k2_ssm(smem, blockIdx.x - 256, lar, aim, cre, cim, ldt, dsk, ws);
}

#define K3_LEAD 3
struct K3G { const char* wo; const char* hf; u32 woff; u32 hoff[4]; u32 wdst; const char* yt; u32 yoff; u32 ydst; const char* xb; u32 xo; };
template <int I> DEV void k3_issue(const K3G& g, u32x4 (&hf)[7][4], u32x4 (&xv)[2]) {
  if constexpr (I < 7) {
#pragma unroll
    for (int i = 0; i < 4; i++) dma16(g.wo + (size_t)(I * 32 + i * 8) * (DM * 2), g.woff, g.wdst + (I * 4 + i) * 1024);
#pragma unroll
    for (int t = 0; t < 4; t++) gload16<0>(hf[I][t], g.hf + I * 1024, g.hoff[t]);
  } else if constexpr (I == 7) {
    dma16(g.yt, g.yoff, g.ydst);
    dma16(g.yt + 32 * (SEQ * 2), g.yoff, g.ydst + 1024);
    gload16<0>(xv[0], g.xb, g.xo);
    gload16<0>(xv[1], g.xb + 8 * (DM * 2), g.xo);
  }
}
constexpr int k3_cnt(int units) { return units <= 7 ? 8 * units : 60; }
template <int I> DEV void k3_step(const K3G& g, u32x4 (&hf)[7][4], u32x4 (&xv)[2], const char* const (&wp)[4], f32x4 (&acc)[4][4]) {
  wait_vm<k3_cnt(I + K3_LEAD) - 8 * (I + 1)>();
#pragma unroll
  for (int t = 0; t < 4; t++) pin(hf[I][t]);
  u32x4 wf[4];
#pragma unroll
  for (int p = 0; p < 4; p++) wf[p] = tr_frag(wp[p] + I * 4096, wp[p] + I * 4096 + 2048);
  k3_issue<I + K3_LEAD>(g, hf, xv);
#pragma unroll
  for (int p = 0; p < 4; p++)
#pragma unroll
    for (int t = 0; t < 4; t++) acc[t][p] = mfma_bf(wf[p], hf[I][t], acc[t][p]);
  if constexpr (I < 6) k3_step<I + 1>(g, hf, xv, wp, acc);
}
template <int A, int B> DEV void k3_pro(const K3G& g, u32x4 (&hf)[7][4], u32x4 (&xv)[2]) {
  if constexpr (A < B) { k3_issue<A>(g, hf, xv); k3_pro<A + 1, B>(g, hf, xv); }
}
extern "C" __global__ __launch_bounds__(256, 1) void k3(const u16* x, const u16* wout, u16* out, char* ws) {
  __shared__ __attribute__((aligned(16))) char smem[4 * 28672 + 8192];
  STAMP_DECL; STAMP(0);
  LST(2);
  const int b = blockIdx.x, xcd = b & 7, idx = b >> 3;
  const int tp = xcd * 4 + idx / 7, dp = idx % 7;
  const int w = __builtin_amdgcn_readfirstlane(threadIdx.x >> 6), l = threadIdx.x & 63;
  const int rho = l & 15, q = l >> 4, qq = (l >> 2) & 3, pp = l & 3;
  char* const reg = smem + w * 28672;
  char* const Y = smem + 4 * 28672;
  K3G g;
  g.wo = (const char*)wout + (size_t)(w * 224) * (DM * 2) + dp * 128;
  g.hf = ws + WS_HF + ((size_t)tp * 4 * 28 + w * 7) * 1024;
  g.woff = (u32)((l >> 3) * (DM * 2) + (((l & 7) ^ ((l >> 4) << 1)) * 16));
#pragma unroll
  for (int t = 0; t < 4; t++) g.hoff[t] = (u32)((t ^ w) * 28 * 1024 + l * 16);
  g.wdst = lds_addr(reg);
  g.yt = ws + WS_YT + (size_t)(dp * 64) * (SEQ * 2) + tp * 128 + w * 32;
  g.yoff = (u32)((l >> 1) * (SEQ * 2) + (l & 1) * 16);
  g.ydst = lds_addr(Y) + w * 2048;
  g.xo = (u32)((l >> 3) * (DM * 2) + (l & 7) * 16);
  const size_t xb = (size_t)(tp * 64 + w * 16) * (DM * 2) + dp * 128;
  g.xb = (const char*)x + xb;
  u32x4 hf[7][4], xv[2];
  k3_pro<0, K3_LEAD>(g, hf, xv);
  const char* wp[4];
  const int sg = ((q & 1) << 1) | (qq >> 1);
#pragma unroll
  for (int p = 0; p < 4; p++) wp[p] = reg + (4 * q + qq) * 128 + ((p ^ sg) * 32) + 8 * pp;
  f32x4 acc[4][4] = {};
  STAMP(1);
  k3_step<0>(g, hf, xv, wp, acc);
  STAMP(2);
#pragma unroll
  for (int t = 1; t < 4; t++)
#pragma unroll
    for (int p = 0; p < 4; p++) *(f32x4*)(reg + t * 4352 + rho * 272 + p * 64 + q * 16) = acc[t][p];
  wait_vm<0>();
  pin(xv[0]); pin(xv[1]);
#pragma unroll
  for (int p = 0; p < 4; p++) {
    u32x2 yv = tr_read(Y + w * 2048 + (16 * p + 4 * q + qq) * 32 + 8 * pp);
    *(f32x4*)(reg + rho * 272 + p * 64 + q * 16) = acc[0][p] + (f32x4){bflo(yv.x), bfhi(yv.x), bflo(yv.y), bfhi(yv.y)};
  }
  bar();
  STAMP(3);
#pragma unroll
  for (int i = 0; i < 2; i++) {
    f32x4 a = {bflo(xv[i].x), bfhi(xv[i].x), bflo(xv[i].y), bfhi(xv[i].y)}, c = {bflo(xv[i].z), bfhi(xv[i].z), bflo(xv[i].w), bfhi(xv[i].w)};
#pragma unroll
    for (int t = 0; t < 4; t++) {
      const char* rp = smem + (w ^ t) * 28672 + t * 4352 + (8 * i + (l >> 3)) * 272 + (l & 7) * 32;
      a += *(const f32x4*)(rp); c += *(const f32x4*)(rp + 16);
    }
    u32x4 o = {pk_bf16(a.x, a.y), pk_bf16(a.z, a.w), pk_bf16(c.x, c.y), pk_bf16(c.z, c.w)};
    gstore16(out, (u32)xb + i * 8 * (DM * 2) + g.xo, o);
  }
  STAMP(4); STAMP_FLUSH(2);
  LST(3);
  asm volatile("k3_code_end:");
}

#define K1_GRID 256
#define K2_GRID 480
extern "C" void kernel_launch(void* const* d_in, const int* in_sizes, int n_in, void* d_out, int out_size, void* d_ws, size_t ws_size,
                              hipStream_t stream) {
  const u16* x = (const u16*)d_in[0];
  char* ws = (char*)d_ws;
  Extra ex = {{(const char*)d_out, (const char*)d_in[1], (const char*)d_in[2], (const char*)d_in[3], (const char*)d_in[4], (const char*)d_in[5], (const char*)d_in[6], (const char*)d_in[10]}};
  k1<<<K1_GRID, 512, 0, stream>>>(x, (const u16*)d_in[8], (const u16*)d_in[9], (const u16*)d_in[11], ws, ex);
  k2<<<K2_GRID, 256, 0, stream>>>((const u16*)d_in[9], (const u16*)d_in[10], (const u16*)d_in[11], (const u16*)d_in[1], (const u16*)d_in[2],
                              (const u16*)d_in[3], (const u16*)d_in[4], (const u16*)d_in[5], (const u16*)d_in[6], ws);
  k3<<<224, 256, 0, stream>>>(x, (const u16*)d_in[11], (u16*)d_out, ws);
}
```

```cpp
#include <hip/hip_runtime.h>
#include <stdint.h>

typedef unsigned short u16;
typedef unsigned int u32;
typedef unsigned long long u64;
typedef __attribute__((ext_vector_type(4))) u32 u32x4;
typedef __attribute__((ext_vector_type(2))) u32 u32x2;
typedef __attribute__((ext_vector_type(4))) float f32x4;
typedef __attribute__((ext_vector_type(2))) float f32x2;
typedef __attribute__((ext_vector_type(16))) float f32x16;
typedef __attribute__((ext_vector_type(8))) __bf16 bf16x8;
typedef __attribute__((ext_vector_type(2))) __bf16 bf16x2;
typedef __attribute__((ext_vector_type(8))) _Float16 f16x8;
typedef __attribute__((ext_vector_type(2))) _Float16 f16x2;
typedef __attribute__((ext_vector_type(4))) short s16x4;
#define LDSAS __attribute__((address_space(3)))
#define GAS __attribute__((address_space(1)))
#define DEV __device__ __forceinline__
#define SB() __builtin_amdgcn_sched_barrier(0)

constexpr int SEQ = 2048, DM = 448, NMODE = 256, FF = 1792, FH = 896;

constexpr unsigned SPARE_WAIT = 750u;
constexpr unsigned G3_DELAY = 10;
constexpr unsigned G2_PRIO = 2;
constexpr unsigned POLL_SLEEP = 2;
constexpr unsigned K1_BDELAY = 10;
constexpr unsigned PF_STOP = 16000u;
constexpr int      K2_PF = 7;
constexpr int      K3_LEAD = 10;

constexpr size_t WS_Z2F = 0;
constexpr size_t WS_Z1F = 4u << 20;
constexpr size_t WS_HF  = 8u << 20;
constexpr size_t WS_UT  = 12u << 20;
constexpr size_t WS_YT  = 14u << 20;
constexpr size_t WS_GR  = 16u << 20;
constexpr size_t WS_EP  = 17u << 20;
constexpr size_t WS_FA  = WS_EP + 4096;
constexpr size_t WS_FU  = WS_FA + 8192;
constexpr size_t WS_FB  = WS_FU + 2048;
constexpr size_t WS_FH  = WS_FB + 2048;
constexpr size_t WS_FY  = WS_FH + 4096;

DEV float bf2f(u32 v) { return __uint_as_float(v << 16); }
DEV float bflo(u32 v) { return __uint_as_float(v << 16); }
DEV float bfhi(u32 v) { return __uint_as_float(v & 0xffff0000u); }
DEV u32 pk_bf16(float a, float b) { bf16x2 v = {(__bf16)a, (__bf16)b}; return __builtin_bit_cast(u32, v); }
DEV u32 pk_f16(float a, float b) { f16x2 v = {(_Float16)a, (_Float16)b}; return __builtin_bit_cast(u32, v); }
DEV f16x2 as_h2(u32 v) { return __builtin_bit_cast(f16x2, v); }
DEV float rbf(float a) { return __uint_as_float(pk_bf16(a, a) << 16); }
DEV f32x4 mfma_bf(u32x4 a, u32x4 b, f32x4 c) {
  return __builtin_amdgcn_mfma_f32_16x16x32_bf16(__builtin_bit_cast(bf16x8, a), __builtin_bit_cast(bf16x8, b), c, 0, 0, 0);
}
DEV f32x4 mfma_h(u32x4 a, u32x4 b, f32x4 c) {
  return __builtin_amdgcn_mfma_f32_16x16x32_f16(__builtin_bit_cast(f16x8, a), __builtin_bit_cast(f16x8, b), c, 0, 0, 0);
}
DEV f32x16 mfma32_h(u32x4 a, u32x4 b, f32x16 c) {
  return __builtin_amdgcn_mfma_f32_32x32x16_f16(__builtin_bit_cast(f16x8, a), __builtin_bit_cast(f16x8, b), c, 0, 0, 0);
}
DEV u32x2 tr_read(const char* p) {
  s16x4 v = __builtin_amdgcn_ds_read_tr16_b64_v4i16((LDSAS s16x4*)p);
  return __builtin_bit_cast(u32x2, v);
}
DEV u32x4 tr_frag(const char* p0, const char* p1) {
  u32x2 a = tr_read(p0), b = tr_read(p1);
  return (u32x4){a.x, a.y, b.x, b.y};
}
DEV u32 lds_addr(const void* p) { return (u32)(uintptr_t)(LDSAS const char*)p; }
DEV void dma16(const void* sbase, u32 voff, u32 ldst) {
  asm volatile("s_mov_b32 m0, %2\n\ts_nop 0\n\tglobal_load_lds_dwordx4 %0, %1" ::"v"(voff), "s"(sbase), "s"(ldst) : "memory");
}
template <int OFF> DEV void gload16(u32x4& d, const void* sbase, u32 voff) {
  asm volatile("global_load_dwordx4 %0, %1, %2 offset:%3" : "=v"(d) : "v"(voff), "s"(sbase), "i"(OFF) : "memory");
}
template <int OFF> DEV void gload16c(u32x4& d, const void* sbase, u32 voff) {
  asm volatile("global_load_dwordx4 %0, %1, %2 offset:%3 sc1" : "=v"(d) : "v"(voff), "s"(sbase), "i"(OFF) : "memory");
}
DEV void gstore16(void* sbase, u32 voff, u32x4 d) {
  asm volatile("global_store_dwordx4 %0, %1, %2 sc1\n\ts_nop 1" ::"v"(voff), "v"(d), "s"(sbase) : "memory");
}
DEV void gstore8(void* sbase, u32 voff, u32x2 d) {
  asm volatile("global_store_dwordx2 %0, %1, %2 sc1" ::"v"(voff), "v"(d), "s"(sbase) : "memory");
}
DEV void gload4(u32& d, const void* sbase, u32 voff) {
  asm volatile("global_load_dword %0, %1, %2" : "=v"(d) : "v"(voff), "s"(sbase) : "memory");
}
template <int N> DEV void wait_vm() { asm volatile("s_waitcnt vmcnt(%0)" ::"i"(N) : "memory"); }
DEV void touch(const void* sbase, u32 voff, u32 ldst) {
  asm volatile("s_mov_b32 m0, %2\n\ts_nop 0\n\tglobal_load_lds_dword %0, %1" ::"v"(voff), "s"(sbase), "s"(ldst) : "memory");
}
DEV void keep(u32& a) { asm volatile("" : "+v"(a)); }
DEV void pin(u32x4& a) { asm volatile("" : "+v"(a)); }
DEV void bar() { asm volatile("s_waitcnt lgkmcnt(0)\n\ts_barrier" ::: "memory"); }
DEV float xsum16(float v) { auto r = __builtin_amdgcn_permlane16_swap(__float_as_uint(v), __float_as_uint(v), false, false); return __uint_as_float(r[0]) + __uint_as_float(r[1]); }
DEV float xsum32(float v) { auto r = __builtin_amdgcn_permlane32_swap(__float_as_uint(v), __float_as_uint(v), false, false); return __uint_as_float(r[0]) + __uint_as_float(r[1]); }
DEV f32x2 ln_scalars(float sum, float sq, float inv_n) {
  float m = sum * inv_n;
  float v = fmaxf(sq * inv_n - m * m, 0.f);
  float sd = rbf(__builtin_amdgcn_sqrtf(rbf(rbf(v) + 1e-5f)));
  return (f32x2){rbf(m), __builtin_amdgcn_rcpf(sd)};
}
typedef LDSAS char* lptr;
DEV lptr pinned(lptr p) { asm volatile("" : "+v"(p)); return p; }
DEV u32x4 lread16(lptr p) { return *(LDSAS u32x4*)p; }
DEV u32x4 ltr_frag(lptr p0, lptr p1) {
  s16x4 a = __builtin_amdgcn_ds_read_tr16_b64_v4i16((LDSAS s16x4*)p0), b = __builtin_amdgcn_ds_read_tr16_b64_v4i16((LDSAS s16x4*)p1);
  u32x2 x = __builtin_bit_cast(u32x2, a), y = __builtin_bit_cast(u32x2, b);
  return (u32x4){x.x, x.y, y.x, y.y};
}
DEV void mma(f32x4& c, const u32x4& a, const u32x4& b) { asm volatile("v_mfma_f32_16x16x32_bf16 %0, %1, %2, %0" : "+v"(c) : "v"(a), "v"(b)); }
DEV void mma0(f32x4& c, const u32x4& a, const u32x4& b) { asm volatile("v_mfma_f32_16x16x32_bf16 %0, %1, %2, 0" : "=&v"(c) : "v"(a), "v"(b)); }
DEV void mma_drain() { SB(); asm volatile("s_nop 7\n\ts_nop 1" ::: "memory"); SB(); }
DEV void sbar() { asm volatile("s_barrier" ::: "memory"); }
DEV int lane_id() { int l = __builtin_amdgcn_mbcnt_hi(~0u, __builtin_amdgcn_mbcnt_lo(~0u, 0u)); asm volatile("" : "+v"(l)); return l; }
DEV u32 arrive(lptr cnt, int l) {
  asm volatile("s_waitcnt lgkmcnt(0)" ::: "memory");
  u32 old = 0;
  if (l == 0) old = __hip_atomic_fetch_add((LDSAS u32*)cnt, 1u, __ATOMIC_RELAXED, __HIP_MEMORY_SCOPE_WORKGROUP);
  return __builtin_amdgcn_readfirstlane(old);
}
template <int SLEEP = 0> DEV void await(lptr cnt, u32 n) {
  for (u32 spins = 0; __builtin_amdgcn_readfirstlane(*(volatile LDSAS u32*)cnt) < n && spins < (1u << 22); spins++) __builtin_amdgcn_s_sleep(SLEEP);
  asm volatile("" ::: "memory");
}
DEV void signal(lptr cnt, int l) {
  asm volatile("s_waitcnt lgkmcnt(0)" ::: "memory");
  if (l == 0) (void)__hip_atomic_fetch_add((LDSAS u32*)cnt, 1u, __ATOMIC_RELAXED, __HIP_MEMORY_SCOPE_WORKGROUP);
}
DEV void gsync(lptr cnt, int l, u32 n) { signal(cnt, l); await(cnt, n); }
DEV void await_asm(lptr cnt, u32 want) {
  u32 v, sv, n = 1u << 22;
  asm volatile("1:\n\tds_read_b32 %0, %3\n\ts_waitcnt lgkmcnt(0)\n\tv_readfirstlane_b32 %1, %0\n\ts_sub_u32 %2, %2, 1\n\ts_cbranch_scc1 2f\n\t"
               "s_cmp_lt_u32 %1, %4\n\ts_cbranch_scc1 1b\n2:" : "=&v"(v), "=&s"(sv), "+s"(n) : "v"(cnt), "s"(want) : "memory", "scc");
}
DEV void publish(lptr cnt, int l, u32 n, char* ws, u32 flag_off, u32 tag, int nl = 1) {
  wait_vm<0>();
  if (arrive(cnt, l) == n - 1 && l < nl) __hip_atomic_store((GAS u32*)(ws + flag_off), tag, __ATOMIC_RELAXED, __HIP_MEMORY_SCOPE_AGENT);
}
DEV u32 f1_off(int sib, int panel, int l) {
  const int fp = ((sib * 4 + (l & 1) * 3) * 37) >> 8;
  return (u32)(l < 8 ? WS_FA + (fp * 4 + (l >> 1)) * 256 + ((sib - ((fp * 7) >> 2)) * 16 + panel) * 4 : WS_FU + sib * 128 + panel * 4);
}
DEV void poll(char* ws, u32 flag_off, u32 tag) {
  for (u32 spins = 0; spins < (1u << 20); spins++) {
    const u32 v = __hip_atomic_load((GAS u32*)(ws + flag_off), __ATOMIC_RELAXED, __HIP_MEMORY_SCOPE_AGENT);
    if (__all(v == tag)) break;
    __builtin_amdgcn_s_sleep(POLL_SLEEP);
  }
  asm volatile("" ::: "memory");
}

constexpr int L_X = 73728;
constexpr int L_M = L_X + 65536;
constexpr int L_UT = L_M, L_STG = L_UT + 32 * 272, L_CS = L_STG + 4 * 2304, L_LN1 = L_CS + 512, L_END = 163840, L_CNT = L_END - 128, L_SCRAP = L_CNT - 256;
constexpr int L_U = L_M, L_CMP = L_U + 2 * 4608;
constexpr int L_W4 = L_M;
enum { C_CS1, C_UT, C_FA, C_FB, C_G2, C_R1, C_FH, C_FY, C_GU, C_G3, C_S0 };
DEV lptr cnt(lptr lds, int i) { return lds + L_CNT + i * 4; }

struct Args { const u16 *x, *win, *wsp, *bsp, *wout, *lar, *aim, *cre, *cim, *ldt, *dsk; u16* out; char* ws; };

constexpr int k1_slot(int u) { return u < 9 ? u : u - 9; }
DEV const char* wsp_piece(const u16* wsp, int xcd, int n) { return (const char*)wsp + (size_t)(xcd * 256 + (n & 31) * 8) * 4096 + ((n >> 5) & 3) * 1024 + (n >> 7) * 128; }
struct K1L { const char* win; const char* xb; u32 xoff[2]; u32 woff[2]; u32 wdst; u32 xdst; int m0; };
template <int U> DEV void k1_unit(const K1L& g) {
  int line = U % 7 + g.m0; line = line >= 7 ? line - 7 : line;
  if constexpr (U < 7) {
#pragma unroll
    for (int i = 0; i < 4; i++) dma16(g.xb + line * 128 + (i >> 1) * 16 * (DM * 2), g.xoff[i & 1], g.xdst + (U & 3) * 16384 + i * 1024);
  }
#pragma unroll
  for (int i = 0; i < 2; i++) dma16(g.win + (size_t)line * 64 * (FF * 2) + (U < 7 ? FH * 2 : 0), g.woff[i], g.wdst + k1_slot(U) * 8192 + i * 1024);
}
struct K1C { lptr str[4]; lptr wp[4]; };
DEV void k1_xr(const K1C& g, u32x4 (&xf)[4], int M) {
#pragma unroll
  for (int i = 0; i < 4; i++) xf[i] = lread16(g.str[i] + (M & 3) * 16384);
}
template <int U> DEV u32x4 k1_wf(const K1C& g, int f) {
  if constexpr (k1_slot(U) < 8) return ltr_frag(g.wp[f & 3] + k1_slot(U) * 8192 + (f >> 2) * 4096, g.wp[f & 3] + k1_slot(U) * 8192 + (f >> 2) * 4096 + 512);
  else { lptr p = pinned(g.wp[f & 3] + 65536); return ltr_frag(p + (f >> 2) * 4096, p + (f >> 2) * 4096 + 512); }
}
template <int U> DEV void k1_main(const K1C& g, const u32x4 (&xf)[4], u32x4 (&wf)[8], f32x4 (&acc)[2][4], f32x4& cs, const u32x4& ones) {
#pragma unroll
  for (int f = 0; f < 8; f++) {
    if (U % 7 == 0 && f < 4) {
      if (f == 0) mma0(cs, wf[f], ones);
      mma0(acc[0][f & 3], wf[f], xf[0]);
      mma0(acc[1][f & 3], wf[f], xf[1]);
    } else {
      if ((f & 3) == 0) mma(cs, wf[f], ones);
      mma(acc[0][f & 3], wf[f], xf[(f >> 2) * 2]);
      mma(acc[1][f & 3], wf[f], xf[(f >> 2) * 2 + 1]);
    }
    SB(); if constexpr (U % 7 < 6) wf[f] = k1_wf<U + 1>(g, f); SB();
  }
}
template <int M>
DEV void k1_stage(const K1C& g, u32x4 (&xf)[7][4], u32x4 (&wf)[8], f32x4 (&acc)[2][4], f32x4& cs, const u32x4& ones) {
  asm volatile("s_waitcnt lgkmcnt(15)\n\ts_barrier" ::: "memory");
  if constexpr (M < 6) k1_xr(g, xf[M + 1], M + 1);
  SB();
  k1_main<M>(g, xf[M], wf, acc, cs, ones);
  if constexpr (M < 6) k1_stage<M + 1>(g, xf, wf, acc, cs, ones);
}
template <bool FIRST> DEV void k1_stats(const u32x4 (&xf)[4], f32x4 (&rsum)[2], f32x4 (&gram)[2], const u32x4& ones) {
#pragma unroll
  for (int i = 0; i < 4; i++) {
    if (FIRST && i < 2) { mma0(rsum[i & 1], ones, xf[i]); mma0(gram[i & 1], xf[i], xf[i]); }
    else { mma(rsum[i & 1], ones, xf[i]); mma(gram[i & 1], xf[i], xf[i]); }
  }
}
template <int M> DEV void k1_z1loop(const K1C& g, u32x4 (&xf)[7][4], u32x4 (&wf)[8], f32x4 (&acc)[2][4], f32x4& cs, const u32x4& ones) {
  k1_main<M + 7>(g, xf[M], wf, acc, cs, ones);
  if constexpr (M < 6) k1_z1loop<M + 1>(g, xf, wf, acc, cs, ones);
}
DEV void k1_act(f32x4 (&acc)[2][4], const char* csb, int w, const f32x2 (&ln1)[2]) {
  float k1[2], k2[2], sd[2];
#pragma unroll
  for (int t = 0; t < 2; t++) {
    const float rs = ln1[t].y;
    k1[t] = -2.3022081983f * rs; k2[t] = -0.1029432396f * rs * rs * rs; sd[t] = __builtin_amdgcn_rcpf(rs);
  }
#pragma unroll
  for (int p = 0; p < 4; p++) {
    const f32x4 c = *(const f32x4*)(csb + (p ^ w) * 64);
#pragma unroll
    for (int t = 0; t < 2; t++) {
      const f32x4 a = acc[t][p] - c * ln1[t].x;
      const f32x4 u = a * (a * a * k2[t] + k1[t]);
      f32x4 e = {__builtin_amdgcn_exp2f(u.x), __builtin_amdgcn_exp2f(u.y), __builtin_amdgcn_exp2f(u.z), __builtin_amdgcn_exp2f(u.w)};
      e = e * sd[t] + sd[t];
      acc[t][p] = a * (f32x4){__builtin_amdgcn_rcpf(e.x), __builtin_amdgcn_rcpf(e.y), __builtin_amdgcn_rcpf(e.z), __builtin_amdgcn_rcpf(e.w)};
    }
  }
}
DEV void lbar0() { asm volatile("s_waitcnt vmcnt(6) lgkmcnt(0)\n\ts_barrier" ::: "memory"); }
DEV K1C k1_addr(lptr lds, int w, int l) {
  K1C c;
  const int rho = l & 15, q = l >> 4, qq = (l >> 2) & 3, pp = l & 3;
#pragma unroll
  for (int i = 0; i < 4; i++) c.str[i] = pinned(lds + L_X + w * 4096 + (i & 1) * 2048 + rho * 128 + (((4 * (i >> 1) + q) ^ (rho >> 1)) * 16));
  const int sg = (qq >> 1) | ((q & 1) << 1);
#pragma unroll
  for (int f = 0; f < 4; f++) c.wp[f] = lds + (8 * q + qq) * 128 + (((f ^ w) ^ sg) * 32) + 8 * pp;
  return c;
}

constexpr int K2_R = 18, K2_EPI = 5 + K2_PF, K2_EPI_AT = 112 - K2_R + 2;
constexpr int k2_apiece(int tau) {
  int s = tau / 7, f = tau % 7;
  if (s & 1) { int a = (s + 3) / 2; return (a <= 7 && (f & 1) == 0) ? 8 * a + f / 2 : -1; }
  int a = (s + 2) / 2;
  return (a >= 2 && a <= 7 && f < 4) ? 8 * a + 4 + f : -1;
}
constexpr int k2_ops_after(int tau) { return (tau + K2_R < 112 ? 1 : 0) + (k2_apiece(tau) >= 0 ? 1 : 0) + (tau == K2_EPI_AT ? K2_EPI : 0); }
constexpr int k2_issued(int tau) { int n = K2_R; for (int i = 0; i < tau; i++) n += k2_ops_after(i); return n; }
constexpr int k2_pos_B(int n) { return n < K2_R ? n + 1 : k2_issued(n - K2_R) + 1; }
constexpr int k2_pos_A(int a) { return a < 2 ? 0 : k2_issued(7 * (2 * a - 2) + 4); }
constexpr int k2_wait(int tau) {
  int need = k2_pos_B(tau);
  int s = tau / 7, f = tau % 7;
  if ((s & 1) && f == 2 && (s + 1) / 2 <= 7) { int a = k2_pos_A((s + 1) / 2); need = a > need ? a : need; }
  return k2_issued(tau) - need;
}
struct K2G { lptr gz; u32 gn; const char* pf; u32 dump; const char* wsp; const char* z2; const char* z1; const char* bsp; u32 aoff[2]; u32 boff; u32 adst; int w; };
template <int P> DEV void k2_a1(const K2G& g) {
  constexpr int SS = P / 8, i = P % 8;
  dma16(g.wsp + SS * 128 + (size_t)i * 8 * 4096, g.aoff[i & 1], g.adst + (SS & 1) * 8192 + ((i ^ (2 * g.w)) * 1024));
}
template <int A, int B> DEV void k2_as(const K2G& g) { if constexpr (A < B) { k2_a1<A>(g); k2_as<A + 1, B>(g); } }
template <int N> DEV void k2_b1(const K2G& g, u32x4 (&bf)[K2_R]) { gload16c<(N % 8 - 4) * 1024>(bf[N % K2_R], g.z2 + (N / 8) * 8192, g.boff); }
template <int A, int B> DEV void k2_bs(const K2G& g, u32x4 (&bf)[K2_R]) { if constexpr (A < B) { k2_b1<A>(g, bf); k2_bs<A + 1, B>(g, bf); } }
template <int I> DEV u32x4 k2_read_a(lptr ap0, lptr ap1, int t) { return lread16(((I & 1) ? ap1 : ap0) + ((I >> 1) & 1) * 8192 + t * 2048); }
template <int TAU>
DEV void k2_group(const K2G& g, u32x4 (&bf)[K2_R], u32x4 (&af)[2][4], lptr ap0, lptr ap1, f32x4 (&acc)[4][7], u32x4 (&ep)[4], u32& bw) {
  constexpr int S = TAU / 7, F = TAU % 7;
  wait_vm<k2_wait(TAU)>();
  pin(bf[TAU % K2_R]);
  if constexpr (S < 15 && F >= 2 && F < 6) af[(S + 1) & 1][F - 2] = k2_read_a<S + 1>(ap0, ap1, F - 2);
  SB();
#pragma unroll
  for (int t = 0; t < 4; t++) if (TAU < 98 || t) acc[t][F] = mfma_bf(bf[TAU % K2_R], af[S & 1][t], acc[t][F]);
  SB();
  if constexpr (TAU + K2_R < 112) k2_b1<TAU + K2_R>(g, bf);
  if constexpr (k2_apiece(TAU) >= 0) k2_a1<k2_apiece(TAU)>(g);
  if constexpr (TAU == K2_EPI_AT) {
    await_asm(g.gz, g.gn);
    gload16c<0>(ep[0], g.z1, g.boff); gload16c<1024>(ep[1], g.z1, g.boff); gload16c<2048>(ep[2], g.z1, g.boff); gload16c<3072>(ep[3], g.z1, g.boff);
    gload4(bw, g.bsp, (g.boff >> 3) & 28u);
#pragma unroll
    for (int i = 0; i < K2_PF; i++) dma16(g.pf + i * 1024, g.boff, g.dump);
  }
  if constexpr (TAU < 111) k2_group<TAU + 1>(g, bf, af, ap0, ap1, acc, ep, bw);
}
DEV K2G k2_setup(const Args& a, char* smem, int xcd, int idx, int w, int l) {
  const int tp = (xcd >> 1) * 8 + (idx >> 2), fp = (xcd & 1) * 4 + (idx & 3), tt = tp * 4 + w;
  K2G g;
  g.wsp = (const char*)a.wsp + (size_t)tp * 64 * 4096 + w * 1024;
  g.z2 = a.ws + WS_Z2F + ((size_t)(fp * 64 + w * 16) * 7 + 4) * 1024;
  g.z1 = a.ws + WS_Z1F + ((size_t)tt * 28 + ((fp * 7) >> 1)) * 1024;
  g.bsp = (const char*)a.bsp + tt * 32;
  g.aoff[0] = (u32)((l >> 3) * 4096 + (((l & 7) ^ (l >> 4)) * 16));
  g.aoff[1] = (u32)((l >> 3) * 4096 + (((l & 7) ^ ((l >> 4) | 4)) * 16));
  g.boff = l * 16;
  g.adst = lds_addr(smem) + L_X + w * 16384;
  {
    int pc = (idx * 4 + w) * 7;
    pc = pc > 784 - 7 ? 784 - 7 : pc;
    g.pf = (const char*)a.wout + pc * 1024;
    g.dump = lds_addr(smem) + L_CMP + 13 * 1024;
  }
  g.w = w;
  g.gz = cnt((lptr)smem, C_GU);
  g.gn = idx < 28 ? 4u : 1u;
  return g;
}
DEV char* k2_box(char* smem, int w, int i) {
  if (i < 16) return smem + L_X + w * 16384 + i * 1024;
  const int e = w * 5 + i - 16;
  return smem + (e < 7 ? 66560 + e * 1024 : L_CMP + (e - 7) * 1024);
}
DEV void k2_gemm(const Args& a, char* smem, int xcd, int idx, int w, u32 tag) {
  const int l = lane_id(), rho = l & 15, q = l >> 4;
  const int tp = (xcd >> 1) * 8 + (idx >> 2), fp = (xcd & 1) * 4 + (idx & 3), tt = tp * 4 + w;
  char* const ws = a.ws;
  const lptr lds = (lptr)smem;
  const K2G g = k2_setup(a, smem, xcd, idx, w, l);
  touch(g.wsp, (u32)((fp * 8 + (l & 7)) * 4096 + (2 + ((l >> 3) < 6 ? (l >> 3) : 5)) * 128), lds_addr(smem) + L_SCRAP);
  wait_vm<0>();
  if (w == 0) {
    const int ns = (((fp * 7 + 6) >> 2) - ((fp * 7) >> 2) + 1) * 16;
    poll(ws, l < 48 ? (u32)(WS_FA + (fp * 4 + (xcd >> 1)) * 256 + (l < ns ? l : ns - 1) * 4) : (u32)(WS_FU + (idx < 28 ? (idx * 8 + xcd) >> 4 : 0) * 128 + (l - 48) * 4), tag);
    signal(cnt(lds, C_G2), l);
  } else {
    await<1>(cnt(lds, C_G2), 1);
  }
  __builtin_amdgcn_s_setprio(G2_PRIO);
  u32x4 bf[K2_R], af[2][4], ep[4];
  u32 bw;
  k2_bs<0, K2_R>(g, bf);
  const lptr ap0 = pinned(lds + L_X + w * 16384 + rho * 128 + (((0 + q) ^ (rho >> 1)) * 16));
  const lptr ap1 = pinned(lds + L_X + w * 16384 + rho * 128 + (((4 + q) ^ (rho >> 1)) * 16));
  f32x4 acc[4][7] = {};
#pragma unroll
  for (int t = 0; t < 4; t++) af[0][t] = k2_read_a<0>(ap0, ap1, t);
  k2_group<0>(g, bf, af, ap0, ap1, acc, ep, bw);
  wait_vm<K2_PF>();
#pragma unroll
  for (int i = 0; i < 4; i++) pin(ep[i]);
  keep(bw);
  const int odd = fp & 1;
  f32x4 zf[7];
#pragma unroll
  for (int i = 0; i < 21; i++) {
    *(f32x4*)(k2_box(smem, w, i) + l * 16) = acc[1 + i / 7][i % 7];
    SB();
    if (i < 14) acc[0][i % 7] = mfma_bf(bf[(98 + i) % K2_R], af[i / 7][0], acc[0][i % 7]);
    else {
      const int f = i - 14;
      const u32x4 ze = ep[f >> 1], zo = ep[(f + 1) >> 1];
      const u32 za = odd ? ((f & 1) ? zo.x : zo.z) : ((f & 1) ? ze.z : ze.x);
      const u32 zb = odd ? ((f & 1) ? zo.y : zo.w) : ((f & 1) ? ze.w : ze.y);
      zf[f] = (f32x4){bflo(za), bfhi(za), bflo(zb), bfhi(zb)};
      asm volatile("" : "+v"(zf[f]));
    }
    SB();
  }
  signal(cnt(lds, C_R1), l);
  const float bs = (rho & 1) ? bfhi(bw) : bflo(bw);
#pragma unroll
  for (int f = 0; f < 7; f++) { acc[0][f] += bs; asm volatile("" : "+v"(acc[0][f])); }
  await(cnt(lds, C_R1), 4);
  u32x2 hv[8];
#pragma unroll
  for (int f = 0; f < 7; f++) {
    const f32x4 p1 = *(const f32x4*)(k2_box(smem, w ^ 1, f) + l * 16), p2 = *(const f32x4*)(k2_box(smem, w ^ 2, 7 + f) + l * 16);
    const f32x4 p3 = *(const f32x4*)(k2_box(smem, w ^ 3, 14 + f) + l * 16);
    const f32x4 v = ((acc[0][f] + p1) + (p2 + p3)) * zf[f];
    hv[f] = (u32x2){pk_bf16(v.x, v.y), pk_bf16(v.z, v.w)};
  }
  const u32 hb = (u32)(WS_HF + (tt * 28 + ((fp * 7) >> 1)) * 1024 + l * 16);
  if (odd) {
    gstore8(ws, hb + 8, hv[0]);
#pragma unroll
    for (int i = 0; i < 3; i++) gstore16(ws, hb + (i + 1) * 1024, (u32x4){hv[1 + 2 * i].x, hv[1 + 2 * i].y, hv[2 + 2 * i].x, hv[2 + 2 * i].y});
  } else {
#pragma unroll
    for (int i = 0; i < 3; i++) gstore16(ws, hb + i * 1024, (u32x4){hv[2 * i].x, hv[2 * i].y, hv[1 + 2 * i].x, hv[1 + 2 * i].y});
    gstore8(ws, hb + 3 * 1024, hv[6]);
  }
  publish(cnt(lds, C_FH), l, 4, ws, (u32)(WS_FH + tp * 128 + fp * 4), tag);
}

constexpr int KG_ROW = 272, KG_WAVE = 53 * KG_ROW;
constexpr int CV_CS = 4160, CV_T = 8 * CV_CS;
DEV f32x2 cmul(f32x2 a, f32x2 b) {
  float re, im;
  asm("v_mul_f32 %0, %3, %5\n\tv_mul_f32 %1, %3, %4\n\tv_fma_f32 %0, %2, %4, -%0\n\tv_fmac_f32 %1, %2, %5" : "=&v"(re), "=&v"(im) : "v"(a.x), "v"(a.y), "v"(b.x), "v"(b.y));
  return (f32x2){re, im};
}
DEV f32x2 csqr(f32x2 a) { return cmul(a, a); }
DEV u32 cmul_h(f32x2 a, f32x2 b) {
  float t; u32 o;
  asm("v_mul_f32 %1, %3, %5\n\tv_fma_mixlo_f16 %0, %2, %4, -%1\n\tv_mul_f32 %1, %3, %4\n\tv_fma_mixhi_f16 %0, %2, %5, %1" : "=&v"(o), "=&v"(t) : "v"(a.x), "v"(a.y), "v"(b.x), "v"(b.y));
  return o;
}
DEV u32 cmul_hc(f32x2 a, f32x2 b) {
  float t; u32 o;
  asm("v_mul_f32 %1, %3, %5\n\tv_fma_mixlo_f16 %0, %2, %4, -%1\n\tv_mul_f32 %1, %3, %4\n\tv_fma_mixhi_f16 %0, -%2, %5, -%1" : "=&v"(o), "=&v"(t) : "v"(a.x), "v"(a.y), "v"(b.x), "v"(b.y));
  return o;
}
DEV f32x2 cexp_pow2(float gl, float fh, float fl, float p) {
  float m = __builtin_amdgcn_exp2f(gl * p);
  float r = __builtin_amdgcn_fractf(fh * p) + fl * p;
  return (f32x2){m * __builtin_amdgcn_cosf(r), m * __builtin_amdgcn_sinf(r)};
}
DEV float exp_acc(float x) {
  const float h = x * 1.44269504089f;
  const float lo = fmaf(x, 1.44269504089f, -h) + x * 1.925963e-8f;
  return __builtin_amdgcn_exp2f(h) * fmaf(lo, 0.69314718056f, 1.f);
}
struct KGen { f32x2 e1, e2, e4, e8, e16, e32, e64, e128, e256, e512, e1024, e2048, c0; };
DEV KGen kg_gens(float la, float ai, float cr, float ci, float dt, float sc) {
  const float zr = -rbf(exp_acc(la)) * dt, zi = ai * dt;
  const float gl = zr * 1.44269504089f;
  const float fh = zi * 0.15915494309f;
  const float fl = fmaf(zi, 0.15915494309f, -fh) + zi * 6.4206382e-9f;
  KGen k;
  k.c0 = (f32x2){cr * sc, ci * sc};
  k.e1 = cexp_pow2(gl, fh, fl, 1.f); k.e16 = cexp_pow2(gl, fh, fl, 16.f); k.e256 = cexp_pow2(gl, fh, fl, 256.f);
  k.e2 = csqr(k.e1); k.e32 = csqr(k.e16); k.e512 = csqr(k.e256);
  k.e4 = csqr(k.e2); k.e64 = csqr(k.e32); k.e1024 = csqr(k.e512);
  k.e8 = csqr(k.e4); k.e128 = csqr(k.e64); k.e2048 = csqr(k.e1024);
  return k;
}
DEV void kg_tables(const KGen& k, u32* tw) {
#define TW(row, v) tw[(row) * (KG_ROW / 4)] = (v)
  {
    f32x2 tb[16];
    tb[1] = k.e8; tb[2] = k.e16; tb[3] = cmul(k.e8, k.e16); tb[4] = k.e32;
#pragma unroll
    for (int i = 1; i < 4; i++) tb[4 + i] = cmul(tb[i], k.e32);
    tb[8] = k.e64;
#pragma unroll
    for (int i = 1; i < 8; i++) tb[8 + i] = cmul(tb[i], k.e64);
    TW(0, 0x00003c00u);
    TW(16, pk_f16(k.e128.x, k.e128.y));
#pragma unroll
    for (int i = 1; i < 16; i++) { TW(i, pk_f16(tb[i].x, tb[i].y)); TW(16 + i, cmul_h(tb[i], k.e128)); }
  }
  {
    f32x2 a0 = k.c0, a1 = cmul(a0, k.e1), a2 = cmul(a0, k.e2), a3 = cmul(a1, k.e2), a4 = cmul(a0, k.e4), a6 = cmul(a2, k.e4);
    f32x2 a8 = cmul(a0, k.e256), a10 = cmul(a4, k.e256), a12 = cmul(a0, k.e512), a14 = cmul(a8, k.e512);
    TW(32, pk_f16(a0.x, -a0.y)); TW(33, pk_f16(a1.x, -a1.y)); TW(34, pk_f16(a2.x, -a2.y)); TW(35, pk_f16(a3.x, -a3.y));
    TW(36, pk_f16(a4.x, -a4.y)); TW(37, cmul_hc(a1, k.e4)); TW(38, pk_f16(a6.x, -a6.y)); TW(39, cmul_hc(a3, k.e4));
    TW(40, pk_f16(a8.x, -a8.y)); TW(41, cmul_hc(a2, k.e256)); TW(42, pk_f16(a10.x, -a10.y)); TW(43, cmul_hc(a6, k.e256));
    TW(44, pk_f16(a12.x, -a12.y)); TW(45, cmul_hc(a4, k.e512)); TW(46, pk_f16(a14.x, -a14.y)); TW(47, cmul_hc(a10, k.e512));
    TW(48, cmul_hc(a0, k.e1024)); TW(49, cmul_hc(a8, k.e1024)); TW(50, cmul_hc(a12, k.e1024)); TW(51, cmul_hc(a14, k.e1024));
    TW(52, cmul_hc(a0, k.e2048));
  }
#undef TW
}
template <int CTRL> DEV u32 dpp(u32 old, u32 src) { return __builtin_amdgcn_update_dpp(old, src, CTRL, 0xf, 0xf, false); }
template <int CTRL> DEV float dpp_add(float v) { return v + __uint_as_float(__builtin_amdgcn_update_dpp(0u, __float_as_uint(v), CTRL, 0xf, 0xf, true)); }
DEV void cv_frags(u32x4 (&A)[8], u32x4 (&B)[8], const char* pa, const char* pb) {
#pragma unroll
  for (int i = 0; i < 8; i++) {
    A[i] = *(const u32x4*)(pa - 512 * i);
    B[i] = *(const u32x4*)(pb + 512 * i);
  }
}
DEV void cv_mma(const u32x4 (&A)[8], const u32x4 (&B)[8], f32x4 (&acc)[8]) {
#pragma unroll
  for (int a = 0; a < 8; a++)
#pragma unroll
    for (int s = 7; s >= a; s--) acc[s] = mfma_h(A[a], B[s - a], acc[s]);
}
DEV u32 fb_off(int xcd, int idx, int i) {
  const int tp = (xcd >> 1) * 8 + (idx >> 2), fp = (xcd & 1) * 4 + (idx & 3), s1 = (fp * 7 + 6) >> 2;
  int sb = ((fp * 7) >> 2) + i; sb = sb > s1 ? s1 : sb;
  return (u32)(WS_FB + (tp >> 1) * 128 + sb * 4);
}
struct SsmIn { u32 vla, vai, vcr, vci, ocr, oci, vdt, vdk; };
DEV SsmIn ssm_load(const Args& a, int cb, int w, int l) {
  const int d = cb * 2 + (w >> 1), hw = w & 1;
  const size_t o = (size_t)d * NMODE;
  SsmIn p;
  p.vla = *(const u32*)(a.lar + o + hw * 128 + 2 * l); p.vai = *(const u32*)(a.aim + o + hw * 128 + 2 * l);
  p.vcr = *(const u32*)(a.cre + o + hw * 128 + 2 * l); p.vci = *(const u32*)(a.cim + o + hw * 128 + 2 * l);
  p.ocr = *(const u32*)(a.cre + o + (hw ^ 1) * 128 + 2 * l); p.oci = *(const u32*)(a.cim + o + (hw ^ 1) * 128 + 2 * l);
  p.vdt = a.ldt[d]; p.vdk = a.dsk[d];
  return p;
}
struct SsmPre { KGen k0, k1; float ks, dk; };
DEV SsmPre ssm_pre(const SsmIn& in, int w) {
  const int hw = w & 1;
  SsmPre o;
  const float dt = rbf(exp_acc(bf2f(in.vdt)));
  o.dk = bf2f(in.vdk);
  float S = fabsf(bflo(in.vcr)) + fabsf(bfhi(in.vcr)) + fabsf(bflo(in.vci)) + fabsf(bfhi(in.vci));
  float S2 = fabsf(bflo(in.ocr)) + fabsf(bfhi(in.ocr)) + fabsf(bflo(in.oci)) + fabsf(bfhi(in.oci));
  S = hw ? S2 + S : S + S2;
  S = dpp_add<0xb1>(S); S = dpp_add<0x4e>(S); S = dpp_add<0x141>(S); S = dpp_add<0x140>(S);
  S = xsum32(xsum16(S));
  int e = (int)((__float_as_uint(S) >> 23) & 255u) - 125;
  e = e < -100 ? -100 : (e > 100 ? 100 : e);
  e = __builtin_amdgcn_readfirstlane(e);
  const float sc = __uint_as_float((u32)(127 + 15 - e) << 23);
  o.ks = dt * __uint_as_float((u32)(127 + e - 14) << 23);
  o.k0 = kg_gens(bflo(in.vla), bflo(in.vai), bflo(in.vcr), bflo(in.vci), dt, sc);
  o.k1 = kg_gens(bfhi(in.vla), bfhi(in.vai), bfhi(in.vcr), bfhi(in.vci), dt, sc);
  return o;
}
DEV void k2_ssm(const Args& a, const SsmPre& in, char* smem, int cb, int w, u32 tag) {
  const int l = lane_id();
  const int ch = w >> 1, d = cb * 2 + ch, hw = w & 1, t = hw * 64 + l;
  const int rho = l & 15, q = l >> 4;
  char* const ws = a.ws;
  const lptr lds = (lptr)smem;
  const lptr pc = cnt(lds, C_S0 + ch * 5);
  char* const T = smem + ch * CV_T;
  char* const U = smem + L_U + ch * 4608;
  u16* const cmp = (u16*)(smem + L_CMP + ch * 1408);
  if (t < 32) *(u32x4*)(U + t * 16) = (u32x4){0u, 0u, 0u, 0u};
  const float ks = in.ks, dk = in.dk;
  char* tab = smem + w * KG_WAVE;
  f32x16 kacc = {};
  u32x4 uu[2];
  {
    const int r = l & 31, h = l >> 5;
    const char* pa = tab + (32 + (r < 21 ? r : 20)) * KG_ROW + h * 16;
    const char* pb = tab + r * KG_ROW + h * 16;
    kg_tables(in.k0, (u32*)(tab + l * 4));
    SB();
    u32x4 fa[8], fb[8];
#pragma unroll
    for (int kk = 0; kk < 8; kk++) { fa[kk] = *(const u32x4*)(pa + kk * 32); fb[kk] = *(const u32x4*)(pb + kk * 32); }
    SB();
#pragma unroll
    for (int kk = 0; kk < 8; kk++) kacc = mfma32_h(fa[kk], fb[kk], kacc);
    SB();
    await<1>(cnt(lds, C_G2), 1);
    gload16c<0>(uu[0], ws, (u32)(WS_UT + d * (SEQ * 2) + t * 16));
    gload16c<2048>(uu[1], ws, (u32)(WS_UT + d * (SEQ * 2) + t * 16));
    SB();
    kg_tables(in.k1, (u32*)(tab + l * 4));
#pragma unroll
    for (int kk = 0; kk < 8; kk++) kacc = mfma32_h(*(const u32x4*)(pa + kk * 32), *(const u32x4*)(pb + kk * 32), kacc);
    SB();
    wait_vm<0>();
    pin(uu[0]); pin(uu[1]);
    *(u32x4*)(U + 512 + t * 16) = uu[0];
    *(u32x4*)(U + 2560 + t * 16) = uu[1];
    float* give = (float*)tab + l;
    float keepv[6];
#pragma unroll
    for (int i = 0; i < 6; i++) { give[i * 64] = hw ? kacc[i] : kacc[6 + i]; keepv[i] = hw ? kacc[6 + i] : kacc[i]; }
    const int b1 = h ? 384 + 2 * r : 256 + 4 * r, b2 = h ? (r == 0 ? 640 : 672) : 512 + r;
    const int x0 = hw ? b1 + (h ? 64 : 2) : 4 * h + 8 * r;
    const int x1 = hw ? b2 : x0 + 2, d1 = hw ? (h ? (r == 0 ? 32 : 0) : 32) : 1;
    const int x2 = hw ? (h ? 672 : b2 + 64) : b1, d2 = hw ? (h ? 0 : 32) : 1;
    gsync(pc, l, 2);
    const float* take = (const float*)(smem + (w ^ 1) * KG_WAVE) + l;
    const int xi[6] = {x0, x0 + 1, x1, x1 + d1, x2, x2 + d2};
#pragma unroll
    for (int i = 0; i < 6; i++) {
      const float o = take[i * 64];
      cmp[xi[i]] = __builtin_bit_cast(u16, (_Float16)(hw ? o + keepv[i] : keepv[i] + o));
    }
  }
  u32 gi[2][8];
#pragma unroll
  for (int c = 0; c < 2; c++)
#pragma unroll
    for (int j = 0; j < 4; j++) {
      const int l0 = 2047 - (hw * 128 + c * 64 + l) * 8 - 2 * j, l1 = l0 - 1;
      const u32 bits = pk_bf16((float)l0, (float)l1);
      int i0 = (int)(bits & 0xffffu) - 0x4280, i1 = (int)(bits >> 16) - 0x4280;
      if (c == 1) { i0 = l0 < 128 ? l0 : i0; i1 = l1 < 128 ? l1 : i1; }
      gi[c][2 * j] = i0; gi[c][2 * j + 1] = i1;
    }
  gsync(cnt(lds, C_S0 + 1), l, 4);
  {
    u32 ra[3][4];
#pragma unroll
    for (int c = 0; c < 2; c++)
#pragma unroll
      for (int j = 0; j < 4; j++) ra[c][j] = (u32)cmp[gi[c][2 * j]] | ((u32)cmp[gi[c][2 * j + 1]] << 16);
    {
      const u32 k8 = hw ? 0u : cmp[512], k4 = hw ? 0u : cmp[511], k0 = hw ? 0u : cmp[510];
      ra[2][0] = k8 * 0x10001u; ra[2][1] = k4 * 0x10001u; ra[2][2] = k4 | (k0 << 16); ra[2][3] = k0 * 0x10001u;
    }
#pragma unroll
    for (int c = 0; c < 2; c++) {
      u32 dw[8], od[7];
#pragma unroll
      for (int j = 0; j < 4; j++) {
        dw[j] = ra[c][j];
        dw[4 + j] = dpp<0x130>(c == 0 ? dpp<0x134>(0u, ra[1][j]) : ra[2][j], ra[c][j]);
      }
#pragma unroll
      for (int i = 0; i < 7; i++) od[i] = __builtin_amdgcn_alignbit(dw[i + 1], dw[i], 16);
      char* tp = T + (hw * 128 + c * 64 + l) * 16;
#pragma unroll
      for (int k = 0; k < 4; k++) {
        *(u32x4*)(tp + (2 * k) * CV_CS) = (u32x4){dw[k], dw[k + 1], dw[k + 2], dw[k + 3]};
        *(u32x4*)(tp + (2 * k + 1) * CV_CS) = (u32x4){od[k], od[k + 1], od[k + 2], od[k + 3]};
      }
    }
    if (t < 8) *(u32x4*)(T + t * CV_CS + 4096) = (u32x4){0u, 0u, 0u, 0u};
  }
  if (w == 3) poll(ws, fb_off(cb & 7, cb >> 3, l), tag);
  signal(cnt(lds, C_GU), l);
  gsync(pc + 8, l, 2);
  const char* pa = T + (7 - (rho & 7)) * CV_CS + (255 - (rho >> 3) + q) * 16 - 32 - 256 * hw;
  const char* pb = U + 512 + rho * 32 + q * 16 - 32 - 256 * hw;
  f32x4 acc[8] = {};
  {
    u32x4 A0[8], B0[8], A1[8], B1[8];
    cv_frags(A0, B0, pa, pb);
    SB(); cv_frags(A1, B1, pa - 64, pb - 64); SB();
    cv_mma(A0, B0, acc);
    SB(); cv_frags(A0, B0, pa - 128, pb - 128); SB();
    cv_mma(A1, B1, acc);
    SB(); cv_frags(A1, B1, pa - 192, pb - 192); SB();
    cv_mma(A0, B0, acc);
    cv_mma(A1, B1, acc);
  }
  gsync(pc + 12, l, 2);
  {
    char* box = T + hw * 4096;
#pragma unroll
    for (int i = 0; i < 4; i++) {
      const f32x4 give = hw ? acc[i] : acc[4 + i];
      acc[i] = hw ? acc[4 + i] : acc[i];
      *(f32x4*)(box + i * 1024 + l * 16) = give;
    }
  }
  u32x2 ub[4];
#pragma unroll
  for (int i = 0; i < 4; i++) ub[i] = *(const u32x2*)(U + 512 + (256 * (4 * hw + i) + 16 * rho + 4 * q) * 2);
  gsync(pc + 16, l, 2);
  {
    const char* box = T + (hw ^ 1) * 4096;
    u32x2 yv[4];
#pragma unroll
    for (int i = 0; i < 4; i++) {
      f32x4 v = (acc[i] + *(const f32x4*)(box + i * 1024 + l * 16)) * ks;
      f16x2 u0 = as_h2(ub[i].x), u1 = as_h2(ub[i].y);
      yv[i] = (u32x2){pk_bf16(fmaf((float)u0.x, dk, v.x), fmaf((float)u0.y, dk, v.y)), pk_bf16(fmaf((float)u1.x, dk, v.z), fmaf((float)u1.y, dk, v.w))};
    }
#pragma unroll
    for (int i = 0; i < 4; i += 2) {
      auto sx = __builtin_amdgcn_permlane16_swap(yv[i].x, yv[i + 1].x, false, false);
      auto sy = __builtin_amdgcn_permlane16_swap(yv[i].y, yv[i + 1].y, false, false);
      const u32 off = (u32)(512 * (4 * hw + i + (q & 1)) + 32 * rho + 16 * (q >> 1));
      gstore16(ws, (u32)(WS_YT + d * (SEQ * 2)) + off, (u32x4){sx[0], sy[0], sx[1], sy[1]});
    }
  }
  publish(cnt(lds, C_FY), l, 4, ws, (u32)(WS_FY + l * 1024 + cb * 4), tag, 4);
}

DEV int k3_wloc(int w, int i) { return i < 4 ? w * 16384 + i * 4096 : (i == 4 ? (w < 2 ? 65536 + w * 4096 : L_W4 + (w - 2) * 4096) : L_X + w * 16384 + (i - 5) * 4096); }
DEV void k3_wload(const Args& a, char* smem, int dp, int w, int i, int l) {
  const char* wo = (const char*)a.wout + (size_t)(w * 224 + i * 32) * (DM * 2) + dp * 128;
  const u32 woff = (u32)((l >> 3) * (DM * 2) + (((l & 7) ^ ((l >> 4) << 1)) * 16));
#pragma unroll
  for (int j = 0; j < 4; j++) dma16(wo + j * 8 * (DM * 2), woff, lds_addr(smem) + k3_wloc(w, i) + j * 1024);
}
constexpr int k3_cloc(int n) {
  int w = n / 7, i = n % 7;
  return i < 4 ? w * 16384 + i * 4096 : (i == 4 ? (w < 2 ? 65536 + w * 4096 : L_W4 + (w - 2) * 4096) : L_X + w * 16384 + (i - 5) * 4096);
}
template <int N> DEV void k3_h(const char* hb, u32 ho, u32 yo, u32x4 (&hf)[30]) {
  if constexpr (N < 28) gload16c<(N % 8 - 4) * 1024>(hf[N], hb + (N / 8) * 8192, ho);
  else if constexpr (N < 30) gload16c<0>(hf[N], hb, yo + (N - 28) * 32 * (SEQ * 2));
}
template <int A, int B> DEV void k3_hs(const char* hb, u32 ho, u32 yo, u32x4 (&hf)[30]) { if constexpr (A < B) { k3_h<A>(hb, ho, yo, hf); k3_hs<A + 1, B>(hb, ho, yo, hf); } }
template <int N> DEV u32x4 k3_wf(const lptr (&wp)[3][4], int p) {
  constexpr int o = k3_cloc(N), bk = o / 61440, r = o - bk * 61440;
  return ltr_frag(wp[bk][p] + r, wp[bk][p] + r + 2048);
}
template <int N> DEV void k3_step(const char* hb, u32 ho, u32 yo, u32x4 (&hf)[30], u32x4 (&wf)[2][4], const lptr (&wp)[3][4], f32x4 (&acc)[4]) {
  wait_vm<(N + K3_LEAD < 30 ? N + K3_LEAD : 30) - N - 1>();
  pin(hf[N]);
  k3_h<N + K3_LEAD>(hb, ho, yo, hf);
#pragma unroll
  for (int p = 0; p < 4; p++) {
    if (N == 0) mma0(acc[p], wf[N & 1][p], hf[N]); else mma(acc[p], wf[N & 1][p], hf[N]);
    SB(); if constexpr (N < 26) wf[N & 1][p] = k3_wf<N + 2>(wp, p); SB();
  }
  if constexpr (N < 27) k3_step<N + 1>(hb, ho, yo, hf, wf, wp, acc);
}
DEV void k3_gemm(const Args& a, char* smem, int xcd, int idx, int w, u32 tag) {
  const int l = lane_id(), rho = l & 15, q = l >> 4, qq = (l >> 2) & 3, pp = l & 3;
  const int i7 = (idx * 37) >> 8, tp = xcd * 4 + i7, dp = idx - 7 * i7;
  char* const ws = a.ws;
  const lptr lds = (lptr)smem;
  await(cnt(lds, C_FH), 4);
  k3_wload(a, smem, dp, w, 5, l);
  k3_wload(a, smem, dp, w, 6, l);
  const u32 xo = (u32)((tp * 64 + w * 16 + (l >> 3)) * (DM * 2) + dp * 128 + (l & 7) * 16);
  u32x4 xv[2] = {*(const u32x4*)((const char*)a.x + xo), *(const u32x4*)((const char*)a.x + xo + 8 * (DM * 2))};
  if (w == 0) { __builtin_amdgcn_s_sleep(G3_DELAY); poll(ws, (u32)(WS_FH + tp * 128 + (l & 7) * 4), tag); signal(cnt(lds, C_G3), l); }
  else await<1>(cnt(lds, C_G3), 1);
  wait_vm<0>();
  u32x4 hf[30], wf[2][4];
  const char* hb = ws + WS_HF + ((size_t)(tp * 4 + w) * 28 + 4) * 1024;
  const u32 ho = l * 16;
  const u32 yo = (u32)(WS_YT - WS_HF + (dp * 64 + (l >> 1)) * (SEQ * 2) + tp * 128 + w * 32 + (l & 1) * 16 - ((tp * 4 + w) * 28 + 4) * 1024);
  k3_hs<0, K3_LEAD>(hb, ho, yo, hf);
  lptr wp[3][4];
  const int sg = ((q & 1) << 1) | (qq >> 1);
#pragma unroll
  for (int k = 0; k < 3; k++)
#pragma unroll
    for (int p = 0; p < 4; p++) wp[k][p] = pinned(lds + k * 61440 + (4 * q + qq) * 128 + ((p ^ sg) * 32) + 8 * pp);
  sbar();
#pragma unroll
  for (int p = 0; p < 4; p++) { wf[0][p] = k3_wf<0>(wp, p); wf[1][p] = k3_wf<1>(wp, p); }
  f32x4 acc[4];
  SB();
  k3_step<0>(hb, ho, yo, hf, wf, wp, acc);
  mma_drain();
  char* const Y = smem + L_X + w * 16384 + 12800;
  char* const fin = smem + L_X + w * 16384 + 8192;
  wait_vm<0>();
  pin(hf[28]); pin(hf[29]);
  *(u32x4*)(Y + l * 16) = hf[28];
  *(u32x4*)(Y + 1024 + l * 16) = hf[29];
#pragma unroll
  for (int p = 0; p < 4; p++) {
    const u32x2 y2 = tr_read(Y + (16 * p + 4 * q + qq) * 32 + 8 * pp);
    *(f32x4*)(fin + rho * 272 + p * 64 + q * 16) = acc[p] + (f32x4){bflo(y2.x), bfhi(y2.x), bflo(y2.y), bfhi(y2.y)};
  }
#pragma unroll
  for (int i = 0; i < 2; i++) {
    const char* rp = fin + (8 * i + (l >> 3)) * 272 + (l & 7) * 32;
    const f32x4 s0 = *(const f32x4*)(rp) + (f32x4){bflo(xv[i].x), bfhi(xv[i].x), bflo(xv[i].y), bfhi(xv[i].y)};
    const f32x4 s1 = *(const f32x4*)(rp + 16) + (f32x4){bflo(xv[i].z), bfhi(xv[i].z), bflo(xv[i].w), bfhi(xv[i].w)};
    const u32x4 o = {pk_bf16(s0.x, s0.y), pk_bf16(s0.z, s0.w), pk_bf16(s1.x, s1.y), pk_bf16(s1.z, s1.w)};
    gstore16(a.out, xo + i * 8 * (DM * 2), o);
  }
}

DEV void body(const Args& a, char* smem) {
  const int b = blockIdx.x, xcd = b & 7, idx = b >> 3;
  const int w8 = __builtin_amdgcn_readfirstlane(threadIdx.x >> 6), l = threadIdx.x & 63, w = w8 & 3;
  char* const ws = a.ws;
  const lptr lds = (lptr)smem;
  const int i7 = (idx * 37) >> 8;
  const int panel = (xcd >> 1) * 4 + i7, sib = (xcd & 1) * 7 + idx - 7 * i7;
  const bool full = b < 224;
  const u32 t_entry = full ? 0u : (u32)__builtin_amdgcn_s_memrealtime();
  K1L g;
  if (w8 >= 4 && full) {
    g.win = (const char*)a.win + (size_t)(16 * w) * (FF * 2) + sib * 128;
    g.xb = (const char*)a.x + (size_t)(panel * 128 + w * 32) * (DM * 2);
    g.xoff[0] = (u32)((l >> 3) * (DM * 2) + (((l & 7) ^ (l >> 4)) * 16));
    g.xoff[1] = (u32)((8 + (l >> 3)) * (DM * 2) + (((l & 7) ^ ((l >> 4) | 4)) * 16));
    g.woff[0] = (u32)((l >> 3) * (FF * 2) + (((l & 7) ^ (((l >> 4) & 1) << 1)) * 16));
    g.woff[1] = (u32)((8 + (l >> 3)) * (FF * 2) + (((l & 7) ^ (((l >> 4) & 1) << 1) ^ 4) * 16));
    g.xdst = lds_addr(smem) + L_X + w * 4096;
    g.wdst = lds_addr(smem) + w * 2048;
    g.m0 = sib >> 1;
    k1_unit<0>(g); k1_unit<1>(g);
  }
  u32 tag;
  asm volatile("s_load_dword %0, %1, 0x0\n\ts_waitcnt lgkmcnt(0)" : "=s"(tag) : "s"(ws + WS_EP) : "memory");
  tag += 1u;
  if (threadIdx.x < 32) *(u32*)(smem + L_CNT + threadIdx.x * 4) = 0u;

  if (w8 >= 4) {
    if (full) {
      const K1C c = k1_addr(lds, w, l);
      u32x4 ones = {0x3f803f80u, 0x3f803f80u, 0x3f803f80u, 0x3f803f80u};
      pin(ones);
      u32x4 xf[7][4], wf[8];
      f32x4 acc[2][4], cs;
      f32x4 rsum[2], gram[2];
      lbar0();
      k1_unit<2>(g); k1_xr(c, xf[0], 0); lbar0();
      k1_unit<3>(g); k1_xr(c, xf[1], 1); k1_stats<true>(xf[0], rsum, gram, ones); lbar0();
      k1_unit<4>(g); k1_xr(c, xf[2], 2); k1_stats<false>(xf[1], rsum, gram, ones); lbar0();
      k1_unit<5>(g); k1_xr(c, xf[3], 3); k1_stats<false>(xf[2], rsum, gram, ones); lbar0();
      k1_unit<6>(g); k1_xr(c, xf[4], 4); k1_stats<false>(xf[3], rsum, gram, ones); lbar0();
      k1_unit<7>(g); k1_unit<8>(g); k1_unit<9>(g); k1_xr(c, xf[5], 5); k1_stats<false>(xf[4], rsum, gram, ones); lbar0();
      k1_unit<10>(g); k1_unit<11>(g); k1_unit<12>(g); k1_xr(c, xf[6], 6); k1_stats<false>(xf[5], rsum, gram, ones);
      k1_stats<false>(xf[6], rsum, gram, ones);
      bar();
      k1_unit<13>(g);
      mma_drain();
      f32x2 ln1[2];
      {
        const int rho = l & 15, q = l >> 4;
#pragma unroll
        for (int t = 0; t < 2; t++) {
          float d = (rho & 2) ? ((rho & 1) ? gram[t].w : gram[t].z) : ((rho & 1) ? gram[t].y : gram[t].x);
          d = ((rho >> 2) == q) ? d : 0.f;
          d = xsum32(xsum16(d));
          ln1[t] = ln_scalars(rsum[t].x, d, 1.f / DM);
          if (q == 0) *(f32x2*)(smem + L_LN1 + w * 256 + (t * 16 + rho) * 8) = ln1[t];
        }
      }
      wait_vm<0>(); bar();
      k2_as<0, 16>(k2_setup(a, smem, xcd, idx, w, l));
#pragma unroll
      for (int f = 0; f < 8; f++) wf[f] = k1_wf<7>(c, f);
      __builtin_amdgcn_s_sleep(K1_BDELAY);
      SB();
      k1_z1loop<0>(c, xf, wf, acc, cs, ones);
      mma_drain();
      const int l2 = lane_id(), rho = l2 & 15, q = l2 >> 4;
      if (rho == 0) *(f32x4*)(smem + L_CS + 256 + w * 64 + q * 16) = cs;
      gsync(cnt(lds, C_CS1), l2, 4);
      k1_act(acc, smem + L_CS + 256 + q * 16, w, ln1);
#pragma unroll
      for (int t = 0; t < 2; t++)
#pragma unroll
        for (int cc = 0; cc < 2; cc++) {
          u32x2 x0 = {pk_bf16(acc[t][2 * cc].x, acc[t][2 * cc].y), pk_bf16(acc[t][2 * cc].z, acc[t][2 * cc].w)};
          u32x2 x1 = {pk_bf16(acc[t][2 * cc + 1].x, acc[t][2 * cc + 1].y), pk_bf16(acc[t][2 * cc + 1].z, acc[t][2 * cc + 1].w)};
          const u32x4 o = (w & 1) ? (u32x4){x1.x, x1.y, x0.x, x0.y} : (u32x4){x0.x, x0.y, x1.x, x1.y};
          gstore16(ws, (u32)(WS_Z1F + ((panel * 8 + w * 2 + t) * 28 + sib * 2 + (cc ^ (w >> 1))) * 1024 + l2 * 16), o);
        }
      publish(cnt(lds, C_FB), l2, 4, ws, (u32)(WS_FB + panel * 128 + sib * 4), tag);
    } else {
      k2_as<0, 16>(k2_setup(a, smem, xcd, idx, w, l));
      wait_vm<0>();
      bar();
      while ((u32)__builtin_amdgcn_s_memrealtime() - t_entry < SPARE_WAIT) __builtin_amdgcn_s_sleep(8);
    }
    k2_gemm(a, smem, xcd, idx, w, tag);
    if (full) k3_gemm(a, smem, xcd, idx, w, tag);
    return;
  }
  if (!full) {
    bar();
    const int j = (idx - 28) * 4 + w;
    const u32 t_start = (u32)__builtin_amdgcn_s_memtime();
    const u32 dump = lds_addr(smem) + w * 1024;
    {
      u64 c0, c1;
      asm volatile("s_getpc_b64 vcc\n\ts_add_u32 vcc_lo, vcc_lo, km@rel32@lo+4\n\ts_addc_u32 vcc_hi, vcc_hi, km@rel32@hi+12\n\ts_mov_b64 %0, vcc" : "=s"(c0) :: "scc", "vcc");
      asm volatile("s_getpc_b64 vcc\n\ts_add_u32 vcc_lo, vcc_lo, km_code_end@rel32@lo+4\n\ts_addc_u32 vcc_hi, vcc_hi, km_code_end@rel32@hi+12\n\ts_mov_b64 %0, vcc" : "=s"(c1) :: "scc", "vcc");
      const u32 len = (u32)(c1 - c0) & ~3u, o = (u32)(j * 64 + l) * 128u;
      if (j < 8) touch((const void*)c0, o < len ? o : len - 4u, dump);
      const int n = j * 64 + l;
      if (j < 5) touch(ws, (u32)(n < 288 ? n : 287) * 65536u, dump);
      if (j < 7) touch(a.out, (u32)n * 4096u, dump);
    }
    const u16* const par[4] = {a.lar, a.aim, a.cre, a.cim};
#pragma unroll
    for (int k = 0; k < 4; k++)
#pragma unroll
      for (int i = 0; i < 2; i++) { const int c = j + 16 * i; dma16((const char*)par[k] + (xcd * 28 + (c < 28 ? c : 27)) * 1024, l * 16, dump); }
    const u32 poff = (u32)((l >> 3) * 4096 + (l & 7) * 16);
#pragma unroll 1
    for (int i = 256 + j; i < 1024; i += 64) {
#pragma unroll
      for (int k = 0; k < 4; k++) dma16(wsp_piece(a.wsp, xcd, i + 16 * k < 1024 ? i + 16 * k : 1023), poff, dump);
      if ((u32)__builtin_amdgcn_s_memtime() - t_start > PF_STOP) break;
    }
#pragma unroll
    for (int k = 0; k < 7; k++) { const int c = j + 16 * k; dma16((const char*)a.wout + (size_t)(xcd * 98 + (c < 98 ? c : 97)) * 1024, l * 16, dump); }
    if (w == 0) {
      await<4>(cnt(lds, C_G2), 1);
      poll(ws, fb_off(xcd, idx, l), tag);
      signal(cnt(lds, C_GU), l);
    }
    if (b == 255 && w == 0) {
      await<4>(cnt(lds, C_FH), 4);
#pragma unroll 1
      for (int i = 0; i < 8; i++) {
        const int n = (i & 3) * 64 + l;
        poll(ws, (u32)(i < 4 ? WS_FH + (n >> 3) * 128 + (n & 7) * 4 : WS_FY + (n < 224 ? n : 223) * 4), tag);
      }
      if (l == 0) __hip_atomic_store((GAS u32*)(ws + WS_EP), tag, __ATOMIC_RELAXED, __HIP_MEMORY_SCOPE_AGENT);
    }
    return;
  }
  SsmIn sin;
  {
    const int grp = panel * 4 + w;
    {
      const int n = (w & 1) * 32 + (l & 31);
      const int wl = panel * 56 + (n < 56 ? n : 55);
      const u32 xo = (u32)(panel * 128 * (DM * 2) + (sib * 64 + n) * 128);
      const u32 wo = (u32)((wl >> 1) * (FF * 2) + ((wl & 1) ? 0 : FH * 2) + sib * 128);
      touch(w < 2 ? (const void*)a.x : (const void*)a.win, w < 2 ? xo : wo, lds_addr(smem) + L_SCRAP);
    }
    u32x4 xs[2];
#pragma unroll
    for (int i = 0; i < 2; i++) gload16<0>(xs[i], a.x, (u32)((panel * 128 + w * 32 + i * 16 + (l >> 2)) * (DM * 2) + sib * 64 + (l & 3) * 16));
    const K1C c = k1_addr(lds, w, l);
    u32x4 ones = {0x3f803f80u, 0x3f803f80u, 0x3f803f80u, 0x3f803f80u};
    pin(ones);
    u32x4 xf[7][4], wf[8];
    f32x4 acc[2][4], cs;
    bar();
    k1_xr(c, xf[0], 0);
#pragma unroll
    for (int f = 0; f < 8; f++) wf[f] = k1_wf<0>(c, f);
    k1_stage<0>(c, xf, wf, acc, cs, ones);
    mma_drain();
    wait_vm<0>(); pin(xs[0]); pin(xs[1]);
    const int rho = l & 15, q = l >> 4, qq = (l >> 2) & 3, pp = l & 3;
    if (rho == 0) *(f32x4*)(smem + L_CS + w * 64 + q * 16) = cs;
    sin = ssm_load(a, b, w, l);
    bar();
    f32x2 ln1[2];
#pragma unroll
    for (int t = 0; t < 2; t++) ln1[t] = *(const f32x2*)(smem + L_LN1 + w * 256 + (t * 16 + rho) * 8);
    GAS u64* gr = (GAS u64*)(ws + WS_GR) + (size_t)grp * 14 * 64;
    {
      k1_act(acc, smem + L_CS + q * 16, w, ln1);
      float st[2][2];
#pragma unroll
      for (int t = 0; t < 2; t++) {
        f32x2 s = {0.f, 0.f}, s2 = {0.f, 0.f};
#pragma unroll
        for (int p = 0; p < 4; p++) {
          s += acc[t][p].xy; s += acc[t][p].zw;
          s2 += acc[t][p].xy * acc[t][p].xy; s2 += acc[t][p].zw * acc[t][p].zw;
        }
        st[t][0] = xsum32(xsum16(s.x + s.y)); st[t][1] = xsum32(xsum16(s2.x + s2.y));
      }
      const float v = (q & 2) ? ((q & 1) ? st[1][1] : st[0][1]) : ((q & 1) ? st[1][0] : st[0][0]);
      __hip_atomic_store(gr + sib * 64 + l, ((u64)tag << 32) | __float_as_uint(v), __ATOMIC_RELAXED, __HIP_MEMORY_SCOPE_AGENT);
    }
#pragma unroll
    for (int i = 0; i < 2; i++) {
      const f32x2 lx = *(const f32x2*)(smem + L_LN1 + w * 256 + (i * 16 + (l >> 2)) * 8);
      const float mr = -lx.x * lx.y;
      u16* ub = (u16*)(smem + L_UT + (8 * (l & 3)) * 272 + (w * 32 + i * 16 + (l >> 2)) * 2);
#pragma unroll
      for (int j = 0; j < 4; j++) {
        const u32 o = pk_f16(fmaf(bflo(xs[i][j]), lx.y, mr), fmaf(bfhi(xs[i][j]), lx.y, mr));
        ub[(2 * j) * 136] = (u16)o;
        ub[(2 * j + 1) * 136] = (u16)(o >> 16);
      }
    }
    gsync(cnt(lds, C_UT), l, 4);
#pragma unroll
    for (int i = 0; i < 2; i++) {
      const u32x4 uo = *(const u32x4*)(smem + L_UT + (w * 8 + i * 4 + q) * 272 + rho * 16);
      gstore16(ws, (u32)(WS_UT + (sib * 32 + w * 8 + i * 4 + q) * (SEQ * 2) + panel * 256 + rho * 16), uo);
    }
    u64 rv[14];
#pragma unroll
    for (int i = 0; i < 14; i++) rv[i] = __hip_atomic_load(gr + i * 64 + l, __ATOMIC_RELAXED, __HIP_MEMORY_SCOPE_AGENT);
    float tot;
    for (u32 spins = 0;; spins++) {
      bool ok = true; tot = 0.f;
#pragma unroll
      for (int i = 0; i < 14; i++) { ok &= (u32)(rv[i] >> 32) == tag; tot += __uint_as_float((u32)rv[i]); }
      if (__all(ok) || spins > (1u << 20)) break;
#pragma unroll
      for (int i = 0; i < 14; i++) rv[i] = __hip_atomic_load(gr + i * 64 + l, __ATOMIC_RELAXED, __HIP_MEMORY_SCOPE_AGENT);
    }
    {
      auto bb = __builtin_amdgcn_permlane32_swap(__float_as_uint(tot), __float_as_uint(tot), false, false);
      const f32x2 lr = ln_scalars(__uint_as_float(bb[0]), __uint_as_float(bb[1]), 1.f / FH);
      const u32 nm = __float_as_uint(-lr.x * lr.y);
      auto lm = __builtin_amdgcn_permlane16_swap(nm, nm, false, false);
      auto ls = __builtin_amdgcn_permlane16_swap(__float_as_uint(lr.y), __float_as_uint(lr.y), false, false);
      char* const stg = smem + L_STG + w * 2304;
#pragma unroll
      for (int h = 0; h < 2; h++) {
#pragma unroll
        for (int t = 0; t < 2; t++)
#pragma unroll
          for (int p = 0; p < 2; p++) {
            const f32x4 v = acc[t][2 * h + p] * __uint_as_float(ls[t]) + __uint_as_float(lm[t]);
            *(volatile LDSAS u32x2*)((lptr)stg + (16 * t + rho) * 72 + p * 32 + q * 8) = (u32x2){pk_bf16(v.x, v.y), pk_bf16(v.z, v.w)};
          }
        asm volatile("" ::: "memory");
#pragma unroll
        for (int p = 0; p < 2; p++) {
          const char* rp = stg + (8 * q + qq) * 72 + p * 32 + pp * 8;
          const u32x4 o = tr_frag(rp, rp + 4 * 72);
          const int ft = sib * 4 + ((2 * h + p) ^ w), f7 = (ft * 37) >> 8;
          gstore16(ws, (u32)(WS_Z2F + ((f7 * 64 + grp) * 7 + ft - 7 * f7) * 1024 + l * 16), o);
        }
      }
    }
    publish(cnt(lds, C_FA), l, 4, ws, f1_off(sib, panel, l), tag, 9);
    await(cnt(lds, C_FA), 4);
    await<1>(cnt(lds, C_FB), 4);
  }
  SB();
  const SsmPre spre = ssm_pre(sin, w);
  k2_ssm(a, spre, smem, b, w, tag);
  await(cnt(lds, C_FY), 4);
  {
    const int l2 = lane_id(), dp = idx - 7 * i7;
    await<2>(cnt(lds, C_FH), 4);
#pragma unroll
    for (int i = 0; i < 5; i++) k3_wload(a, smem, dp, w, i, l2);
  }
  if (w == 0) {
    const int l2 = lane_id();
    poll(ws, (u32)(WS_FY + (xcd >> 1) * 1024 + ((idx - 7 * i7) * 32 + (l2 & 31)) * 4), tag);
  }
  wait_vm<0>();
  sbar();
}

extern "C" __global__ __launch_bounds__(512, 2) void km(Args a) {
  __shared__ __attribute__((aligned(16))) char smem[L_END];
  {
    u64 c0;
    asm volatile("s_getpc_b64 %0" : "=s"(c0));
    const u32 t = threadIdx.x, o = ((t >> 6) * 16 + ((t & 63) < 16 ? (t & 63) : 15)) * 128u;
    touch((const void*)c0, o, lds_addr(smem) + L_SCRAP);
  }
  body(a, smem);
  asm volatile("km_code_end:");
}

extern "C" void kernel_launch(void* const* d_in, const int* in_sizes, int n_in, void* d_out, int out_size, void* d_ws, size_t ws_size,
                              hipStream_t stream) {
  Args a;
  a.x = (const u16*)d_in[0]; a.lar = (const u16*)d_in[1]; a.aim = (const u16*)d_in[2]; a.cre = (const u16*)d_in[3]; a.cim = (const u16*)d_in[4];
  a.ldt = (const u16*)d_in[5]; a.dsk = (const u16*)d_in[6]; a.win = (const u16*)d_in[8]; a.wsp = (const u16*)d_in[9]; a.bsp = (const u16*)d_in[10];
  a.wout = (const u16*)d_in[11]; a.out = (u16*)d_out; a.ws = (char*)d_ws;
  km<<<256, 512, 0, stream>>>(a);
}
```
